```python
import torch

def _softmax(x, axis=-1):
    return torch.softmax(x, dim=axis)
def _gelu(x):
    return torch.nn.functional.gelu(x, approximate="tanh")
def _silu(x):
    return torch.nn.functional.silu(x)
def _sigmoid(x):
    return torch.sigmoid(x)
def _layernorm(x, axis=-1, eps=1e-5):
    m = x.mean(dim=axis, keepdim=True)
    v = x.var(dim=axis, keepdim=True, unbiased=False)
    return (x - m) / torch.sqrt(v + eps)
def _scatter_add(tgt, idx, src, axis=0):
    return tgt.clone().index_add_(axis, idx, src)
def _segment_sum(src, seg, n):
    out = torch.zeros((n,) + src.shape[1:], dtype=src.dtype)
    return out.index_add_(0, seg, src)
def _pad(x, pw):
    flat = tuple(v for pair in reversed(pw) for v in pair)
    return torch.nn.functional.pad(x, flat)

kernel_name = 'gated_mix__sliding_block_cached__dense_activation_relu__decode'

def setup_inputs():

    _w = (lambda a: (lambda t: t.to(torch.bfloat16) if t.is_floating_point() else t)(torch.as_tensor(a)))
    def _A_setup():
        B, Sq, D = 4, 1, 448
        Hq, Hkv, Sk, Dh = 16, 8, 8192, 256
        assert Hq % Hkv == 0, (Hq, Hkv)
        return {
            "x": _w((lambda *s: torch.randn(*s, dtype=torch.bfloat16) * 0.05)(B, Sq, D)),
            "k_cache": _w((lambda *s: torch.randn(*s, dtype=torch.bfloat16) * 6.4)(B, Hkv, Sk, Dh)),
            "v_cache": _w((lambda *s: torch.randn(*s, dtype=torch.bfloat16) * 0.4)(B, Hkv, Sk, Dh)),
            "wq": _w((lambda *s: torch.randn(*s, dtype=torch.bfloat16) * 6.4)(D, Hq * Dh) * (D ** -0.5)),
            "wo": _w((lambda *s: torch.randn(*s, dtype=torch.bfloat16) * 0.4)(Hq * Dh, D) * ((Hq * Dh) ** -0.5)),
        }
    _A_in = _A_setup()

    def _B_setup():
        B, S, D, Dff = 4, 1, 448, 1792
        return {
            "x": _w((lambda *s: torch.randn(*s, dtype=torch.bfloat16) * 0.05)(B, S, D)),
            "w1": _w((lambda *s: torch.randn(*s, dtype=torch.bfloat16) * 0.05)(D, Dff)),
            "w2": _w((lambda *s: torch.randn(*s, dtype=torch.bfloat16) * 0.05)(Dff, D)),
        }
    _B_in = _B_setup()

    return {
        "A_x": _A_in["x"],
        "A_k_cache": _A_in["k_cache"],
        "A_v_cache": _A_in["v_cache"],
        "A_wq": _A_in["wq"],
        "A_wo": _A_in["wo"],
        "B_x": _B_in["x"],
        "B_w1": _B_in["w1"],
        "B_w2": _B_in["w2"],
        "gate_w": _w((lambda *s: torch.randn(*s, dtype=torch.bfloat16) * 0.05)(448) * 16.0),
    }

def reference(A_x, A_k_cache, A_v_cache, A_wq, A_wo, B_x, B_w1, B_w2, gate_w):
    def _A_ref(x, k_cache, v_cache, wq, wo):
        B, Sq, D = x.shape
        Hq, Dh = 16, 256
        Hkv, Sk = k_cache.shape[1], k_cache.shape[2]
        W = min(4096, max(256, Sk // 4))
        rep = Hq // Hkv
        q = (lambda x, *d: x.permute(*d))((x @ wq).reshape(B, Sq, Hq, Dh), 0, 2, 1, 3)
        k = (lambda x, n, axis: torch.repeat_interleave(x, n, dim=axis))(k_cache[:, :, Sk - W:, :], rep, axis=1)
        v = (lambda x, n, axis: torch.repeat_interleave(x, n, dim=axis))(v_cache[:, :, Sk - W:, :], rep, axis=1)
        scores = torch.einsum("bhqd,bhkd->bhqk", q, k) * (Dh ** -0.5)
        p = _softmax(scores, axis=-1)
        o = torch.einsum("bhqk,bhkd->bhqd", p, v)
        return (lambda x, *d: x.permute(*d))(o, 0, 2, 1, 3).reshape(B, Sq, Hq * Dh) @ wo

    def _B_ref(x, w1, w2):
        h = torch.einsum("bsd,df->bsf", x, w1)
        zero = torch.zeros_like(h)
        h = torch.maximum(h, zero)
        return torch.einsum("bsf,fd->bsd", h, w2)

    x = A_x
    g = _sigmoid((x * gate_w).sum(-1, keepdims=True))
    a = _A_ref(x, A_k_cache, A_v_cache, A_wq, A_wo)
    b = _B_ref(x, B_w1, B_w2)
    return g * a + (1 - g) * b
```

```cpp
#include <hip/hip_runtime.h>
#include <hip/hip_bf16.h>
#include <cstdint>

typedef unsigned v4u __attribute__((ext_vector_type(4)));
using bf16x2 = __attribute__((ext_vector_type(2))) __bf16;

constexpr int B = 4, D = 448, HQ = 16, HKV = 8, SK = 8192, DH = 256, W = 2048, DFF = 1792;
constexpr int NS = 8;

__device__ __forceinline__ float bf2f(unsigned short u) { return __builtin_bit_cast(float, (unsigned)u << 16); }
__device__ __forceinline__ float lo16(unsigned u) { return __builtin_bit_cast(float, u << 16); }
__device__ __forceinline__ float hi16(unsigned u) { return __builtin_bit_cast(float, u & 0xffff0000u); }

__global__ void __launch_bounds__(256) k1(const unsigned short* x, const unsigned short* wq, const unsigned short* w1, float* q, float* h) {
  __shared__ float xs[B * D];
  __shared__ float red[4][B][64];
  for (int i = threadIdx.x; i < B * D; i += 256) xs[i] = bf2f(x[i]);
  __syncthreads();
  const int blk = blockIdx.x, lane = threadIdx.x & 63, wv = threadIdx.x >> 6;
  const bool isq = blk < 64;
  const unsigned short* w = isq ? wq : w1;
  const int ld = isq ? HQ * DH : DFF;
  const int col = (isq ? blk : blk - 64) * 64 + lane;
  float acc[B] = {0, 0, 0, 0};
  for (int k = wv * 112; k < wv * 112 + 112; ++k) {
    const float wv_ = bf2f(w[(size_t)k * ld + col]);
#pragma unroll
    for (int b = 0; b < B; ++b) acc[b] += xs[b * D + k] * wv_;
  }
#pragma unroll
  for (int b = 0; b < B; ++b) red[wv][b][lane] = acc[b];
  __syncthreads();
  const int b = wv;
  float s = red[0][b][lane] + red[1][b][lane] + red[2][b][lane] + red[3][b][lane];
  if (isq) q[b * HQ * DH + col] = s; else h[b * DFF + col] = fmaxf(s, 0.f);
}

__global__ void __launch_bounds__(256) k2(const unsigned short* kc, const unsigned short* vc, const float* q, float* po, float* pml) {
  __shared__ float sc[2][256];
  __shared__ float ored[8][2][256];
  __shared__ float ml[2][2];
  const int s = blockIdx.x, kvh = blockIdx.y, b = blockIdx.z;
  const int lane = threadIdx.x & 63, wv = threadIdx.x >> 6;
  const int c = lane & 31, r = lane >> 5;
  const size_t base = ((size_t)(b * HKV + kvh) * SK + (SK - W) + s * 256) * DH;
  float qv[2][8];
#pragma unroll
  for (int g = 0; g < 2; ++g)
#pragma unroll
    for (int j = 0; j < 8; ++j) qv[g][j] = q[b * HQ * DH + (kvh * 2 + g) * DH + c * 8 + j] * 0.0625f;
  for (int i = 0; i < 32; ++i) {
    const int key = wv * 64 + i * 2 + r;
    const v4u kk = __builtin_nontemporal_load((const v4u*)(kc + base + (size_t)key * DH + c * 8));
    float d0 = 0, d1 = 0;
#pragma unroll
    for (int j = 0; j < 4; ++j) {
      d0 += lo16(kk[j]) * qv[0][2 * j] + hi16(kk[j]) * qv[0][2 * j + 1];
      d1 += lo16(kk[j]) * qv[1][2 * j] + hi16(kk[j]) * qv[1][2 * j + 1];
    }
#pragma unroll
    for (int m = 16; m >= 1; m >>= 1) { d0 += __shfl_xor(d0, m); d1 += __shfl_xor(d1, m); }
    if (c == 0) { sc[0][key] = d0; sc[1][key] = d1; }
  }
  __syncthreads();
  if (wv < 2) {
    float m = -INFINITY;
    float v[4];
#pragma unroll
    for (int i = 0; i < 4; ++i) { v[i] = sc[wv][lane + 64 * i]; m = fmaxf(m, v[i]); }
#pragma unroll
    for (int o = 32; o >= 1; o >>= 1) m = fmaxf(m, __shfl_xor(m, o));
    float l = 0;
#pragma unroll
    for (int i = 0; i < 4; ++i) { v[i] = __expf(v[i] - m); l += v[i]; sc[wv][lane + 64 * i] = v[i]; }
#pragma unroll
    for (int o = 32; o >= 1; o >>= 1) l += __shfl_xor(l, o);
    if (lane == 0) { ml[wv][0] = m; ml[wv][1] = l; }
  }
  __syncthreads();
  float a0[8] = {0, 0, 0, 0, 0, 0, 0, 0}, a1[8] = {0, 0, 0, 0, 0, 0, 0, 0};
  for (int i = 0; i < 32; ++i) {
    const int key = wv * 64 + i * 2 + r;
    const v4u vv = __builtin_nontemporal_load((const v4u*)(vc + base + (size_t)key * DH + c * 8));
    const float p0 = sc[0][key], p1 = sc[1][key];
#pragma unroll
    for (int j = 0; j < 4; ++j) {
      a0[2 * j] += p0 * lo16(vv[j]); a0[2 * j + 1] += p0 * hi16(vv[j]);
      a1[2 * j] += p1 * lo16(vv[j]); a1[2 * j + 1] += p1 * hi16(vv[j]);
    }
  }
#pragma unroll
  for (int j = 0; j < 8; ++j) { ored[wv * 2 + r][0][c * 8 + j] = a0[j]; ored[wv * 2 + r][1][c * 8 + j] = a1[j]; }
  __syncthreads();
  const int d = threadIdx.x;
#pragma unroll
  for (int g = 0; g < 2; ++g) {
    float t = 0;
#pragma unroll
    for (int i = 0; i < 8; ++i) t += ored[i][g][d];
    po[(((size_t)b * HQ + kvh * 2 + g) * NS + s) * DH + d] = t;
  }
  if (threadIdx.x < 4) pml[(((size_t)b * HQ + kvh * 2 + (threadIdx.x >> 1)) * NS + s) * 2 + (threadIdx.x & 1)] = ml[threadIdx.x >> 1][threadIdx.x & 1];
}

__global__ void __launch_bounds__(1024) k3(const unsigned short* x, const unsigned short* gw, const unsigned short* wo, const unsigned short* w2,
                                           const float* po, const float* pml, const float* h, __hip_bfloat16* out) {
  __shared__ float o[HQ * DH];
  __shared__ float hs[DFF];
  __shared__ float red[16][2][64];
  __shared__ float gs;
  const int b = blockIdx.y, cb = blockIdx.x;
  const int lane = threadIdx.x & 63, wv = threadIdx.x >> 6;
  for (int i = threadIdx.x; i < HQ * DH; i += 1024) {
    const int hd = i >> 8, d = i & 255;
    const float* mlp = pml + ((size_t)b * HQ + hd) * NS * 2;
    float M = -INFINITY;
    for (int s = 0; s < NS; ++s) M = fmaxf(M, mlp[2 * s]);
    float L = 0, t = 0;
    for (int s = 0; s < NS; ++s) { const float w = __expf(mlp[2 * s] - M); L += w * mlp[2 * s + 1]; t += w * po[(((size_t)b * HQ + hd) * NS + s) * DH + d]; }
    o[i] = t / L;
  }
  for (int i = threadIdx.x; i < DFF; i += 1024) hs[i] = h[b * DFF + i];
  if (wv == 0) {
    float g = 0;
    for (int i = lane; i < D; i += 64) g += bf2f(x[b * D + i]) * bf2f(gw[i]);
#pragma unroll
    for (int m = 32; m >= 1; m >>= 1) g += __shfl_xor(g, m);
    if (lane == 0) gs = 1.f / (1.f + __expf(-g));
  }
  __syncthreads();
  const int col = cb * 64 + lane;
  float a = 0, f = 0;
  for (int k = wv * 256; k < wv * 256 + 256; ++k) a += o[k] * bf2f(wo[(size_t)k * D + col]);
  for (int k = wv * 112; k < wv * 112 + 112; ++k) f += hs[k] * bf2f(w2[(size_t)k * D + col]);
  red[wv][0][lane] = a; red[wv][1][lane] = f;
  __syncthreads();
  if (wv == 0) {
    float A = 0, F = 0;
    for (int i = 0; i < 16; ++i) { A += red[i][0][lane]; F += red[i][1][lane]; }
    out[b * D + col] = __float2bfloat16(gs * A + (1.f - gs) * F);
  }
}

extern "C" void kernel_launch(void* const* d_in, const int* in_sizes, int n_in, void* d_out, int out_size, void* d_ws, size_t ws_size, hipStream_t stream) {
  const unsigned short* x = (const unsigned short*)d_in[0];
  const unsigned short* kc = (const unsigned short*)d_in[1];
  const unsigned short* vc = (const unsigned short*)d_in[2];
  const unsigned short* wq = (const unsigned short*)d_in[3];
  const unsigned short* wo = (const unsigned short*)d_in[4];
  const unsigned short* w1 = (const unsigned short*)d_in[6];
  const unsigned short* w2 = (const unsigned short*)d_in[7];
  const unsigned short* gw = (const unsigned short*)d_in[8];
  float* q = (float*)d_ws;
  float* h = q + B * HQ * DH;
  float* po = h + B * DFF;
  float* pml = po + B * HQ * NS * DH;
  k1<<<64 + 28, 256, 0, stream>>>(x, wq, w1, q, h);
  k2<<<dim3(NS, HKV, B), 256, 0, stream>>>(kc, vc, q, po, pml);
  k3<<<dim3(7, B), 1024, 0, stream>>>(x, gw, wo, w2, po, pml, h, (__hip_bfloat16*)d_out);
}
```

```cpp
#include <hip/hip_runtime.h>
#include <hip/hip_bf16.h>
#include <cstdint>

namespace {

typedef unsigned v4u __attribute__((ext_vector_type(4)));
typedef unsigned v2u __attribute__((ext_vector_type(2)));
typedef float f32x4 __attribute__((ext_vector_type(4)));
typedef float f32x2 __attribute__((ext_vector_type(2)));
typedef short s16x4 __attribute__((ext_vector_type(4)));
typedef __bf16 bf16x8 __attribute__((ext_vector_type(8)));
typedef __bf16 bf16x2 __attribute__((ext_vector_type(2)));
typedef __amdgpu_buffer_rsrc_t rsrc_t;

constexpr int NB = 4, DM = 448, HKV = 8, SK = 8192, DH = 256, WIN = 2048, DFF = 1792, NQ = 4096;
constexpr int AUX_NT = 2, AUX_SC1 = 16;
#define WAUX 2
#define KVAUX 2
constexpr int OOB = 0x40000000;
constexpr unsigned SPIN_CAP = 200000;

constexpr int WS_E = 0;
constexpr int WS_Q = 4096;
constexpr int WS_S = WS_Q + 8 * 4 * 16384;
constexpr int WS_O = WS_S + 32 * 1024;
constexpr int WS_F = WS_O + 32 * 8 * 4096;
constexpr int WS_END = WS_F + 4 * 56 * 4096;
constexpr int WS_DIAG = 4 << 20;

constexpr int L_QP = 0;
constexpr int L_Q = 4096;
constexpr int L_P = 5120;
constexpr int L_Z = 6144;
constexpr int L_H = 6400;
constexpr int L_OM = 8448;
constexpr int L_FM = 16640;
constexpr int L_END = 23808;
constexpr int LDS_BYTES = 96 * 1024;
static_assert(L_END <= LDS_BYTES, "lds");

__device__ __forceinline__ rsrc_t mk(const void* p, int bytes) { return __builtin_amdgcn_make_buffer_rsrc((void*)p, 0, bytes, 0x00020000); }
template <int AUX> __device__ __forceinline__ v4u ld16(rsrc_t r, int vo, int so) { return __builtin_amdgcn_raw_buffer_load_b128(r, vo, so, AUX); }
__device__ __forceinline__ void st16_dev(rsrc_t r, v4u v, int vo) { __builtin_amdgcn_raw_buffer_store_b128(v, r, vo, 0, AUX_SC1); }
__device__ __forceinline__ float u2f(unsigned u) { return __builtin_bit_cast(float, u); }
__device__ __forceinline__ unsigned f2u(float f) { return __builtin_bit_cast(unsigned, f); }
__device__ __forceinline__ float dot2(unsigned a, unsigned b, float c) {
  return __builtin_amdgcn_fdot2_f32_bf16(__builtin_bit_cast(bf16x2, a), __builtin_bit_cast(bf16x2, b), c, false);
}
__device__ __forceinline__ unsigned pkbf(float lo, float hi) {
  const bf16x2 v = {(__bf16)lo, (__bf16)hi};
  return __builtin_bit_cast(unsigned, v);
}
__device__ __forceinline__ unsigned perm_lo(unsigned a, unsigned b) { return __builtin_amdgcn_perm(b, a, 0x05040100u); }
__device__ __forceinline__ unsigned perm_hi(unsigned a, unsigned b) { return __builtin_amdgcn_perm(b, a, 0x07060302u); }
template <int CTRL> __device__ __forceinline__ float dpp(float x) {
  return __builtin_bit_cast(float, __builtin_amdgcn_mov_dpp(__builtin_bit_cast(int, x), CTRL, 0xf, 0xf, true));
}
constexpr int XOR1 = 0xB1, XOR2 = 0x4E, XOR7 = 0x141, ROR8 = 0x128, ROR4 = 0x124;
__device__ __forceinline__ float x16_sum(float x) {
  auto s = __builtin_amdgcn_permlane16_swap(f2u(x), f2u(x), false, false);
  return u2f(s[0]) + u2f(s[1]);
}
__device__ __forceinline__ float x32_sum(float x) {
  auto s = __builtin_amdgcn_permlane32_swap(f2u(x), f2u(x), false, false);
  return u2f(s[0]) + u2f(s[1]);
}
__device__ __forceinline__ float x16_max(float x) {
  auto s = __builtin_amdgcn_permlane16_swap(f2u(x), f2u(x), false, false);
  return fmaxf(u2f(s[0]), u2f(s[1]));
}
__device__ __forceinline__ float x32_max(float x) {
  auto s = __builtin_amdgcn_permlane32_swap(f2u(x), f2u(x), false, false);
  return fmaxf(u2f(s[0]), u2f(s[1]));
}
__device__ __forceinline__ float row_sum(float x) { x += dpp<XOR1>(x); x += dpp<XOR2>(x); x += dpp<ROR4>(x); x += dpp<ROR8>(x); return x; }
__device__ __forceinline__ float row_max(float x) {
  x = fmaxf(x, dpp<XOR1>(x)); x = fmaxf(x, dpp<XOR2>(x)); x = fmaxf(x, dpp<ROR4>(x)); x = fmaxf(x, dpp<ROR8>(x)); return x;
}
__device__ __forceinline__ f32x4 mfma32(v4u a, v4u b, f32x4 c) {
  return __builtin_amdgcn_mfma_f32_16x16x32_bf16(__builtin_bit_cast(bf16x8, a), __builtin_bit_cast(bf16x8, b), c, 0, 0, 0);
}
__device__ __forceinline__ f32x4 mfma16(v2u a, v2u b, f32x4 c) {
  return __builtin_amdgcn_mfma_f32_16x16x16bf16_1k(__builtin_bit_cast(s16x4, a), __builtin_bit_cast(s16x4, b), c, 0, 0, 0);
}
__device__ __forceinline__ bool tag_ok(v4u g, unsigned t0, unsigned t1) { return g.y == t0 && g.w == t1; }
__device__ __forceinline__ bool wave_all(bool p) { return __builtin_amdgcn_ballot_w64(!p) == 0; }

#define SB() __builtin_amdgcn_sched_barrier(0)
#define REREAD(off) asm volatile("" : "+v"(off) :: "memory")
#define VMWAIT(n) __builtin_amdgcn_s_waitcnt(((n) & 15) | (7 << 4) | (15 << 8) | ((((n) >> 4) & 3) << 14))
#define THR 7
#define THROTTLE() do { SB(); VMWAIT(THR); SB(); } while (0)
#define LDSBAR() do { __builtin_amdgcn_fence(__ATOMIC_RELEASE, "workgroup", "local"); __builtin_amdgcn_s_barrier(); \
                      __builtin_amdgcn_fence(__ATOMIC_ACQUIRE, "workgroup", "local"); } while (0)

#define STAMP(k) do { } while (0)

#define QG_AT 24
#define SG_AT 20

__global__ void __launch_bounds__(256, 1)
gated_mix_decode_kernel(const unsigned short* __restrict__ x, const unsigned short* __restrict__ kc, const unsigned short* __restrict__ vc,
                        const unsigned short* __restrict__ wq, const unsigned short* __restrict__ wo, const unsigned short* __restrict__ w1,
                        const unsigned short* __restrict__ w2, const unsigned short* __restrict__ gw, unsigned short* __restrict__ out,
                        unsigned char* ws) {
  extern __shared__ __attribute__((aligned(16))) unsigned char lds[];
  STAMP(0);
  const int id = blockIdx.x, kvh = id & 7, b = (id >> 3) & 3, j = id >> 5;
  const int tid = threadIdx.x, lane = tid & 63, w = __builtin_amdgcn_readfirstlane(tid >> 6);
  const int bk = b * HKV + kvh, u = kvh * 8 + j;
  const size_t head = ((size_t)bk * SK + (SK - WIN) + j * 256) * DH;
  const rsrc_t rK = mk(kc + head, 256 * DH * 2), rV = mk(vc + head, 256 * DH * 2);
  const rsrc_t rQ = mk(wq, DM * NQ * 2), rO = mk(wo, NQ * DM * 2), r1 = mk(w1, DM * DFF * 2), r2 = mk(w2, DFF * DM * 2);
  const rsrc_t rX = mk(x, NB * DM * 2), rG = mk(gw, DM * 2), rW = mk(ws, WS_END);

  const v2u ep = __builtin_amdgcn_raw_buffer_load_b64(rW, WS_E, 0, AUX_SC1);
  const int m16 = lane & 15, g4 = lane >> 4;
  v2u xq;
  {
    const int krow = 32 * w + 4 * (2 * g4 + (m16 & 3));
    const bool ok = (m16 & 3) < 2 && krow < 112;
    xq = __builtin_amdgcn_raw_buffer_load_b64(rX, ok ? ((m16 >> 2) * DM + 112 * b + krow) * 2 : OOB, 0, 0);
  }
  v4u wqr[4];
  {
    const int row = 32 * w + 4 * (lane >> 3);
    const int vo = row < 112 ? (112 * b + row) * (NQ * 2) + u * 128 + (lane & 7) * 16 : OOB;
#pragma unroll
    for (int i = 0; i < 4; ++i) wqr[i] = ld16<AUX_NT>(rQ, vo, i * (NQ * 2));
  }
  SB();
  v4u kr[32];
  const int ko = w * 4096 + (lane >> 3) * 512 + (lane & 7) * 16;
#define KLOAD(i) kr[i] = ld16<KVAUX>(rK, ko, ((i) >> 2) * 16384 + ((i) & 3) * 128)
  KLOAD(0); KLOAD(1); KLOAD(2); KLOAD(3);
  SB();
  const int f = kvh * 7 + j;
  v4u xh, xg, gg;
  {
    const int row = 128 * w + 8 * (4 * g4 + (m16 >> 2));
    xh = ld16<0>(rX, ((m16 & 3) == 0 && row < DM) ? (b * DM + row) * 2 : OOB, 0);
    xg = ld16<0>(rX, lane < 56 ? b * DM * 2 + lane * 16 : OOB, 0);
    gg = ld16<0>(rG, lane < 56 ? lane * 16 : OOB, 0);
  }
  const int row1 = 128 * w + 8 * (lane >> 2);
  const int o1 = (j < 7 && row1 < DM) ? row1 * (DFF * 2) + f * 64 + (lane & 3) * 16 : OOB;
  v4u w1r[8];
#define W1LOAD(i) w1r[i] = ld16<WAUX>(r1, o1, (i) * (DFF * 2))
  W1LOAD(0);
  SB();
  KLOAD(4); KLOAD(5); KLOAD(6); KLOAD(7);
  SB();
  if (tid < 16) *(unsigned*)(lds + L_Z + tid * 4) = 0u;
  const unsigned t0 = __builtin_amdgcn_readfirstlane(ep.x) + 1u, t1 = __builtin_amdgcn_readfirstlane(ep.y) + 1u;

  {
    float qv[8];
#pragma unroll
    for (int dw = 0; dw < 4; ++dw) {
      const v2u blo = {perm_lo(wqr[0][dw], wqr[1][dw]), perm_lo(wqr[2][dw], wqr[3][dw])};
      const v2u bhi = {perm_hi(wqr[0][dw], wqr[1][dw]), perm_hi(wqr[2][dw], wqr[3][dw])};
      const f32x4 z = {0.f, 0.f, 0.f, 0.f};
      const f32x4 dl = mfma16(xq, blo, z), dh = mfma16(xq, bhi, z);
      const float sl = (lane & 8) ? dl[1] : dl[0], sh = (lane & 8) ? dh[1] : dh[0];
      qv[2 * dw] = sl + dpp<ROR8>(sl);
      qv[2 * dw + 1] = sh + dpp<ROR8>(sh);
    }
    if ((lane & 8) == 0) {
      f32x4* p = (f32x4*)(lds + L_QP + ((w * 4 + g4) * 64 + 8 * (lane & 7)) * 4);
      p[0] = f32x4{qv[0], qv[1], qv[2], qv[3]};
      p[1] = f32x4{qv[4], qv[5], qv[6], qv[7]};
    }
  }
  STAMP(1);
  W1LOAD(1);
  THROTTLE(); KLOAD(8);
  SB();
  LDSBAR();
  {
    f32x2 s = {0.f, 0.f};
#pragma unroll
    for (int ww = 0; ww < 4; ++ww) s += *(const f32x2*)(lds + L_QP + ((ww * 4 + w) * 64 + 2 * (lane & 31)) * 4);
    const v4u gq = {f2u(s.x), t0, f2u(s.y), t1};
    st16_dev(rW, gq, lane < 32 ? WS_Q + (kvh * 4 + w) * 16384 + j * 2048 + b * 512 + lane * 16 : OOB);
  }
  STAMP(2);

  float gate;
  {
    float d = 0.f;
#pragma unroll
    for (int k = 0; k < 4; ++k) d = dot2(xg[k], gg[k], d);
    d = x32_sum(x16_sum(row_sum(d)));
    gate = 1.f / (1.f + __expf(-d));
  }

  v4u gq4[4];
  int qgo = WS_Q + (kvh * 4 + b) * 16384 + (2 * w + (lane >> 5)) * 2048 + (lane & 31) * 16;
#pragma unroll
  for (int i = 9; i < 32; ++i) {
    THROTTLE(); KLOAD(i);
    if ((i & 3) == 3) { THROTTLE(); W1LOAD(i >> 2); }
    if (i == QG_AT) {
      SB();
#pragma unroll
      for (int k = 0; k < 4; ++k) gq4[k] = ld16<AUX_SC1>(rW, qgo, k * 512);
    }
  }
  v4u vr[32];
  const int vo = w * 4096 + lane * 16;
#define VLOAD(i) vr[i] = ld16<KVAUX>(rV, vo, ((i) >> 2) * 16384 + ((i) & 3) * 1024)
#pragma unroll
  for (int i = 0; i < 4; ++i) { THROTTLE(); VLOAD(i); }
  SB();
  STAMP(3);

  {
    bool ok = tag_ok(gq4[0], t0, t1) && tag_ok(gq4[1], t0, t1) && tag_ok(gq4[2], t0, t1) && tag_ok(gq4[3], t0, t1);
    if (!wave_all(ok)) {
      _Pragma("nounroll") for (unsigned spin = 0; spin < SPIN_CAP; ++spin) {
        REREAD(qgo);
#pragma unroll
        for (int k = 0; k < 4; ++k) gq4[k] = ld16<AUX_SC1>(rW, qgo, k * 512);
        ok = tag_ok(gq4[0], t0, t1) && tag_ok(gq4[1], t0, t1) && tag_ok(gq4[2], t0, t1) && tag_ok(gq4[3], t0, t1);
        if (wave_all(ok)) break;
        __builtin_amdgcn_s_sleep(2);
      }
    }
    const float qa = (u2f(gq4[0].x) + u2f(gq4[1].x)) + (u2f(gq4[2].x) + u2f(gq4[3].x));
    const float qb = (u2f(gq4[0].z) + u2f(gq4[1].z)) + (u2f(gq4[2].z) + u2f(gq4[3].z));
    *(unsigned*)(lds + L_Q + (64 * w + lane) * 4) = pkbf(qa, qb);
  }
#pragma unroll
  for (int i = 4; i < 8; ++i) { THROTTLE(); VLOAD(i); }
  SB();

  {
    float hv[8];
#pragma unroll
    for (int dw = 0; dw < 4; ++dw) {
      const v4u blo = {perm_lo(w1r[0][dw], w1r[1][dw]), perm_lo(w1r[2][dw], w1r[3][dw]), perm_lo(w1r[4][dw], w1r[5][dw]), perm_lo(w1r[6][dw], w1r[7][dw])};
      const v4u bhi = {perm_hi(w1r[0][dw], w1r[1][dw]), perm_hi(w1r[2][dw], w1r[3][dw]), perm_hi(w1r[4][dw], w1r[5][dw]), perm_hi(w1r[6][dw], w1r[7][dw])};
      const f32x4 z = {0.f, 0.f, 0.f, 0.f};
      hv[2 * dw] = mfma32(xh, blo, z)[0];
      hv[2 * dw + 1] = mfma32(xh, bhi, z)[0];
    }
    if ((m16 >> 2) == g4) {
      f32x4* p = (f32x4*)(lds + L_H + ((w * 4 + g4) * 32 + 8 * (lane & 3)) * 4);
      p[0] = f32x4{hv[0], hv[1], hv[2], hv[3]};
      p[1] = f32x4{hv[4], hv[5], hv[6], hv[7]};
    }
  }
  SB();
  LDSBAR();
  STAMP(4);

  float m0, m1;
  {
    v4u qf[2][4];
#pragma unroll
    for (int h = 0; h < 2; ++h)
#pragma unroll
      for (int t = 0; t < 4; ++t) qf[h][t] = *(const v4u*)(lds + L_Q + (h * 256 + 64 * t + 8 * (lane & 7)) * 2);
    float s[8][2];
#pragma unroll
    for (int i = 0; i < 8; ++i) {
      float a0 = 0.f, a1 = 0.f;
#pragma unroll
      for (int t = 0; t < 4; ++t)
#pragma unroll
        for (int dw = 0; dw < 4; ++dw) { a0 = dot2(kr[4 * i + t][dw], qf[0][t][dw], a0); a1 = dot2(kr[4 * i + t][dw], qf[1][t][dw], a1); }
      s[i][0] = a0; s[i][1] = a1;
      if (i & 1) { THROTTLE(); VLOAD(8 + (i >> 1)); SB(); }
    }
#pragma unroll
    for (int i = 0; i < 4; ++i)
#pragma unroll
      for (int h = 0; h < 2; ++h) {
        const float lo = s[i][h] + dpp<XOR7>(s[i][h]), hi = s[i + 4][h] + dpp<XOR7>(s[i + 4][h]);
        s[i][h] = (lane & 4) ? hi : lo;
      }
#pragma unroll
    for (int i = 0; i < 2; ++i)
#pragma unroll
      for (int h = 0; h < 2; ++h) {
        const float lo = s[i][h] + dpp<XOR2>(s[i][h]), hi = s[i + 2][h] + dpp<XOR2>(s[i + 2][h]);
        s[i][h] = (lane & 2) ? hi : lo;
      }
#pragma unroll
    for (int h = 0; h < 2; ++h) {
      const float lo = s[0][h] + dpp<XOR1>(s[0][h]), hi = s[1][h] + dpp<XOR1>(s[1][h]);
      s[0][h] = (lane & 1) ? hi : lo;
    }
    constexpr float C2 = 0.0625f * 1.4426950408889634f;
    const float z0 = s[0][0] * C2, z1 = s[0][1] * C2;
    m0 = x32_max(x16_max(row_max(z0)));
    m1 = x32_max(x16_max(row_max(z1)));
    const float p0 = __builtin_amdgcn_exp2f(z0 - m0), p1 = __builtin_amdgcn_exp2f(z1 - m1);
    const unsigned pp = pkbf(p0, p1);
    unsigned short* pl = (unsigned short*)(lds + L_P + w * 256 + ((lane >> 3) & 1) * 64 + (lane & 7) * 8 + (lane >> 4) * 2);
    pl[0] = (unsigned short)pp;
    pl[64] = (unsigned short)(pp >> 16);
    const float l0 = x32_sum(x16_sum(row_sum(p0))), l1 = x32_sum(x16_sum(row_sum(p1)));
    const v4u gs = {f2u(lane ? m1 : m0), t0, f2u(lane ? l1 : l0), t1};
    st16_dev(rW, gs, lane < 2 ? WS_S + bk * 1024 + lane * 512 + (j * 4 + w) * 16 : OOB);
  }
  STAMP(5);
  v4u pa[4];
  {
    const bool valid = (m16 & 3) == 0 && ((m16 >> 2) & 1) == (g4 & 1);
    const int a = valid ? L_P + w * 256 + (m16 >> 3) * 128 + (g4 >> 1) * 64 : L_Z;
    __builtin_amdgcn_fence(__ATOMIC_ACQ_REL, "wavefront", "local");
#pragma unroll
    for (int G = 0; G < 4; ++G) pa[G] = *(const v4u*)(lds + a + G * 16);
  }
  unsigned hpair;
  {
    f32x2 hs = *(const f32x2*)(lds + L_H + ((lane >> 2) * 32 + 8 * w + 2 * (lane & 3)) * 4);
    float a = hs.x, c = hs.y;
    a += dpp<ROR4>(a); c += dpp<ROR4>(c);
    a += dpp<ROR8>(a); c += dpp<ROR8>(c);
    a = x32_sum(x16_sum(a)); c = x32_sum(x16_sum(c));
    hpair = pkbf(fmaxf(a, 0.f), fmaxf(c, 0.f));
  }

  f32x4 acc[8];
#pragma unroll
  for (int e = 0; e < 8; ++e) acc[e] = f32x4{0.f, 0.f, 0.f, 0.f};
  v4u tr[24];
  const int o2 = (j < 7 && lane < 56) ? (32 * f + 8 * w) * (DM * 2) + lane * 16 : OOB;
  const int oo = lane < 56 ? (u * 64 + 16 * w) * (DM * 2) + lane * 16 : OOB;
#define TLOAD(i) tr[i] = (i) < 8 ? ld16<WAUX>(r2, o2, (i) * (DM * 2)) : ld16<WAUX>(rO, oo, ((i) - 8) * (DM * 2))
  v4u gst;
  int sgo = WS_S + bk * 1024 + lane * 16;
  float oscale = 0.f;
#define PVGROUP(G) do { \
    _Pragma("unroll") for (int dw = 0; dw < 4; ++dw) { \
      const v4u blo = {perm_lo(vr[8 * (G)][dw], vr[8 * (G) + 1][dw]), perm_lo(vr[8 * (G) + 2][dw], vr[8 * (G) + 3][dw]), \
                       perm_lo(vr[8 * (G) + 4][dw], vr[8 * (G) + 5][dw]), perm_lo(vr[8 * (G) + 6][dw], vr[8 * (G) + 7][dw])}; \
      const v4u bhi = {perm_hi(vr[8 * (G)][dw], vr[8 * (G) + 1][dw]), perm_hi(vr[8 * (G) + 2][dw], vr[8 * (G) + 3][dw]), \
                       perm_hi(vr[8 * (G) + 4][dw], vr[8 * (G) + 5][dw]), perm_hi(vr[8 * (G) + 6][dw], vr[8 * (G) + 7][dw])}; \
      acc[2 * dw] = mfma32(pa[G], blo, acc[2 * dw]); \
      acc[2 * dw + 1] = mfma32(pa[G], bhi, acc[2 * dw + 1]); } } while (0)
#pragma unroll
  for (int i = 12; i < 40; ++i) {
    THROTTLE();
    if (i < 32) VLOAD(i); else TLOAD(i - 32);
    SB();
    if (i == SG_AT) { gst = ld16<AUX_SC1>(rW, sgo, 0); SB(); }
    if (i == 15) { PVGROUP(0); SB(); }
    if (i == 23) { PVGROUP(1); SB(); }
    if (i == 31) { PVGROUP(2); SB(); }
    if (i == 33) {
      if (!wave_all(tag_ok(gst, t0, t1))) {
        _Pragma("nounroll") for (unsigned spin = 0; spin < SPIN_CAP; ++spin) {
        REREAD(sgo);
          gst = ld16<AUX_SC1>(rW, sgo, 0);
          if (wave_all(tag_ok(gst, t0, t1))) break;
          __builtin_amdgcn_s_sleep(2);
        }
      }
      const float me = u2f(gst.x), M = x16_max(row_max(me));
      const float L = x16_sum(row_sum(u2f(gst.z) * __builtin_amdgcn_exp2f(me - M)));
      oscale = __builtin_amdgcn_exp2f((lane < 32 ? m0 : m1) - M) / L;
      SB();
    }
  }
  PVGROUP(3);
  STAMP(6);
  {
    f32x4* p = (f32x4*)(lds + L_OM + (w * 512 + 8 * lane) * 4);
    p[0] = f32x4{acc[0][0] * oscale, acc[1][0] * oscale, acc[2][0] * oscale, acc[3][0] * oscale};
    p[1] = f32x4{acc[4][0] * oscale, acc[5][0] * oscale, acc[6][0] * oscale, acc[7][0] * oscale};
  }
  SB();
  LDSBAR();
  {
    f32x2 s = {0.f, 0.f};
#pragma unroll
    for (int ww = 0; ww < 4; ++ww) s += *(const f32x2*)(lds + L_OM + (ww * 512 + 2 * tid) * 4);
    const v4u go = {f2u(s.x), t0, f2u(s.y), t1};
    st16_dev(rW, go, WS_O + (bk * 8 + (tid >> 5)) * 4096 + ((tid >> 3) & 3) * 1024 + j * 128 + (tid & 7) * 16);
  }
  SB();
  STAMP(7);
#pragma unroll
  for (int i = 8; i < 24; ++i) TLOAD(i);
  SB();
  int ogo = WS_O + (bk * 8 + j) * 4096 + w * 1024 + lane * 16;
  v4u go = ld16<AUX_SC1>(rW, ogo, 0);
  SB();

  float af[8], ao[8];
#pragma unroll
  for (int e = 0; e < 8; ++e) { af[e] = 0.f; ao[e] = 0.f; }
#pragma unroll
  for (int pr = 0; pr < 4; ++pr) {
    const unsigned hp = __builtin_amdgcn_readlane(hpair, pr);
#pragma unroll
    for (int dw = 0; dw < 4; ++dw) {
      af[2 * dw] = dot2(perm_lo(tr[2 * pr][dw], tr[2 * pr + 1][dw]), hp, af[2 * dw]);
      af[2 * dw + 1] = dot2(perm_hi(tr[2 * pr][dw], tr[2 * pr + 1][dw]), hp, af[2 * dw + 1]);
    }
  }
  unsigned wp[8][8];
#pragma unroll
  for (int pr = 0; pr < 8; ++pr)
#pragma unroll
    for (int dw = 0; dw < 4; ++dw) {
      wp[pr][2 * dw] = perm_lo(tr[8 + 2 * pr][dw], tr[9 + 2 * pr][dw]);
      wp[pr][2 * dw + 1] = perm_hi(tr[8 + 2 * pr][dw], tr[9 + 2 * pr][dw]);
    }
#pragma unroll
  for (int pr = 0; pr < 8; ++pr)
#pragma unroll
    for (int e = 0; e < 8; ++e) asm volatile("" : "+v"(wp[pr][e]));
#pragma unroll
  for (int e = 0; e < 8; ++e) { af[e] *= (1.f - gate); asm volatile("" : "+v"(af[e])); }
  SB();
  if (!wave_all(tag_ok(go, t0, t1))) {
    _Pragma("nounroll") for (unsigned spin = 0; spin < SPIN_CAP; ++spin) {
        REREAD(ogo);
      go = ld16<AUX_SC1>(rW, ogo, 0);
      if (wave_all(tag_ok(go, t0, t1))) break;
    }
  }
  STAMP(8);
  {
    float a = u2f(go.x), c = u2f(go.z);
    a += dpp<ROR8>(a); c += dpp<ROR8>(c);
    a = x32_sum(x16_sum(a)); c = x32_sum(x16_sum(c));
    const unsigned op = pkbf(a, c);
#pragma unroll
    for (int pr = 0; pr < 8; ++pr) {
      const unsigned so = __builtin_amdgcn_readlane(op, pr);
#pragma unroll
      for (int e = 0; e < 8; ++e) ao[e] = dot2(wp[pr][e], so, ao[e]);
    }
    if (lane < 56) {
      f32x4* p = (f32x4*)(lds + L_FM + (w * DM + 8 * lane) * 4);
      p[0] = f32x4{fmaf(gate, ao[0], af[0]), fmaf(gate, ao[1], af[1]), fmaf(gate, ao[2], af[2]), fmaf(gate, ao[3], af[3])};
      p[1] = f32x4{fmaf(gate, ao[4], af[4]), fmaf(gate, ao[5], af[5]), fmaf(gate, ao[6], af[6]), fmaf(gate, ao[7], af[7])};
    }
  }
  SB();
  LDSBAR();
  {
    const int t = tid < 224 ? tid : 0;
    f32x2 s = {0.f, 0.f};
#pragma unroll
    for (int ww = 0; ww < 4; ++ww) s += *(const f32x2*)(lds + L_FM + (ww * DM + 2 * t) * 4);
    const v4u gf = {f2u(s.x), t0, f2u(s.y), t1};
    st16_dev(rW, gf, tid < 224 ? WS_F + (b * 56 + (tid >> 2)) * 4096 + u * 64 + (tid & 3) * 16 : OOB);
  }
  STAMP(9);
  if (w == 0 && u < 56) {
    int fgo = WS_F + (b * 56 + u) * 4096 + lane * 16;
    v4u gf[4];
    _Pragma("nounroll") for (unsigned spin = 0; spin < SPIN_CAP; ++spin) {
        REREAD(fgo);
#pragma unroll
      for (int k = 0; k < 4; ++k) gf[k] = ld16<AUX_SC1>(rW, fgo, k * 1024);
      const bool ok = tag_ok(gf[0], t0, t1) && tag_ok(gf[1], t0, t1) && tag_ok(gf[2], t0, t1) && tag_ok(gf[3], t0, t1);
      if (wave_all(ok)) break;
    }
    float a = (u2f(gf[0].x) + u2f(gf[1].x)) + (u2f(gf[2].x) + u2f(gf[3].x));
    float c = (u2f(gf[0].z) + u2f(gf[1].z)) + (u2f(gf[2].z) + u2f(gf[3].z));
    a += dpp<ROR4>(a); c += dpp<ROR4>(c);
    a += dpp<ROR8>(a); c += dpp<ROR8>(c);
    a = x32_sum(x16_sum(a)); c = x32_sum(x16_sum(c));
    if (lane < 4) *(unsigned*)(out + b * DM + 8 * u + 2 * lane) = pkbf(a, c);
    if (id == 0 && lane == 0) __builtin_amdgcn_raw_buffer_store_b64(v2u{t0, t1}, rW, WS_E, 0, AUX_SC1);
  }
}

}

extern "C" void kernel_launch(void* const* d_in, const int* in_sizes, int n_in, void* d_out, int out_size, void* d_ws, size_t ws_size,
                              hipStream_t stream) {
  (void)in_sizes; (void)n_in; (void)out_size; (void)ws_size;
  (void)hipFuncSetAttribute((const void*)gated_mix_decode_kernel, hipFuncAttributeMaxDynamicSharedMemorySize, LDS_BYTES);
  gated_mix_decode_kernel<<<256, 256, LDS_BYTES, stream>>>(
      (const unsigned short*)d_in[0], (const unsigned short*)d_in[1], (const unsigned short*)d_in[2], (const unsigned short*)d_in[3],
      (const unsigned short*)d_in[4], (const unsigned short*)d_in[6], (const unsigned short*)d_in[7], (const unsigned short*)d_in[8],
      (unsigned short*)d_out, (unsigned char*)d_ws);
}
```

```cpp
#include <hip/hip_runtime.h>
#include <hip/hip_bf16.h>
#include <cstdint>

namespace {

typedef unsigned v4u __attribute__((ext_vector_type(4)));
typedef unsigned v2u __attribute__((ext_vector_type(2)));
typedef unsigned v16u __attribute__((ext_vector_type(16)));
typedef float f32x4 __attribute__((ext_vector_type(4)));
typedef float f32x2 __attribute__((ext_vector_type(2)));
typedef short s16x4 __attribute__((ext_vector_type(4)));
typedef __bf16 bf16x8 __attribute__((ext_vector_type(8)));
typedef __bf16 bf16x2 __attribute__((ext_vector_type(2)));
typedef __amdgpu_buffer_rsrc_t rsrc_t;

constexpr int NB = 4, DM = 448, HKV = 8, SK = 8192, DH = 256, WIN = 2048, DFF = 1792, NQ = 4096;
constexpr int AUX_NT = 2, AUX_SC1 = 16;
#define WAUX 2
#define KVAUX 2
constexpr int OOB = 0x40000000;
constexpr unsigned SPIN_CAP = 200000;

constexpr int WS_E = 0;
constexpr int WS_Q = 4096;
constexpr int WS_S = WS_Q + 8 * 4 * 16384;
constexpr int WS_O = WS_S + 32 * 1024;
constexpr int WS_F = WS_O + 32 * 8 * 4096;
constexpr int WS_END = WS_F + 4 * 56 * 4096;
constexpr int WS_DIAG = 4 << 20;

constexpr int L_QP = 0;
constexpr int L_Q = 4096;
constexpr int L_P = 5120;
constexpr int L_Z = 6144;
constexpr int L_H = 6400;
constexpr int L_OM = 8448;
constexpr int L_FM = 16640;
constexpr int L_END = 23808;
constexpr int LDS_BYTES = 96 * 1024;
static_assert(L_END <= LDS_BYTES, "lds");

__device__ __forceinline__ rsrc_t mk(const void* p, int bytes) { return __builtin_amdgcn_make_buffer_rsrc((void*)p, 0, bytes, 0x00020000); }
template <int AUX> __device__ __forceinline__ v4u ld16(rsrc_t r, int vo, int so) { return __builtin_amdgcn_raw_buffer_load_b128(r, vo, so, AUX); }
__device__ __forceinline__ void st16_dev(rsrc_t r, v4u v, int vo) { __builtin_amdgcn_raw_buffer_store_b128(v, r, vo, 0, AUX_SC1); }
__device__ __forceinline__ float u2f(unsigned u) { return __builtin_bit_cast(float, u); }
__device__ __forceinline__ unsigned f2u(float f) { return __builtin_bit_cast(unsigned, f); }
__device__ __forceinline__ float dot2(unsigned a, unsigned b, float c) {
  return __builtin_amdgcn_fdot2_f32_bf16(__builtin_bit_cast(bf16x2, a), __builtin_bit_cast(bf16x2, b), c, false);
}
__device__ __forceinline__ unsigned pkbf(float lo, float hi) {
  const bf16x2 v = {(__bf16)lo, (__bf16)hi};
  return __builtin_bit_cast(unsigned, v);
}
__device__ __forceinline__ unsigned perm_lo(unsigned a, unsigned b) { return __builtin_amdgcn_perm(b, a, 0x05040100u); }
__device__ __forceinline__ unsigned perm_hi(unsigned a, unsigned b) { return __builtin_amdgcn_perm(b, a, 0x07060302u); }
template <int CTRL> __device__ __forceinline__ float dpp(float x) {
  return __builtin_bit_cast(float, __builtin_amdgcn_mov_dpp(__builtin_bit_cast(int, x), CTRL, 0xf, 0xf, true));
}
constexpr int XOR1 = 0xB1, XOR2 = 0x4E, XOR7 = 0x141, ROR8 = 0x128, ROR4 = 0x124;
__device__ __forceinline__ float x16_sum(float x) {
  auto s = __builtin_amdgcn_permlane16_swap(f2u(x), f2u(x), false, false);
  return u2f(s[0]) + u2f(s[1]);
}
__device__ __forceinline__ float x32_sum(float x) {
  auto s = __builtin_amdgcn_permlane32_swap(f2u(x), f2u(x), false, false);
  return u2f(s[0]) + u2f(s[1]);
}
__device__ __forceinline__ float x16_max(float x) {
  auto s = __builtin_amdgcn_permlane16_swap(f2u(x), f2u(x), false, false);
  return fmaxf(u2f(s[0]), u2f(s[1]));
}
__device__ __forceinline__ float x32_max(float x) {
  auto s = __builtin_amdgcn_permlane32_swap(f2u(x), f2u(x), false, false);
  return fmaxf(u2f(s[0]), u2f(s[1]));
}
__device__ __forceinline__ float row_sum(float x) { x += dpp<XOR1>(x); x += dpp<XOR2>(x); x += dpp<ROR4>(x); x += dpp<ROR8>(x); return x; }
__device__ __forceinline__ float row_max(float x) {
  x = fmaxf(x, dpp<XOR1>(x)); x = fmaxf(x, dpp<XOR2>(x)); x = fmaxf(x, dpp<ROR4>(x)); x = fmaxf(x, dpp<ROR8>(x)); return x;
}
__device__ __forceinline__ f32x4 mfma32(v4u a, v4u b, f32x4 c) {
  return __builtin_amdgcn_mfma_f32_16x16x32_bf16(__builtin_bit_cast(bf16x8, a), __builtin_bit_cast(bf16x8, b), c, 0, 0, 0);
}
__device__ __forceinline__ f32x4 mfma16(v2u a, v2u b, f32x4 c) {
  return __builtin_amdgcn_mfma_f32_16x16x16bf16_1k(__builtin_bit_cast(s16x4, a), __builtin_bit_cast(s16x4, b), c, 0, 0, 0);
}
__device__ __forceinline__ bool tag_ok(v4u g, unsigned t0, unsigned t1) { return g.y == t0 && g.w == t1; }
__device__ __forceinline__ bool wave_all(bool p) { return __builtin_amdgcn_ballot_w64(!p) == 0; }

#define SB() __builtin_amdgcn_sched_barrier(0)
#define REREAD(off) asm volatile("" : "+v"(off) :: "memory")
#define VMWAIT(n) __builtin_amdgcn_s_waitcnt(((n) & 15) | (7 << 4) | (15 << 8) | ((((n) >> 4) & 3) << 14))
#define THR 7
#define THROTTLE() do { SB(); VMWAIT(THR); SB(); } while (0)
#define LDSBAR() do { __builtin_amdgcn_fence(__ATOMIC_RELEASE, "workgroup", "local"); __builtin_amdgcn_s_barrier(); \
                      __builtin_amdgcn_fence(__ATOMIC_ACQUIRE, "workgroup", "local"); } while (0)

#define STAMP(k) do { } while (0)

#define QG_AT 24
#define SG_AT 20

__global__ void __launch_bounds__(256, 1)
gated_mix_decode_kernel(const unsigned short*, const unsigned short*, const unsigned short*, const unsigned short*, const unsigned short*,
                        const unsigned short*, const unsigned short*, const unsigned short*, unsigned short*, unsigned char*) {
  extern __shared__ __attribute__((aligned(16))) unsigned char lds[];
  STAMP(0);
  v16u ka; v4u kb;
  asm volatile("s_load_dwordx16 %0, %2, 0x0\n\ts_load_dwordx4 %1, %2, 0x40\n\ts_waitcnt lgkmcnt(0)"
               : "=&s"(ka), "=&s"(kb) : "s"(__builtin_amdgcn_kernarg_segment_ptr()) : "memory");
  STAMP(11);
#define KARG(T, lo, hi) ((T)(((unsigned long long)(hi) << 32) | (lo)))
  const unsigned short* x = KARG(const unsigned short*, ka[0], ka[1]);
  const unsigned short* kc = KARG(const unsigned short*, ka[2], ka[3]);
  const unsigned short* vc = KARG(const unsigned short*, ka[4], ka[5]);
  const unsigned short* wq = KARG(const unsigned short*, ka[6], ka[7]);
  const unsigned short* wo = KARG(const unsigned short*, ka[8], ka[9]);
  const unsigned short* w1 = KARG(const unsigned short*, ka[10], ka[11]);
  const unsigned short* w2 = KARG(const unsigned short*, ka[12], ka[13]);
  const unsigned short* gw = KARG(const unsigned short*, ka[14], ka[15]);
  unsigned short* out = KARG(unsigned short*, kb[0], kb[1]);
  unsigned char* ws = KARG(unsigned char*, kb[2], kb[3]);
  const int id = blockIdx.x, kvh = id & 7, b = (id >> 3) & 3, j = id >> 5;
  const int tid = threadIdx.x, lane = tid & 63, w = __builtin_amdgcn_readfirstlane(tid >> 6);
  const int bk = b * HKV + kvh, u = kvh * 8 + j;
  const size_t head = ((size_t)bk * SK + (SK - WIN) + j * 256) * DH;
  const rsrc_t rK = mk(kc + head, 256 * DH * 2), rV = mk(vc + head, 256 * DH * 2);
  const rsrc_t rQ = mk(wq, DM * NQ * 2), rO = mk(wo, NQ * DM * 2), r1 = mk(w1, DM * DFF * 2), r2 = mk(w2, DFF * DM * 2);
  const rsrc_t rX = mk(x, NB * DM * 2), rG = mk(gw, DM * 2), rW = mk(ws, WS_END);

  const v2u ep = __builtin_amdgcn_raw_buffer_load_b64(rW, WS_E, 0, AUX_SC1);
  const int m16 = lane & 15, g4 = lane >> 4;
  v2u xq;
  {
    const int krow = 32 * w + 4 * (2 * g4 + (m16 & 3));
    const bool ok = (m16 & 3) < 2 && krow < 112;
    xq = __builtin_amdgcn_raw_buffer_load_b64(rX, ok ? ((m16 >> 2) * DM + 112 * b + krow) * 2 : OOB, 0, 0);
  }
  v4u wqr[4];
  {
    const int row = 32 * w + 4 * (lane >> 3);
    const int vo = row < 112 ? (112 * b + row) * (NQ * 2) + u * 128 + (lane & 7) * 16 : OOB;
#pragma unroll
    for (int i = 0; i < 4; ++i) wqr[i] = ld16<AUX_NT>(rQ, vo, i * (NQ * 2));
  }
  SB();
  v4u kr[32];
  const int ko = w * 4096 + (lane >> 3) * 512 + (lane & 7) * 16;
#define KLOAD(i) kr[i] = ld16<KVAUX>(rK, ko, ((i) >> 2) * 16384 + ((i) & 3) * 128)
  KLOAD(0); KLOAD(1); KLOAD(2); KLOAD(3);
  SB();
  const int f = kvh * 7 + j;
  v4u xh, xg, gg;
  {
    const int row = 128 * w + 8 * (4 * g4 + (m16 >> 2));
    xh = ld16<0>(rX, ((m16 & 3) == 0 && row < DM) ? (b * DM + row) * 2 : OOB, 0);
    xg = ld16<0>(rX, lane < 56 ? b * DM * 2 + lane * 16 : OOB, 0);
    gg = ld16<0>(rG, lane < 56 ? lane * 16 : OOB, 0);
  }
  const int row1 = 128 * w + 8 * (lane >> 2);
  const int o1 = (j < 7 && row1 < DM) ? row1 * (DFF * 2) + f * 64 + (lane & 3) * 16 : OOB;
  v4u w1r[8];
#define W1LOAD(i) w1r[i] = ld16<WAUX>(r1, o1, (i) * (DFF * 2))
  W1LOAD(0);
  SB();
  KLOAD(4); KLOAD(5); KLOAD(6); KLOAD(7);
  SB();
  if (tid < 16) *(unsigned*)(lds + L_Z + tid * 4) = 0u;
  const unsigned t0 = __builtin_amdgcn_readfirstlane(ep.x) + 1u, t1 = __builtin_amdgcn_readfirstlane(ep.y) + 1u;

  {
    float qv[8];
#pragma unroll
    for (int dw = 0; dw < 4; ++dw) {
      const v2u blo = {perm_lo(wqr[0][dw], wqr[1][dw]), perm_lo(wqr[2][dw], wqr[3][dw])};
      const v2u bhi = {perm_hi(wqr[0][dw], wqr[1][dw]), perm_hi(wqr[2][dw], wqr[3][dw])};
      const f32x4 z = {0.f, 0.f, 0.f, 0.f};
      const f32x4 dl = mfma16(xq, blo, z), dh = mfma16(xq, bhi, z);
      const float sl = (lane & 8) ? dl[1] : dl[0], sh = (lane & 8) ? dh[1] : dh[0];
      qv[2 * dw] = sl + dpp<ROR8>(sl);
      qv[2 * dw + 1] = sh + dpp<ROR8>(sh);
    }
    if ((lane & 8) == 0) {
      f32x4* p = (f32x4*)(lds + L_QP + ((w * 4 + g4) * 64 + 8 * (lane & 7)) * 4);
      p[0] = f32x4{qv[0], qv[1], qv[2], qv[3]};
      p[1] = f32x4{qv[4], qv[5], qv[6], qv[7]};
    }
  }
  STAMP(1);
  W1LOAD(1);
  THROTTLE(); KLOAD(8);
  SB();
  LDSBAR();
  {
    f32x2 s = {0.f, 0.f};
#pragma unroll
    for (int ww = 0; ww < 4; ++ww) s += *(const f32x2*)(lds + L_QP + ((ww * 4 + w) * 64 + 2 * (lane & 31)) * 4);
    const v4u gq = {f2u(s.x), t0, f2u(s.y), t1};
    st16_dev(rW, gq, lane < 32 ? WS_Q + (kvh * 4 + w) * 16384 + j * 2048 + b * 512 + lane * 16 : OOB);
  }
  STAMP(2);

  float gate;
  {
    float d = 0.f;
#pragma unroll
    for (int k = 0; k < 4; ++k) d = dot2(xg[k], gg[k], d);
    d = x32_sum(x16_sum(row_sum(d)));
    gate = 1.f / (1.f + __expf(-d));
  }

  v4u gq4[4];
  int qgo = WS_Q + (kvh * 4 + b) * 16384 + (2 * w + (lane >> 5)) * 2048 + (lane & 31) * 16;
#pragma unroll
  for (int i = 9; i < 32; ++i) {
    THROTTLE(); KLOAD(i);
    if ((i & 3) == 3) { THROTTLE(); W1LOAD(i >> 2); }
    if (i == QG_AT) {
      SB();
#pragma unroll
      for (int k = 0; k < 4; ++k) gq4[k] = ld16<AUX_SC1>(rW, qgo, k * 512);
    }
  }
  v4u vr[32];
  const int vo = w * 4096 + lane * 16;
#define VLOAD(i) vr[i] = ld16<KVAUX>(rV, vo, ((i) >> 2) * 16384 + ((i) & 3) * 1024)
#pragma unroll
  for (int i = 0; i < 4; ++i) { THROTTLE(); VLOAD(i); }
  SB();
  STAMP(3);

  {
    bool ok = tag_ok(gq4[0], t0, t1) && tag_ok(gq4[1], t0, t1) && tag_ok(gq4[2], t0, t1) && tag_ok(gq4[3], t0, t1);
    if (!wave_all(ok)) {
      _Pragma("nounroll") for (unsigned spin = 0; spin < SPIN_CAP; ++spin) {
        REREAD(qgo);
#pragma unroll
        for (int k = 0; k < 4; ++k) gq4[k] = ld16<AUX_SC1>(rW, qgo, k * 512);
        ok = tag_ok(gq4[0], t0, t1) && tag_ok(gq4[1], t0, t1) && tag_ok(gq4[2], t0, t1) && tag_ok(gq4[3], t0, t1);
        if (wave_all(ok)) break;
        __builtin_amdgcn_s_sleep(2);
      }
    }
    const float qa = (u2f(gq4[0].x) + u2f(gq4[1].x)) + (u2f(gq4[2].x) + u2f(gq4[3].x));
    const float qb = (u2f(gq4[0].z) + u2f(gq4[1].z)) + (u2f(gq4[2].z) + u2f(gq4[3].z));
    *(unsigned*)(lds + L_Q + (64 * w + lane) * 4) = pkbf(qa, qb);
  }
#pragma unroll
  for (int i = 4; i < 8; ++i) { THROTTLE(); VLOAD(i); }
  SB();

  {
    float hv[8];
#pragma unroll
    for (int dw = 0; dw < 4; ++dw) {
      const v4u blo = {perm_lo(w1r[0][dw], w1r[1][dw]), perm_lo(w1r[2][dw], w1r[3][dw]), perm_lo(w1r[4][dw], w1r[5][dw]), perm_lo(w1r[6][dw], w1r[7][dw])};
      const v4u bhi = {perm_hi(w1r[0][dw], w1r[1][dw]), perm_hi(w1r[2][dw], w1r[3][dw]), perm_hi(w1r[4][dw], w1r[5][dw]), perm_hi(w1r[6][dw], w1r[7][dw])};
      const f32x4 z = {0.f, 0.f, 0.f, 0.f};
      hv[2 * dw] = mfma32(xh, blo, z)[0];
      hv[2 * dw + 1] = mfma32(xh, bhi, z)[0];
    }
    if ((m16 >> 2) == g4) {
      f32x4* p = (f32x4*)(lds + L_H + ((w * 4 + g4) * 32 + 8 * (lane & 3)) * 4);
      p[0] = f32x4{hv[0], hv[1], hv[2], hv[3]};
      p[1] = f32x4{hv[4], hv[5], hv[6], hv[7]};
    }
  }
  SB();
  LDSBAR();
  STAMP(4);

  float m0, m1;
  {
    v4u qf[2][4];
#pragma unroll
    for (int h = 0; h < 2; ++h)
#pragma unroll
      for (int t = 0; t < 4; ++t) qf[h][t] = *(const v4u*)(lds + L_Q + (h * 256 + 64 * t + 8 * (lane & 7)) * 2);
    float s[8][2];
#pragma unroll
    for (int i = 0; i < 8; ++i) {
      float a0 = 0.f, a1 = 0.f;
#pragma unroll
      for (int t = 0; t < 4; ++t)
#pragma unroll
        for (int dw = 0; dw < 4; ++dw) { a0 = dot2(kr[4 * i + t][dw], qf[0][t][dw], a0); a1 = dot2(kr[4 * i + t][dw], qf[1][t][dw], a1); }
      s[i][0] = a0; s[i][1] = a1;
      if (i & 1) { THROTTLE(); VLOAD(8 + (i >> 1)); SB(); }
    }
#pragma unroll
    for (int i = 0; i < 4; ++i)
#pragma unroll
      for (int h = 0; h < 2; ++h) {
        const float lo = s[i][h] + dpp<XOR7>(s[i][h]), hi = s[i + 4][h] + dpp<XOR7>(s[i + 4][h]);
        s[i][h] = (lane & 4) ? hi : lo;
      }
#pragma unroll
    for (int i = 0; i < 2; ++i)
#pragma unroll
      for (int h = 0; h < 2; ++h) {
        const float lo = s[i][h] + dpp<XOR2>(s[i][h]), hi = s[i + 2][h] + dpp<XOR2>(s[i + 2][h]);
        s[i][h] = (lane & 2) ? hi : lo;
      }
#pragma unroll
    for (int h = 0; h < 2; ++h) {
      const float lo = s[0][h] + dpp<XOR1>(s[0][h]), hi = s[1][h] + dpp<XOR1>(s[1][h]);
      s[0][h] = (lane & 1) ? hi : lo;
    }
    constexpr float C2 = 0.0625f * 1.4426950408889634f;
    const float z0 = s[0][0] * C2, z1 = s[0][1] * C2;
    m0 = x32_max(x16_max(row_max(z0)));
    m1 = x32_max(x16_max(row_max(z1)));
    const float p0 = __builtin_amdgcn_exp2f(z0 - m0), p1 = __builtin_amdgcn_exp2f(z1 - m1);
    const unsigned pp = pkbf(p0, p1);
    unsigned short* pl = (unsigned short*)(lds + L_P + w * 256 + ((lane >> 3) & 1) * 64 + (lane & 7) * 8 + (lane >> 4) * 2);
    pl[0] = (unsigned short)pp;
    pl[64] = (unsigned short)(pp >> 16);
    const float l0 = x32_sum(x16_sum(row_sum(p0))), l1 = x32_sum(x16_sum(row_sum(p1)));
    const v4u gs = {f2u(lane ? m1 : m0), t0, f2u(lane ? l1 : l0), t1};
    st16_dev(rW, gs, lane < 2 ? WS_S + bk * 1024 + lane * 512 + (j * 4 + w) * 16 : OOB);
  }
  STAMP(5);
  v4u pa[4];
  {
    const bool valid = (m16 & 3) == 0 && ((m16 >> 2) & 1) == (g4 & 1);
    const int a = valid ? L_P + w * 256 + (m16 >> 3) * 128 + (g4 >> 1) * 64 : L_Z;
    __builtin_amdgcn_fence(__ATOMIC_ACQ_REL, "wavefront", "local");
#pragma unroll
    for (int G = 0; G < 4; ++G) pa[G] = *(const v4u*)(lds + a + G * 16);
  }
  unsigned hpair;
  {
    f32x2 hs = *(const f32x2*)(lds + L_H + ((lane >> 2) * 32 + 8 * w + 2 * (lane & 3)) * 4);
    float a = hs.x, c = hs.y;
    a += dpp<ROR4>(a); c += dpp<ROR4>(c);
    a += dpp<ROR8>(a); c += dpp<ROR8>(c);
    a = x32_sum(x16_sum(a)); c = x32_sum(x16_sum(c));
    hpair = pkbf(fmaxf(a, 0.f), fmaxf(c, 0.f));
  }

  f32x4 acc[8];
#pragma unroll
  for (int e = 0; e < 8; ++e) acc[e] = f32x4{0.f, 0.f, 0.f, 0.f};
  v4u tr[24];
  const int o2 = (j < 7 && lane < 56) ? (32 * f + 8 * w) * (DM * 2) + lane * 16 : OOB;
  const int oo = lane < 56 ? (u * 64 + 16 * w) * (DM * 2) + lane * 16 : OOB;
#define TLOAD(i) tr[i] = (i) < 8 ? ld16<WAUX>(r2, o2, (i) * (DM * 2)) : ld16<WAUX>(rO, oo, ((i) - 8) * (DM * 2))
  v4u gst;
  int sgo = WS_S + bk * 1024 + lane * 16;
  float oscale = 0.f;
#define PVGROUP(G) do { \
    _Pragma("unroll") for (int dw = 0; dw < 4; ++dw) { \
      const v4u blo = {perm_lo(vr[8 * (G)][dw], vr[8 * (G) + 1][dw]), perm_lo(vr[8 * (G) + 2][dw], vr[8 * (G) + 3][dw]), \
                       perm_lo(vr[8 * (G) + 4][dw], vr[8 * (G) + 5][dw]), perm_lo(vr[8 * (G) + 6][dw], vr[8 * (G) + 7][dw])}; \
      const v4u bhi = {perm_hi(vr[8 * (G)][dw], vr[8 * (G) + 1][dw]), perm_hi(vr[8 * (G) + 2][dw], vr[8 * (G) + 3][dw]), \
                       perm_hi(vr[8 * (G) + 4][dw], vr[8 * (G) + 5][dw]), perm_hi(vr[8 * (G) + 6][dw], vr[8 * (G) + 7][dw])}; \
      acc[2 * dw] = mfma32(pa[G], blo, acc[2 * dw]); \
      acc[2 * dw + 1] = mfma32(pa[G], bhi, acc[2 * dw + 1]); } } while (0)
#pragma unroll
  for (int i = 12; i < 40; ++i) {
    THROTTLE();
    if (i < 32) VLOAD(i); else TLOAD(i - 32);
    SB();
    if (i == SG_AT) { gst = ld16<AUX_SC1>(rW, sgo, 0); SB(); }
    if (i == 15) { PVGROUP(0); SB(); }
    if (i == 23) { PVGROUP(1); SB(); }
    if (i == 31) { PVGROUP(2); SB(); }
    if (i == 33) {
      if (!wave_all(tag_ok(gst, t0, t1))) {
        _Pragma("nounroll") for (unsigned spin = 0; spin < SPIN_CAP; ++spin) {
        REREAD(sgo);
          gst = ld16<AUX_SC1>(rW, sgo, 0);
          if (wave_all(tag_ok(gst, t0, t1))) break;
          __builtin_amdgcn_s_sleep(2);
        }
      }
      const float me = u2f(gst.x), M = x16_max(row_max(me));
      const float L = x16_sum(row_sum(u2f(gst.z) * __builtin_amdgcn_exp2f(me - M)));
      oscale = __builtin_amdgcn_exp2f((lane < 32 ? m0 : m1) - M) / L;
      SB();
    }
  }
  PVGROUP(3);
  STAMP(6);
  {
    f32x4* p = (f32x4*)(lds + L_OM + (w * 512 + 8 * lane) * 4);
    p[0] = f32x4{acc[0][0] * oscale, acc[1][0] * oscale, acc[2][0] * oscale, acc[3][0] * oscale};
    p[1] = f32x4{acc[4][0] * oscale, acc[5][0] * oscale, acc[6][0] * oscale, acc[7][0] * oscale};
  }
  SB();
  LDSBAR();
  {
    f32x2 s = {0.f, 0.f};
#pragma unroll
    for (int ww = 0; ww < 4; ++ww) s += *(const f32x2*)(lds + L_OM + (ww * 512 + 2 * tid) * 4);
    const v4u go = {f2u(s.x), t0, f2u(s.y), t1};
    st16_dev(rW, go, WS_O + (bk * 8 + (tid >> 5)) * 4096 + ((tid >> 3) & 3) * 1024 + j * 128 + (tid & 7) * 16);
  }
  SB();
  STAMP(7);
#pragma unroll
  for (int i = 8; i < 24; ++i) TLOAD(i);
  SB();
  int ogo = WS_O + (bk * 8 + j) * 4096 + w * 1024 + lane * 16;
  v4u go = ld16<AUX_SC1>(rW, ogo, 0);
  SB();

  float af[8], ao[8];
#pragma unroll
  for (int e = 0; e < 8; ++e) { af[e] = 0.f; ao[e] = 0.f; }
#pragma unroll
  for (int pr = 0; pr < 4; ++pr) {
    const unsigned hp = __builtin_amdgcn_readlane(hpair, pr);
#pragma unroll
    for (int dw = 0; dw < 4; ++dw) {
      af[2 * dw] = dot2(perm_lo(tr[2 * pr][dw], tr[2 * pr + 1][dw]), hp, af[2 * dw]);
      af[2 * dw + 1] = dot2(perm_hi(tr[2 * pr][dw], tr[2 * pr + 1][dw]), hp, af[2 * dw + 1]);
    }
  }
  unsigned wp[8][8];
#pragma unroll
  for (int pr = 0; pr < 8; ++pr)
#pragma unroll
    for (int dw = 0; dw < 4; ++dw) {
      wp[pr][2 * dw] = perm_lo(tr[8 + 2 * pr][dw], tr[9 + 2 * pr][dw]);
      wp[pr][2 * dw + 1] = perm_hi(tr[8 + 2 * pr][dw], tr[9 + 2 * pr][dw]);
    }
#pragma unroll
  for (int pr = 0; pr < 8; ++pr)
#pragma unroll
    for (int e = 0; e < 8; ++e) asm volatile("" : "+v"(wp[pr][e]));
#pragma unroll
  for (int e = 0; e < 8; ++e) { af[e] *= (1.f - gate); asm volatile("" : "+v"(af[e])); }
  SB();
  if (!wave_all(tag_ok(go, t0, t1))) {
    _Pragma("nounroll") for (unsigned spin = 0; spin < SPIN_CAP; ++spin) {
        REREAD(ogo);
      go = ld16<AUX_SC1>(rW, ogo, 0);
      if (wave_all(tag_ok(go, t0, t1))) break;
    }
  }
  STAMP(8);
  {
    float a = u2f(go.x), c = u2f(go.z);
    a += dpp<ROR8>(a); c += dpp<ROR8>(c);
    a = x32_sum(x16_sum(a)); c = x32_sum(x16_sum(c));
    const unsigned op = pkbf(a, c);
#pragma unroll
    for (int pr = 0; pr < 8; ++pr) {
      const unsigned so = __builtin_amdgcn_readlane(op, pr);
#pragma unroll
      for (int e = 0; e < 8; ++e) ao[e] = dot2(wp[pr][e], so, ao[e]);
    }
    if (lane < 56) {
      f32x4* p = (f32x4*)(lds + L_FM + (w * DM + 8 * lane) * 4);
      p[0] = f32x4{fmaf(gate, ao[0], af[0]), fmaf(gate, ao[1], af[1]), fmaf(gate, ao[2], af[2]), fmaf(gate, ao[3], af[3])};
      p[1] = f32x4{fmaf(gate, ao[4], af[4]), fmaf(gate, ao[5], af[5]), fmaf(gate, ao[6], af[6]), fmaf(gate, ao[7], af[7])};
    }
  }
  SB();
  LDSBAR();
  {
    const int t = tid < 224 ? tid : 0;
    f32x2 s = {0.f, 0.f};
#pragma unroll
    for (int ww = 0; ww < 4; ++ww) s += *(const f32x2*)(lds + L_FM + (ww * DM + 2 * t) * 4);
    const v4u gf = {f2u(s.x), t0, f2u(s.y), t1};
    st16_dev(rW, gf, tid < 224 ? WS_F + (b * 56 + (tid >> 2)) * 4096 + u * 64 + (tid & 3) * 16 : OOB);
  }
  STAMP(9);
  if (w == 0 && u < 56) {
    int fgo = WS_F + (b * 56 + u) * 4096 + lane * 16;
    v4u gf[4];
    _Pragma("nounroll") for (unsigned spin = 0; spin < SPIN_CAP; ++spin) {
        REREAD(fgo);
#pragma unroll
      for (int k = 0; k < 4; ++k) gf[k] = ld16<AUX_SC1>(rW, fgo, k * 1024);
      const bool ok = tag_ok(gf[0], t0, t1) && tag_ok(gf[1], t0, t1) && tag_ok(gf[2], t0, t1) && tag_ok(gf[3], t0, t1);
      if (wave_all(ok)) break;
    }
    float a = (u2f(gf[0].x) + u2f(gf[1].x)) + (u2f(gf[2].x) + u2f(gf[3].x));
    float c = (u2f(gf[0].z) + u2f(gf[1].z)) + (u2f(gf[2].z) + u2f(gf[3].z));
    a += dpp<ROR4>(a); c += dpp<ROR4>(c);
    a += dpp<ROR8>(a); c += dpp<ROR8>(c);
    a = x32_sum(x16_sum(a)); c = x32_sum(x16_sum(c));
    __builtin_amdgcn_raw_buffer_store_b32(pkbf(a, c), mk(out, NB * DM * 2), lane < 4 ? (b * DM + 8 * u + 2 * lane) * 2 : OOB, 0, 0);
    if (id == 0 && lane == 0) __builtin_amdgcn_raw_buffer_store_b64(v2u{t0, t1}, rW, WS_E, 0, AUX_SC1);
  }
}

}

extern "C" void kernel_launch(void* const* d_in, const int* in_sizes, int n_in, void* d_out, int out_size, void* d_ws, size_t ws_size,
                              hipStream_t stream) {
  (void)in_sizes; (void)n_in; (void)out_size; (void)ws_size;
  (void)hipFuncSetAttribute((const void*)gated_mix_decode_kernel, hipFuncAttributeMaxDynamicSharedMemorySize, LDS_BYTES);
  gated_mix_decode_kernel<<<256, 256, LDS_BYTES, stream>>>(
      (const unsigned short*)d_in[0], (const unsigned short*)d_in[1], (const unsigned short*)d_in[2], (const unsigned short*)d_in[3],
      (const unsigned short*)d_in[4], (const unsigned short*)d_in[6], (const unsigned short*)d_in[7], (const unsigned short*)d_in[8],
      (unsigned short*)d_out, (unsigned char*)d_ws);
}
```

```cpp
#define AMP_K 10
#define AMP_A 2
#define AMP_ID 0
#include <hip/hip_runtime.h>
#include <hip/hip_bf16.h>
#include <cstdint>

namespace {

typedef unsigned v4u __attribute__((ext_vector_type(4)));
typedef unsigned v2u __attribute__((ext_vector_type(2)));
typedef unsigned v16u __attribute__((ext_vector_type(16)));
typedef float f32x4 __attribute__((ext_vector_type(4)));
typedef float f32x2 __attribute__((ext_vector_type(2)));
typedef short s16x4 __attribute__((ext_vector_type(4)));
typedef __bf16 bf16x8 __attribute__((ext_vector_type(8)));
typedef __bf16 bf16x2 __attribute__((ext_vector_type(2)));
typedef __amdgpu_buffer_rsrc_t rsrc_t;

constexpr int NB = 4, DM = 448, HKV = 8, SK = 8192, DH = 256, WIN = 2048, DFF = 1792, NQ = 4096;
constexpr int AUX_NT = 2, AUX_SC1 = 16;
#define WAUX 2
#define KVAUX 2
constexpr int OOB = 0x40000000;
constexpr unsigned SPIN_CAP = 200000;

constexpr int WS_E = 0;
constexpr int WS_Q = 4096;
constexpr int WS_S = WS_Q + 8 * 4 * 16384;
constexpr int WS_O = WS_S + 32 * 1024;
constexpr int WS_F = WS_O + 32 * 8 * 4096;
constexpr int WS_END = WS_F + 4 * 56 * 4096;
constexpr int WS_DIAG = 4 << 20;

constexpr int L_QP = 0;
constexpr int L_Q = 4096;
constexpr int L_P = 5120;
constexpr int L_Z = 6144;
constexpr int L_H = 6400;
constexpr int L_OM = 8448;
constexpr int L_FM = 16640;
constexpr int L_END = 23808;
constexpr int LDS_BYTES = 96 * 1024;
static_assert(L_END <= LDS_BYTES, "lds");

__device__ __forceinline__ rsrc_t mk(const void* p, int bytes) { return __builtin_amdgcn_make_buffer_rsrc((void*)p, 0, bytes, 0x00020000); }
template <int AUX> __device__ __forceinline__ v4u ld16(rsrc_t r, int vo, int so) { return __builtin_amdgcn_raw_buffer_load_b128(r, vo, so, AUX); }
__device__ __forceinline__ void st16_dev(rsrc_t r, v4u v, int vo) { __builtin_amdgcn_raw_buffer_store_b128(v, r, vo, 0, AUX_SC1); }
__device__ __forceinline__ float u2f(unsigned u) { return __builtin_bit_cast(float, u); }
__device__ __forceinline__ unsigned f2u(float f) { return __builtin_bit_cast(unsigned, f); }
__device__ __forceinline__ float dot2(unsigned a, unsigned b, float c) {
  return __builtin_amdgcn_fdot2_f32_bf16(__builtin_bit_cast(bf16x2, a), __builtin_bit_cast(bf16x2, b), c, false);
}
__device__ __forceinline__ unsigned pkbf(float lo, float hi) {
  const bf16x2 v = {(__bf16)lo, (__bf16)hi};
  return __builtin_bit_cast(unsigned, v);
}
__device__ __forceinline__ unsigned perm_lo(unsigned a, unsigned b) { return __builtin_amdgcn_perm(b, a, 0x05040100u); }
__device__ __forceinline__ unsigned perm_hi(unsigned a, unsigned b) { return __builtin_amdgcn_perm(b, a, 0x07060302u); }
template <int CTRL> __device__ __forceinline__ float dpp(float x) {
  return __builtin_bit_cast(float, __builtin_amdgcn_mov_dpp(__builtin_bit_cast(int, x), CTRL, 0xf, 0xf, true));
}
constexpr int XOR1 = 0xB1, XOR2 = 0x4E, XOR7 = 0x141, ROR8 = 0x128, ROR4 = 0x124;
__device__ __forceinline__ float x16_sum(float x) {
  auto s = __builtin_amdgcn_permlane16_swap(f2u(x), f2u(x), false, false);
  return u2f(s[0]) + u2f(s[1]);
}
__device__ __forceinline__ float x32_sum(float x) {
  auto s = __builtin_amdgcn_permlane32_swap(f2u(x), f2u(x), false, false);
  return u2f(s[0]) + u2f(s[1]);
}
__device__ __forceinline__ float x16_max(float x) {
  auto s = __builtin_amdgcn_permlane16_swap(f2u(x), f2u(x), false, false);
  return fmaxf(u2f(s[0]), u2f(s[1]));
}
__device__ __forceinline__ float x32_max(float x) {
  auto s = __builtin_amdgcn_permlane32_swap(f2u(x), f2u(x), false, false);
  return fmaxf(u2f(s[0]), u2f(s[1]));
}
__device__ __forceinline__ float row_sum(float x) { x += dpp<XOR1>(x); x += dpp<XOR2>(x); x += dpp<ROR4>(x); x += dpp<ROR8>(x); return x; }
__device__ __forceinline__ float row_max(float x) {
  x = fmaxf(x, dpp<XOR1>(x)); x = fmaxf(x, dpp<XOR2>(x)); x = fmaxf(x, dpp<ROR4>(x)); x = fmaxf(x, dpp<ROR8>(x)); return x;
}
__device__ __forceinline__ f32x4 mfma32(v4u a, v4u b, f32x4 c) {
  return __builtin_amdgcn_mfma_f32_16x16x32_bf16(__builtin_bit_cast(bf16x8, a), __builtin_bit_cast(bf16x8, b), c, 0, 0, 0);
}
__device__ __forceinline__ f32x4 mfma16(v2u a, v2u b, f32x4 c) {
  return __builtin_amdgcn_mfma_f32_16x16x16bf16_1k(__builtin_bit_cast(s16x4, a), __builtin_bit_cast(s16x4, b), c, 0, 0, 0);
}
__device__ __forceinline__ bool tag_ok(v4u g, unsigned t0, unsigned t1) { return g.y == t0 && g.w == t1; }
__device__ __forceinline__ bool wave_all(bool p) { return __builtin_amdgcn_ballot_w64(!p) == 0; }

#define SB() __builtin_amdgcn_sched_barrier(0)
#define REREAD(off) asm volatile("" : "+v"(off) :: "memory")
#define VMWAIT(n) __builtin_amdgcn_s_waitcnt(((n) & 15) | (7 << 4) | (15 << 8) | ((((n) >> 4) & 3) << 14))
#define THR 7
#define THROTTLE() do { SB(); VMWAIT(THR); SB(); } while (0)
#define LDSBAR() do { __builtin_amdgcn_fence(__ATOMIC_RELEASE, "workgroup", "local"); __builtin_amdgcn_s_barrier(); \
                      __builtin_amdgcn_fence(__ATOMIC_ACQUIRE, "workgroup", "local"); } while (0)

#define STAMP(k) do { if ((k) == 0 || (k) == AMP_K) { SB(); stamp[k] = __builtin_amdgcn_s_memrealtime(); SB(); } } while (0)

#define QG_AT 24
#define SG_AT 20

__global__ void __launch_bounds__(256, 1)
gated_mix_decode_kernel(const unsigned short*, const unsigned short*, const unsigned short*, const unsigned short*, const unsigned short*,
                        const unsigned short*, const unsigned short*, const unsigned short*, unsigned short*, unsigned char*) {
  __shared__ __attribute__((aligned(16))) unsigned char lds[L_END];
  unsigned long long stamp[12];
  STAMP(0);
  v16u ka; v4u kb;
  asm volatile("s_load_dwordx16 %0, %2, 0x0\n\ts_load_dwordx4 %1, %2, 0x40\n\ts_waitcnt lgkmcnt(0)"
               : "=&s"(ka), "=&s"(kb) : "s"(__builtin_amdgcn_kernarg_segment_ptr()) : "memory");
  STAMP(11);
#define KARG(T, lo, hi) ((T)(((unsigned long long)(hi) << 32) | (lo)))
  const unsigned short* x = KARG(const unsigned short*, ka[0], ka[1]);
  const unsigned short* kc = KARG(const unsigned short*, ka[2], ka[3]);
  const unsigned short* vc = KARG(const unsigned short*, ka[4], ka[5]);
  const unsigned short* wq = KARG(const unsigned short*, ka[6], ka[7]);
  const unsigned short* wo = KARG(const unsigned short*, ka[8], ka[9]);
  const unsigned short* w1 = KARG(const unsigned short*, ka[10], ka[11]);
  const unsigned short* w2 = KARG(const unsigned short*, ka[12], ka[13]);
  const unsigned short* gw = KARG(const unsigned short*, ka[14], ka[15]);
  unsigned short* out = KARG(unsigned short*, kb[0], kb[1]);
  unsigned char* ws = KARG(unsigned char*, kb[2], kb[3]);
  const int id = blockIdx.x, kvh = id & 7, b = (id >> 3) & 3, j = id >> 5;
  const int tid = threadIdx.x, lane = tid & 63, w = __builtin_amdgcn_readfirstlane(tid >> 6);
  const int bk = b * HKV + kvh, u = kvh * 8 + j;
  const size_t head = ((size_t)bk * SK + (SK - WIN) + j * 256) * DH;
  const rsrc_t rK = mk(kc + head, 256 * DH * 2), rV = mk(vc + head, 256 * DH * 2);
  const rsrc_t rQ = mk(wq, DM * NQ * 2), rO = mk(wo, NQ * DM * 2), r1 = mk(w1, DM * DFF * 2), r2 = mk(w2, DFF * DM * 2);
  const rsrc_t rX = mk(x, NB * DM * 2), rG = mk(gw, DM * 2), rW = mk(ws, WS_END);

  const v2u ep = __builtin_amdgcn_raw_buffer_load_b64(rW, WS_E, 0, AUX_SC1);
  const int m16 = lane & 15, g4 = lane >> 4;
  v2u xq;
  {
    const int krow = 32 * w + 4 * (2 * g4 + (m16 & 3));
    const bool ok = (m16 & 3) < 2 && krow < 112;
    xq = __builtin_amdgcn_raw_buffer_load_b64(rX, ok ? ((m16 >> 2) * DM + 112 * b + krow) * 2 : OOB, 0, 0);
  }
  v4u wqr[4];
  {
    const int row = 32 * w + 4 * (lane >> 3);
    const int vo = row < 112 ? (112 * b + row) * (NQ * 2) + u * 128 + (lane & 7) * 16 : OOB;
#pragma unroll
    for (int i = 0; i < 4; ++i) wqr[i] = ld16<AUX_NT>(rQ, vo, i * (NQ * 2));
  }
  SB();
  v4u kr[32];
  const int ko = w * 4096 + (lane >> 3) * 512 + (lane & 7) * 16;
#define KLOAD(i) kr[i] = ld16<KVAUX>(rK, ko, ((i) >> 2) * 16384 + ((i) & 3) * 128)
  KLOAD(0); KLOAD(1); KLOAD(2); KLOAD(3);
  SB();
  const int f = kvh * 7 + j;
  v4u xh, xg, gg;
  {
    const int row = 128 * w + 8 * (4 * g4 + (m16 >> 2));
    xh = ld16<0>(rX, ((m16 & 3) == 0 && row < DM) ? (b * DM + row) * 2 : OOB, 0);
    xg = ld16<0>(rX, lane < 56 ? b * DM * 2 + lane * 16 : OOB, 0);
    gg = ld16<0>(rG, lane < 56 ? lane * 16 : OOB, 0);
  }
  const int row1 = 128 * w + 8 * (lane >> 2);
  const int o1 = (j < 7 && row1 < DM) ? row1 * (DFF * 2) + f * 64 + (lane & 3) * 16 : OOB;
  v4u w1r[8];
#define W1LOAD(i) w1r[i] = ld16<WAUX>(r1, o1, (i) * (DFF * 2))
  W1LOAD(0);
  SB();
  KLOAD(4); KLOAD(5); KLOAD(6); KLOAD(7);
  SB();
  if (tid < 16) *(unsigned*)(lds + L_Z + tid * 4) = 0u;
  const unsigned t0 = __builtin_amdgcn_readfirstlane(ep.x) + 1u, t1 = __builtin_amdgcn_readfirstlane(ep.y) + 1u;

  {
    float qv[8];
#pragma unroll
    for (int dw = 0; dw < 4; ++dw) {
      const v2u blo = {perm_lo(wqr[0][dw], wqr[1][dw]), perm_lo(wqr[2][dw], wqr[3][dw])};
      const v2u bhi = {perm_hi(wqr[0][dw], wqr[1][dw]), perm_hi(wqr[2][dw], wqr[3][dw])};
      const f32x4 z = {0.f, 0.f, 0.f, 0.f};
      const f32x4 dl = mfma16(xq, blo, z), dh = mfma16(xq, bhi, z);
      const float sl = (lane & 8) ? dl[1] : dl[0], sh = (lane & 8) ? dh[1] : dh[0];
      qv[2 * dw] = sl + dpp<ROR8>(sl);
      qv[2 * dw + 1] = sh + dpp<ROR8>(sh);
    }
    if ((lane & 8) == 0) {
      f32x4* p = (f32x4*)(lds + L_QP + ((w * 4 + g4) * 64 + 8 * (lane & 7)) * 4);
      p[0] = f32x4{qv[0], qv[1], qv[2], qv[3]};
      p[1] = f32x4{qv[4], qv[5], qv[6], qv[7]};
    }
  }
  STAMP(1);
  W1LOAD(1);
  THROTTLE(); KLOAD(8);
  SB();
  LDSBAR();
  {
    f32x2 s = {0.f, 0.f};
#pragma unroll
    for (int ww = 0; ww < 4; ++ww) s += *(const f32x2*)(lds + L_QP + ((ww * 4 + w) * 64 + 2 * (lane & 31)) * 4);
    const v4u gq = {f2u(s.x), t0, f2u(s.y), t1};
    st16_dev(rW, gq, lane < 32 ? WS_Q + (kvh * 4 + w) * 16384 + j * 2048 + b * 512 + lane * 16 : OOB);
  }
  STAMP(2);

  float gate;
  {
    float d = 0.f;
#pragma unroll
    for (int k = 0; k < 4; ++k) d = dot2(xg[k], gg[k], d);
    d = x32_sum(x16_sum(row_sum(d)));
    gate = 1.f / (1.f + __expf(-d));
  }

  v4u gq4[4];
  int qgo = WS_Q + (kvh * 4 + b) * 16384 + (2 * w + (lane >> 5)) * 2048 + (lane & 31) * 16;
#pragma unroll
  for (int i = 9; i < 32; ++i) {
    THROTTLE(); KLOAD(i);
    if ((i & 3) == 3) { THROTTLE(); W1LOAD(i >> 2); }
    if (i == QG_AT) {
      SB();
#pragma unroll
      for (int k = 0; k < 4; ++k) gq4[k] = ld16<AUX_SC1>(rW, qgo, k * 512);
    }
  }
  v4u vr[32];
  const int vo = w * 4096 + lane * 16;
#define VLOAD(i) vr[i] = ld16<KVAUX>(rV, vo, ((i) >> 2) * 16384 + ((i) & 3) * 1024)
#pragma unroll
  for (int i = 0; i < 4; ++i) { THROTTLE(); VLOAD(i); }
  SB();
  STAMP(3);

  {
    bool ok = tag_ok(gq4[0], t0, t1) && tag_ok(gq4[1], t0, t1) && tag_ok(gq4[2], t0, t1) && tag_ok(gq4[3], t0, t1);
    if (!wave_all(ok)) {
      _Pragma("nounroll") for (unsigned spin = 0; spin < SPIN_CAP; ++spin) {
        REREAD(qgo);
#pragma unroll
        for (int k = 0; k < 4; ++k) gq4[k] = ld16<AUX_SC1>(rW, qgo, k * 512);
        ok = tag_ok(gq4[0], t0, t1) && tag_ok(gq4[1], t0, t1) && tag_ok(gq4[2], t0, t1) && tag_ok(gq4[3], t0, t1);
        if (wave_all(ok)) break;
        __builtin_amdgcn_s_sleep(2);
      }
    }
    const float qa = (u2f(gq4[0].x) + u2f(gq4[1].x)) + (u2f(gq4[2].x) + u2f(gq4[3].x));
    const float qb = (u2f(gq4[0].z) + u2f(gq4[1].z)) + (u2f(gq4[2].z) + u2f(gq4[3].z));
    *(unsigned*)(lds + L_Q + (64 * w + lane) * 4) = pkbf(qa, qb);
  }
#pragma unroll
  for (int i = 4; i < 8; ++i) { THROTTLE(); VLOAD(i); }
  SB();

  {
    float hv[8];
#pragma unroll
    for (int dw = 0; dw < 4; ++dw) {
      const v4u blo = {perm_lo(w1r[0][dw], w1r[1][dw]), perm_lo(w1r[2][dw], w1r[3][dw]), perm_lo(w1r[4][dw], w1r[5][dw]), perm_lo(w1r[6][dw], w1r[7][dw])};
      const v4u bhi = {perm_hi(w1r[0][dw], w1r[1][dw]), perm_hi(w1r[2][dw], w1r[3][dw]), perm_hi(w1r[4][dw], w1r[5][dw]), perm_hi(w1r[6][dw], w1r[7][dw])};
      const f32x4 z = {0.f, 0.f, 0.f, 0.f};
      hv[2 * dw] = mfma32(xh, blo, z)[0];
      hv[2 * dw + 1] = mfma32(xh, bhi, z)[0];
    }
    if ((m16 >> 2) == g4) {
      f32x4* p = (f32x4*)(lds + L_H + ((w * 4 + g4) * 32 + 8 * (lane & 3)) * 4);
      p[0] = f32x4{hv[0], hv[1], hv[2], hv[3]};
      p[1] = f32x4{hv[4], hv[5], hv[6], hv[7]};
    }
  }
  SB();
  LDSBAR();
  STAMP(4);

  float m0, m1;
  {
    v4u qf[2][4];
#pragma unroll
    for (int h = 0; h < 2; ++h)
#pragma unroll
      for (int t = 0; t < 4; ++t) qf[h][t] = *(const v4u*)(lds + L_Q + (h * 256 + 64 * t + 8 * (lane & 7)) * 2);
    float s[8][2];
#pragma unroll
    for (int i = 0; i < 8; ++i) {
      float a0 = 0.f, a1 = 0.f;
#pragma unroll
      for (int t = 0; t < 4; ++t)
#pragma unroll
        for (int dw = 0; dw < 4; ++dw) { a0 = dot2(kr[4 * i + t][dw], qf[0][t][dw], a0); a1 = dot2(kr[4 * i + t][dw], qf[1][t][dw], a1); }
      s[i][0] = a0; s[i][1] = a1;
      if (i & 1) { THROTTLE(); VLOAD(8 + (i >> 1)); SB(); }
    }
#pragma unroll
    for (int i = 0; i < 4; ++i)
#pragma unroll
      for (int h = 0; h < 2; ++h) {
        const float lo = s[i][h] + dpp<XOR7>(s[i][h]), hi = s[i + 4][h] + dpp<XOR7>(s[i + 4][h]);
        s[i][h] = (lane & 4) ? hi : lo;
      }
#pragma unroll
    for (int i = 0; i < 2; ++i)
#pragma unroll
      for (int h = 0; h < 2; ++h) {
        const float lo = s[i][h] + dpp<XOR2>(s[i][h]), hi = s[i + 2][h] + dpp<XOR2>(s[i + 2][h]);
        s[i][h] = (lane & 2) ? hi : lo;
      }
#pragma unroll
    for (int h = 0; h < 2; ++h) {
      const float lo = s[0][h] + dpp<XOR1>(s[0][h]), hi = s[1][h] + dpp<XOR1>(s[1][h]);
      s[0][h] = (lane & 1) ? hi : lo;
    }
    constexpr float C2 = 0.0625f * 1.4426950408889634f;
    const float z0 = s[0][0] * C2, z1 = s[0][1] * C2;
    m0 = x32_max(x16_max(row_max(z0)));
    m1 = x32_max(x16_max(row_max(z1)));
    const float p0 = __builtin_amdgcn_exp2f(z0 - m0), p1 = __builtin_amdgcn_exp2f(z1 - m1);
    const unsigned pp = pkbf(p0, p1);
    unsigned short* pl = (unsigned short*)(lds + L_P + w * 256 + ((lane >> 3) & 1) * 64 + (lane & 7) * 8 + (lane >> 4) * 2);
    pl[0] = (unsigned short)pp;
    pl[64] = (unsigned short)(pp >> 16);
    const float l0 = x32_sum(x16_sum(row_sum(p0))), l1 = x32_sum(x16_sum(row_sum(p1)));
    const v4u gs = {f2u(lane ? m1 : m0), t0, f2u(lane ? l1 : l0), t1};
    st16_dev(rW, gs, lane < 2 ? WS_S + bk * 1024 + lane * 512 + (j * 4 + w) * 16 : OOB);
  }
  STAMP(5);
  v4u pa[4];
  {
    const bool valid = (m16 & 3) == 0 && ((m16 >> 2) & 1) == (g4 & 1);
    const int a = valid ? L_P + w * 256 + (m16 >> 3) * 128 + (g4 >> 1) * 64 : L_Z;
    __builtin_amdgcn_fence(__ATOMIC_ACQ_REL, "wavefront", "local");
#pragma unroll
    for (int G = 0; G < 4; ++G) pa[G] = *(const v4u*)(lds + a + G * 16);
  }
  unsigned hpair;
  {
    f32x2 hs = *(const f32x2*)(lds + L_H + ((lane >> 2) * 32 + 8 * w + 2 * (lane & 3)) * 4);
    float a = hs.x, c = hs.y;
    a += dpp<ROR4>(a); c += dpp<ROR4>(c);
    a += dpp<ROR8>(a); c += dpp<ROR8>(c);
    a = x32_sum(x16_sum(a)); c = x32_sum(x16_sum(c));
    hpair = pkbf(fmaxf(a, 0.f), fmaxf(c, 0.f));
  }

  f32x4 acc[8];
#pragma unroll
  for (int e = 0; e < 8; ++e) acc[e] = f32x4{0.f, 0.f, 0.f, 0.f};
  v4u tr[24];
  const int o2 = (j < 7 && lane < 56) ? (32 * f + 8 * w) * (DM * 2) + lane * 16 : OOB;
  const int oo = lane < 56 ? (u * 64 + 16 * w) * (DM * 2) + lane * 16 : OOB;
#define TLOAD(i) tr[i] = (i) < 8 ? ld16<WAUX>(r2, o2, (i) * (DM * 2)) : ld16<WAUX>(rO, oo, ((i) - 8) * (DM * 2))
  v4u gst;
  int sgo = WS_S + bk * 1024 + lane * 16;
  float oscale = 0.f;
#define PVGROUP(G) do { \
    _Pragma("unroll") for (int dw = 0; dw < 4; ++dw) { \
      const v4u blo = {perm_lo(vr[8 * (G)][dw], vr[8 * (G) + 1][dw]), perm_lo(vr[8 * (G) + 2][dw], vr[8 * (G) + 3][dw]), \
                       perm_lo(vr[8 * (G) + 4][dw], vr[8 * (G) + 5][dw]), perm_lo(vr[8 * (G) + 6][dw], vr[8 * (G) + 7][dw])}; \
      const v4u bhi = {perm_hi(vr[8 * (G)][dw], vr[8 * (G) + 1][dw]), perm_hi(vr[8 * (G) + 2][dw], vr[8 * (G) + 3][dw]), \
                       perm_hi(vr[8 * (G) + 4][dw], vr[8 * (G) + 5][dw]), perm_hi(vr[8 * (G) + 6][dw], vr[8 * (G) + 7][dw])}; \
      acc[2 * dw] = mfma32(pa[G], blo, acc[2 * dw]); \
      acc[2 * dw + 1] = mfma32(pa[G], bhi, acc[2 * dw + 1]); } } while (0)
#pragma unroll
  for (int i = 12; i < 40; ++i) {
    THROTTLE();
    if (i < 32) VLOAD(i); else TLOAD(i - 32);
    SB();
    if (i == SG_AT) { gst = ld16<AUX_SC1>(rW, sgo, 0); SB(); }
    if (i == 15) { PVGROUP(0); SB(); }
    if (i == 23) { PVGROUP(1); SB(); }
    if (i == 31) { PVGROUP(2); SB(); }
    if (i == 33) {
      if (!wave_all(tag_ok(gst, t0, t1))) {
        _Pragma("nounroll") for (unsigned spin = 0; spin < SPIN_CAP; ++spin) {
        REREAD(sgo);
          gst = ld16<AUX_SC1>(rW, sgo, 0);
          if (wave_all(tag_ok(gst, t0, t1))) break;
          __builtin_amdgcn_s_sleep(2);
        }
      }
      const float me = u2f(gst.x), M = x16_max(row_max(me));
      const float L = x16_sum(row_sum(u2f(gst.z) * __builtin_amdgcn_exp2f(me - M)));
      oscale = __builtin_amdgcn_exp2f((lane < 32 ? m0 : m1) - M) / L;
      SB();
    }
  }
  PVGROUP(3);
  STAMP(6);
  {
    f32x4* p = (f32x4*)(lds + L_OM + (w * 512 + 8 * lane) * 4);
    p[0] = f32x4{acc[0][0] * oscale, acc[1][0] * oscale, acc[2][0] * oscale, acc[3][0] * oscale};
    p[1] = f32x4{acc[4][0] * oscale, acc[5][0] * oscale, acc[6][0] * oscale, acc[7][0] * oscale};
  }
  SB();
  LDSBAR();
  {
    f32x2 s = {0.f, 0.f};
#pragma unroll
    for (int ww = 0; ww < 4; ++ww) s += *(const f32x2*)(lds + L_OM + (ww * 512 + 2 * tid) * 4);
    const v4u go = {f2u(s.x), t0, f2u(s.y), t1};
    st16_dev(rW, go, WS_O + (bk * 8 + (tid >> 5)) * 4096 + ((tid >> 3) & 3) * 1024 + j * 128 + (tid & 7) * 16);
  }
  SB();
  STAMP(7);
#pragma unroll
  for (int i = 8; i < 24; ++i) TLOAD(i);
  SB();
  int ogo = WS_O + (bk * 8 + j) * 4096 + w * 1024 + lane * 16;
  v4u go = ld16<AUX_SC1>(rW, ogo, 0);
  SB();

  float af[8], ao[8];
#pragma unroll
  for (int e = 0; e < 8; ++e) { af[e] = 0.f; ao[e] = 0.f; }
#pragma unroll
  for (int pr = 0; pr < 4; ++pr) {
    const unsigned hp = __builtin_amdgcn_readlane(hpair, pr);
#pragma unroll
    for (int dw = 0; dw < 4; ++dw) {
      af[2 * dw] = dot2(perm_lo(tr[2 * pr][dw], tr[2 * pr + 1][dw]), hp, af[2 * dw]);
      af[2 * dw + 1] = dot2(perm_hi(tr[2 * pr][dw], tr[2 * pr + 1][dw]), hp, af[2 * dw + 1]);
    }
  }
  unsigned wp[8][8];
#pragma unroll
  for (int pr = 0; pr < 8; ++pr)
#pragma unroll
    for (int dw = 0; dw < 4; ++dw) {
      wp[pr][2 * dw] = perm_lo(tr[8 + 2 * pr][dw], tr[9 + 2 * pr][dw]);
      wp[pr][2 * dw + 1] = perm_hi(tr[8 + 2 * pr][dw], tr[9 + 2 * pr][dw]);
    }
#pragma unroll
  for (int pr = 0; pr < 8; ++pr)
#pragma unroll
    for (int e = 0; e < 8; ++e) asm volatile("" : "+v"(wp[pr][e]));
#pragma unroll
  for (int e = 0; e < 8; ++e) { af[e] *= (1.f - gate); asm volatile("" : "+v"(af[e])); }
  SB();
  if (!wave_all(tag_ok(go, t0, t1))) {
    _Pragma("nounroll") for (unsigned spin = 0; spin < SPIN_CAP; ++spin) {
        REREAD(ogo);
      go = ld16<AUX_SC1>(rW, ogo, 0);
      if (wave_all(tag_ok(go, t0, t1))) break;
    }
  }
  STAMP(8);
  {
    float a = u2f(go.x), c = u2f(go.z);
    a += dpp<ROR8>(a); c += dpp<ROR8>(c);
    a = x32_sum(x16_sum(a)); c = x32_sum(x16_sum(c));
    const unsigned op = pkbf(a, c);
#pragma unroll
    for (int pr = 0; pr < 8; ++pr) {
      const unsigned so = __builtin_amdgcn_readlane(op, pr);
#pragma unroll
      for (int e = 0; e < 8; ++e) ao[e] = dot2(wp[pr][e], so, ao[e]);
    }
    if (lane < 56) {
      f32x4* p = (f32x4*)(lds + L_FM + (w * DM + 8 * lane) * 4);
      p[0] = f32x4{fmaf(gate, ao[0], af[0]), fmaf(gate, ao[1], af[1]), fmaf(gate, ao[2], af[2]), fmaf(gate, ao[3], af[3])};
      p[1] = f32x4{fmaf(gate, ao[4], af[4]), fmaf(gate, ao[5], af[5]), fmaf(gate, ao[6], af[6]), fmaf(gate, ao[7], af[7])};
    }
  }
  SB();
  LDSBAR();
  {
    const int t = tid < 224 ? tid : 0;
    f32x2 s = {0.f, 0.f};
#pragma unroll
    for (int ww = 0; ww < 4; ++ww) s += *(const f32x2*)(lds + L_FM + (ww * DM + 2 * t) * 4);
    const v4u gf = {f2u(s.x), t0, f2u(s.y), t1};
    st16_dev(rW, gf, tid < 224 ? WS_F + (b * 56 + (tid >> 2)) * 4096 + u * 64 + (tid & 3) * 16 : OOB);
  }
  STAMP(9);
  if (w == 0 && u < 56) {
    int fgo = WS_F + (b * 56 + u) * 4096 + lane * 16;
    v4u gf[4];
    _Pragma("nounroll") for (unsigned spin = 0; spin < SPIN_CAP; ++spin) {
        REREAD(fgo);
#pragma unroll
      for (int k = 0; k < 4; ++k) gf[k] = ld16<AUX_SC1>(rW, fgo, k * 1024);
      const bool ok = tag_ok(gf[0], t0, t1) && tag_ok(gf[1], t0, t1) && tag_ok(gf[2], t0, t1) && tag_ok(gf[3], t0, t1);
      if (wave_all(ok)) break;
    }
    float a = (u2f(gf[0].x) + u2f(gf[1].x)) + (u2f(gf[2].x) + u2f(gf[3].x));
    float c = (u2f(gf[0].z) + u2f(gf[1].z)) + (u2f(gf[2].z) + u2f(gf[3].z));
    a += dpp<ROR4>(a); c += dpp<ROR4>(c);
    a += dpp<ROR8>(a); c += dpp<ROR8>(c);
    a = x32_sum(x16_sum(a)); c = x32_sum(x16_sum(c));
    __builtin_amdgcn_raw_buffer_store_b32(pkbf(a, c), mk(out, NB * DM * 2), lane < 4 ? (b * DM + 8 * u + 2 * lane) * 2 : OOB, 0, 0);
    if (id == 0 && lane == 0) __builtin_amdgcn_raw_buffer_store_b64(v2u{t0, t1}, rW, WS_E, 0, AUX_SC1);
  }
  STAMP(10);
  if (true) {
    const unsigned long long now = __builtin_amdgcn_s_memrealtime();
    const unsigned long long until = stamp[0] + 3000;
    while (__builtin_amdgcn_s_memrealtime() < until) __builtin_amdgcn_s_sleep(1);
  }
}

}

extern "C" void kernel_launch(void* const* d_in, const int* in_sizes, int n_in, void* d_out, int out_size, void* d_ws, size_t ws_size,
                              hipStream_t stream) {
  (void)in_sizes; (void)n_in; (void)out_size; (void)ws_size;

  gated_mix_decode_kernel<<<256, 256, 0, stream>>>(
      (const unsigned short*)d_in[0], (const unsigned short*)d_in[1], (const unsigned short*)d_in[2], (const unsigned short*)d_in[3],
      (const unsigned short*)d_in[4], (const unsigned short*)d_in[6], (const unsigned short*)d_in[7], (const unsigned short*)d_in[8],
      (unsigned short*)d_out, (unsigned char*)d_ws);
}
```

```cpp
#include <hip/hip_runtime.h>
#include <hip/hip_bf16.h>
#include <cstdint>

namespace {

typedef unsigned v4u __attribute__((ext_vector_type(4)));
typedef unsigned v2u __attribute__((ext_vector_type(2)));
typedef unsigned v16u __attribute__((ext_vector_type(16)));
typedef float f32x4 __attribute__((ext_vector_type(4)));
typedef float f32x2 __attribute__((ext_vector_type(2)));
typedef short s16x4 __attribute__((ext_vector_type(4)));
typedef __bf16 bf16x8 __attribute__((ext_vector_type(8)));
typedef __bf16 bf16x2 __attribute__((ext_vector_type(2)));
typedef __amdgpu_buffer_rsrc_t rsrc_t;
typedef __attribute__((address_space(3))) unsigned lds_u32;

constexpr int NB = 4, DM = 448, HKV = 8, SK = 8192, DH = 256, WIN = 2048, DFF = 1792, NQ = 4096;
constexpr int AUX_SC1 = 16;
constexpr int OOB = 0x40000000;
constexpr unsigned SPIN_CAP = 200000, TAG_MASK = 1023u;

#define WAUX 2
#define KVAUX 2
#define THR 7
#define QG_AT 25
#define VB2 6
#define KPRE 2
#define CODEPF 0
#define CODE_BYTES 11264
#define ILV 1
#define TPRE 0
#define PREWARM 3
#define PSLEEP 0
#define ABL 0

constexpr int WS_E = 0;
constexpr int WS_Q = 4096;
constexpr int WS_H = WS_Q + 8 * 4 * 8192;
constexpr int WS_O = WS_H + 28 * 4 * 2048;
constexpr int WS_F = WS_O + 16 * 16 * 4096;
constexpr int WS_END = WS_F + 2 * 28 * 16384;
constexpr int WS_DIAG = 8 << 20;

constexpr int OM_ROW = 68 * 4, FM_ROW = 544;
constexpr int L_QP = 0;
constexpr int L_Q = 8192;
constexpr int L_P = 9216;
constexpr int L_Z = 10240;
constexpr int L_CNT = 10304;
constexpr int L_TAG = 10320;
constexpr int L_HP = 10368;
constexpr int L_ST = 10432;
constexpr int L_OM = 10496;
constexpr int L_FM = L_OM + 32 * OM_ROW;
constexpr int L_END = L_FM + 32 * FM_ROW;

__device__ __forceinline__ rsrc_t mk(const void* p, int bytes) { return __builtin_amdgcn_make_buffer_rsrc((void*)p, 0, bytes, 0x00020000); }
template <int AUX> __device__ __forceinline__ v4u ld16(rsrc_t r, int vo, int so) { return __builtin_amdgcn_raw_buffer_load_b128(r, vo, so, AUX); }
__device__ __forceinline__ int oob_if(bool c) { return c ? OOB : 0; }
__device__ __forceinline__ float u2f(unsigned u) { return __builtin_bit_cast(float, u); }
__device__ __forceinline__ unsigned f2u(float f) { return __builtin_bit_cast(unsigned, f); }
__device__ __forceinline__ unsigned tagged(float v, unsigned t) { return (f2u(v) & ~TAG_MASK) | t; }
__device__ __forceinline__ void publish(rsrc_t r, int vo, float a, float b, float c, float d, unsigned t) {
  __builtin_amdgcn_raw_buffer_store_b128(v4u{tagged(a, t), tagged(b, t), tagged(c, t), tagged(d, t)}, r, vo, 0, AUX_SC1);
}
__device__ __forceinline__ unsigned untag(v4u& g, unsigned t) { g ^= t; return (g.x | g.y | g.z | g.w) & TAG_MASK; }
__device__ __forceinline__ bool wave_none(bool p) { return (ABL & 1) ? true : __builtin_amdgcn_ballot_w64(p) == 0; }
__device__ __forceinline__ float dot2(unsigned a, unsigned b, float c) {
  return __builtin_amdgcn_fdot2_f32_bf16(__builtin_bit_cast(bf16x2, a), __builtin_bit_cast(bf16x2, b), c, false);
}
__device__ __forceinline__ unsigned pkbf(float lo, float hi) {
  const bf16x2 v = {(__bf16)lo, (__bf16)hi};
  return __builtin_bit_cast(unsigned, v);
}
__device__ __forceinline__ unsigned perm_lo(unsigned a, unsigned b) { return __builtin_amdgcn_perm(b, a, 0x05040100u); }
__device__ __forceinline__ unsigned perm_hi(unsigned a, unsigned b) { return __builtin_amdgcn_perm(b, a, 0x07060302u); }
template <int CTRL> __device__ __forceinline__ unsigned dppu(unsigned x) { return (unsigned)__builtin_amdgcn_mov_dpp((int)x, CTRL, 0xf, 0xf, true); }
template <int CTRL> __device__ __forceinline__ float dpp(float x) { return u2f(dppu<CTRL>(f2u(x))); }
constexpr int XOR1 = 0xB1, XOR2 = 0x4E, XOR7 = 0x141, ROR8 = 0x128, ROR4 = 0x124;
__device__ __forceinline__ float fmx(float a, float c) { return __builtin_amdgcn_fmed3f(a, c, __builtin_inff()); }
__device__ __forceinline__ float fold16(float a, float b) {
  auto s = __builtin_amdgcn_permlane16_swap(f2u(a), f2u(b), false, false);
  return u2f(s[0]) + u2f(s[1]);
}
__device__ __forceinline__ float fold32(float a, float b) {
  auto s = __builtin_amdgcn_permlane32_swap(f2u(a), f2u(b), false, false);
  return u2f(s[0]) + u2f(s[1]);
}
__device__ __forceinline__ float x16_sum(float x) { return fold16(x, x); }
__device__ __forceinline__ float x32_sum(float x) { return fold32(x, x); }
__device__ __forceinline__ float x16_max(float x) {
  auto s = __builtin_amdgcn_permlane16_swap(f2u(x), f2u(x), false, false);
  return fmx(u2f(s[0]), u2f(s[1]));
}
__device__ __forceinline__ float x32_max(float x) {
  auto s = __builtin_amdgcn_permlane32_swap(f2u(x), f2u(x), false, false);
  return fmx(u2f(s[0]), u2f(s[1]));
}
__device__ __forceinline__ float row_sum(float x) { x += dpp<XOR1>(x); x += dpp<XOR2>(x); x += dpp<ROR4>(x); x += dpp<ROR8>(x); return x; }
__device__ __forceinline__ float row_max(float x) {
  x = fmx(x, dpp<XOR1>(x)); x = fmx(x, dpp<XOR2>(x)); x = fmx(x, dpp<ROR4>(x)); x = fmx(x, dpp<ROR8>(x)); return x;
}
__device__ __forceinline__ f32x4 mfma32(v4u a, v4u b, f32x4 c) {
  return __builtin_amdgcn_mfma_f32_16x16x32_bf16(__builtin_bit_cast(bf16x8, a), __builtin_bit_cast(bf16x8, b), c, 0, 0, 0);
}
__device__ __forceinline__ f32x4 mfma16(v2u a, v2u b, f32x4 c) {
  return __builtin_amdgcn_mfma_f32_16x16x16bf16_1k(__builtin_bit_cast(s16x4, a), __builtin_bit_cast(s16x4, b), c, 0, 0, 0);
}

#define SB() __builtin_amdgcn_sched_barrier(0)
#define REREAD(off) asm volatile("" : "+v"(off) :: "memory")
#define VMWAIT(n) __builtin_amdgcn_s_waitcnt(((n) & 15) | (7 << 4) | (15 << 8) | ((((n) >> 4) & 3) << 14))
#define THROTTLE() do { SB(); VMWAIT(THR); SB(); } while (0)
#define LDSBAR() do { SB(); __builtin_amdgcn_fence(__ATOMIC_RELEASE, "workgroup", "local"); __builtin_amdgcn_s_barrier(); \
                      __builtin_amdgcn_fence(__ATOMIC_ACQUIRE, "workgroup", "local"); SB(); } while (0)

__device__ __forceinline__ void lds_arrive(unsigned char* lds, int off, int lane) {
  __builtin_amdgcn_fence(__ATOMIC_RELEASE, "workgroup", "local");
  if (lane == 0) __hip_atomic_fetch_add((lds_u32*)(lds + off), 1u, __ATOMIC_RELAXED, __HIP_MEMORY_SCOPE_WORKGROUP);
}
__device__ __forceinline__ void lds_wait(unsigned char* lds, int off, unsigned want) {
  _Pragma("nounroll") for (unsigned spin = 0; spin < 16u * SPIN_CAP; ++spin) {
    if ((unsigned)__builtin_amdgcn_readfirstlane(__hip_atomic_load((lds_u32*)(lds + off), __ATOMIC_RELAXED, __HIP_MEMORY_SCOPE_WORKGROUP)) >= want) break;
    __builtin_amdgcn_s_sleep(1);
  }
  __builtin_amdgcn_fence(__ATOMIC_ACQUIRE, "workgroup", "local");
}

#define STAMP(k) do { } while (0)

__global__ void __launch_bounds__(512, 2)
gated_mix_decode_kernel(const unsigned short*, const unsigned short*, const unsigned short*, const unsigned short*, const unsigned short*,
                        const unsigned short*, const unsigned short*, const unsigned short*, unsigned short*, unsigned char*) {
  __shared__ __attribute__((aligned(128))) unsigned char lds[L_END];
  STAMP(0);
  v16u ka; v4u kb;
  asm volatile("s_load_dwordx16 %0, %2, 0x0\n\ts_load_dwordx4 %1, %2, 0x40\n\ts_waitcnt lgkmcnt(0)"
               : "=&s"(ka), "=&s"(kb) : "s"(__builtin_amdgcn_kernarg_segment_ptr()) : "memory");
#define KARG(T, lo, hi) ((T)(((unsigned long long)(hi) << 32) | (lo)))
  const unsigned short* x = KARG(const unsigned short*, ka[0], ka[1]);
  const unsigned short* kc = KARG(const unsigned short*, ka[2], ka[3]);
  const unsigned short* vc = KARG(const unsigned short*, ka[4], ka[5]);
  const unsigned short* wq = KARG(const unsigned short*, ka[6], ka[7]);
  const unsigned short* wo = KARG(const unsigned short*, ka[8], ka[9]);
  const unsigned short* w1 = KARG(const unsigned short*, ka[10], ka[11]);
  const unsigned short* w2 = KARG(const unsigned short*, ka[12], ka[13]);
  const unsigned short* gw = KARG(const unsigned short*, ka[14], ka[15]);
  unsigned short* out = KARG(unsigned short*, kb[0], kb[1]);
  unsigned char* ws = KARG(unsigned char*, kb[2], kb[3]);
  const int id = blockIdx.x, kvh = id & 7, b = (id >> 3) & 3, j = id >> 5;
  const int lane = threadIdx.x & 63, wv = __builtin_amdgcn_readfirstlane(threadIdx.x >> 6), w = wv & 3;
  const int bp = b >> 1, r16 = (b & 1) * 8 + j, pu = kvh * 16 + r16, fs = kvh * 14 + r16;
  const int m16 = lane & 15, g4 = lane >> 4;
  const rsrc_t rW = mk(ws, WS_END);
  const f32x4 zero4 = {0.f, 0.f, 0.f, 0.f};

  if (wv < 4) {
    const int tid = threadIdx.x;
    const rsrc_t rQ = mk(wq, DM * NQ * 2), r1 = mk(w1, DM * DFF * 2), rX = mk(x, NB * DM * 2), rG = mk(gw, DM * 2);
    v4u wqr[4], w1r[2];
    v2u xq, xh;
    {
      const int row = 32 * w + 4 * (lane >> 3);
      const int vo = ((112 * b + row) * (NQ * 2) + (kvh * 8 + j) * 128 + (lane & 7) * 16) | oob_if(row >= 112);
#pragma unroll
      for (int i = 0; i < 4; ++i) wqr[i] = ld16<WAUX>(rQ, vo, i * (NQ * 2));
      SB();
      const int rr = 2 * g4 + (m16 & 3), xrow = (m16 >> 2) * DM;
      const bool off = (m16 & 2) != 0;
      xq = __builtin_amdgcn_raw_buffer_load_b64(rX, ((xrow + 112 * b + 32 * w + 4 * rr) * 2) | oob_if(off | (32 * w + 4 * rr >= 112)), 0, 0);
      xh.x = __builtin_amdgcn_raw_buffer_load_b32(rX, ((xrow + 56 * kvh + 16 * w + 2 * rr) * 2) | oob_if(off | (16 * w + 2 * rr >= 56)), 0, 0);
      xh.y = 0u;
      SB();
      const int row1 = 16 * w + 2 * (lane >> 3);
      const int o1 = ((56 * kvh + row1) * (DFF * 2) + (4 * j + b) * 128 + (lane & 7) * 16) | oob_if((j >= 7) | (row1 >= 56));
#pragma unroll
      for (int i = 0; i < 2; ++i) w1r[i] = ld16<WAUX>(r1, o1, i * (DFF * 2));
    }
    const unsigned ep = __builtin_amdgcn_raw_buffer_load_b32(rW, WS_E, 0, AUX_SC1);
    SB();
    if (tid < 20) *(unsigned*)(lds + L_Z + tid * 4) = 0u;
    LDSBAR();
    {
      float qv[8], hv[8];
#pragma unroll
      for (int dw = 0; dw < 4; ++dw) {
        const v2u blo = {perm_lo(wqr[0][dw], wqr[1][dw]), perm_lo(wqr[2][dw], wqr[3][dw])};
        const v2u bhi = {perm_hi(wqr[0][dw], wqr[1][dw]), perm_hi(wqr[2][dw], wqr[3][dw])};
        const f32x4 dl = mfma16(xq, blo, zero4), dh = mfma16(xq, bhi, zero4);
        const float sl = (lane & 8) ? dl[1] : dl[0], sh = (lane & 8) ? dh[1] : dh[0];
        qv[2 * dw] = sl + dpp<ROR8>(sl);
        qv[2 * dw + 1] = sh + dpp<ROR8>(sh);
      }
#pragma unroll
      for (int dw = 0; dw < 4; ++dw) {
        const v2u blo = {perm_lo(w1r[0][dw], w1r[1][dw]), 0u}, bhi = {perm_hi(w1r[0][dw], w1r[1][dw]), 0u};
        const f32x4 dl = mfma16(xh, blo, zero4), dh = mfma16(xh, bhi, zero4);
        const float sl = (lane & 8) ? dl[1] : dl[0], sh = (lane & 8) ? dh[1] : dh[0];
        hv[2 * dw] = sl + dpp<ROR8>(sl);
        hv[2 * dw + 1] = sh + dpp<ROR8>(sh);
      }
      const bool up = (lane & 8) != 0;
      f32x4* p = (f32x4*)(lds + L_QP + ((w * 4 + g4) * 128 + 8 * m16) * 4);
      p[0] = up ? f32x4{hv[0], hv[1], hv[2], hv[3]} : f32x4{qv[0], qv[1], qv[2], qv[3]};
      p[1] = up ? f32x4{hv[4], hv[5], hv[6], hv[7]} : f32x4{qv[4], qv[5], qv[6], qv[7]};
    }
    lds_arrive(lds, L_CNT, lane);
    const unsigned t = ((unsigned)__builtin_amdgcn_readfirstlane(ep) + 1u) & TAG_MASK;
    STAMP(1);
    lds_wait(lds, L_CNT, 4);
    {
      f32x4 s = zero4;
#pragma unroll
      for (int ww = 0; ww < 4; ++ww) s += *(const f32x4*)(lds + L_QP + ((ww * 4 + w) * 128 + 4 * (lane & 31)) * 4);
      const int dst = lane < 16 ? WS_Q + (kvh * 4 + w) * 8192 + j * 1024 + b * 256 + lane * 16
                    : (lane < 32 && j < 7) ? WS_H + ((4 * j + b) * 4 + w) * 2048 + kvh * 256 + m16 * 16 : OOB;
      publish(rW, (ABL & 8) ? OOB : dst, s[0], s[1], s[2], s[3], t);
    }
    SB();
    STAMP(2);
    STAMP(3);
    lds_wait(lds, L_CNT + 8, 1);
    {
      const int go_ = (lane * 16) | oob_if((lane >= 56) | (w != 3));
      const v4u xg0 = ld16<0>(rX, go_, 2 * bp * DM * 2), xg1 = ld16<0>(rX, go_, (2 * bp + 1) * DM * 2), gg = ld16<0>(rG, go_, 0);
      SB();
      int qgo = (ABL & 2) ? OOB : WS_Q + (kvh * 4 + b) * 8192 + (2 * w + (lane >> 5)) * 1024 + (g4 & 1) * 256 + m16 * 16;
      const bool hon = w == 3 && r16 < 14;
      int hgo = (hon && !(ABL & 2)) ? WS_H + ((fs >> 2) * 4 + 2 * bp + (lane >> 5)) * 2048 + ((lane >> 2) & 7) * 256 + (fs & 3) * 64 + (lane & 3) * 16 : OOB;
      v4u g0, g1, gh;
      _Pragma("nounroll") for (unsigned spin = 0; spin < SPIN_CAP; ++spin) {
        REREAD(qgo); REREAD(hgo);
        g0 = ld16<AUX_SC1>(rW, qgo, 0);
        g1 = ld16<AUX_SC1>(rW, qgo, 512);
        gh = ld16<AUX_SC1>(rW, hgo, 0);
        const unsigned mq = untag(g0, t) | untag(g1, t), mh = untag(gh, t);
        if (wave_none((mq | (hon ? mh : 0u)) != 0u)) break;
        __builtin_amdgcn_s_sleep(8);
      }
      float q[4];
#pragma unroll
      for (int k = 0; k < 4; ++k) q[k] = x16_sum(u2f(g0[k]) + u2f(g1[k]));
      if ((g4 & 1) == 0) *(v2u*)(lds + L_Q + ((2 * w + (lane >> 5)) * 64 + 4 * m16) * 2) = v2u{pkbf(q[0], q[1]), pkbf(q[2], q[3])};
      if (w == 3) {
        float d0 = 0.f, d1 = 0.f;
#pragma unroll
        for (int k = 0; k < 4; ++k) { d0 = dot2(xg0[k], gg[k], d0); d1 = dot2(xg1[k], gg[k], d1); }
        const float gate = 1.f / (1.f + __expf(-x16_sum(row_sum(fold32(d0, d1)))));
        if ((lane & 31) == 0) *(float*)(lds + L_TAG + 4 + 4 * (lane >> 5)) = gate;
        if (lane == 1) *(unsigned*)(lds + L_TAG) = t;
        float h[4];
#pragma unroll
        for (int k = 0; k < 4; ++k) {
          float a = hon ? u2f(gh[k]) : 0.f;
          a += dpp<ROR4>(a);
          a += dpp<ROR8>(a);
          h[k] = fmaxf(x16_sum(a), 0.f) * (1.f - gate);
        }
        if ((lane & 28) == 0) *(v2u*)(lds + L_HP + (lane >> 5) * 32 + (lane & 3) * 8) = v2u{pkbf(h[0], h[1]), pkbf(h[2], h[3])};
      }
    }
    STAMP(4);
    LDSBAR();
    if (PREWARM) {
      const int po = WS_O + ((bp * 8 + kvh) * 16 + r16) * 4096 + (lane & 31) * 128;
      const int pf = WS_F + (bp * 28 + (pu >> 2)) * 16384 + (w * 32 + (lane & 31)) * 128;
      const unsigned pw = __builtin_amdgcn_raw_buffer_load_b32(rW, lane < 32 ? (w == 0 ? po : OOB) : (((PREWARM & 2) && pu < 112) ? pf : OOB), 0, AUX_SC1);
      asm volatile("" :: "v"(pw));
    }
    lds_wait(lds, L_CNT + 4, 4);
    float lse;
    f32x4 fc;
    {
      const int h = tid >> 7;
      const f32x4 e0 = *(const f32x4*)(lds + L_ST + h * 32), e1 = *(const f32x4*)(lds + L_ST + h * 32 + 16);
      const float M = fmx(fmx(e0[0], e0[2]), fmx(e1[0], e1[2]));
      fc = f32x4{__builtin_amdgcn_exp2f(e0[0] - M), __builtin_amdgcn_exp2f(e0[2] - M), __builtin_amdgcn_exp2f(e1[0] - M), __builtin_amdgcn_exp2f(e1[2] - M)};
      const float L = (fc[0] * e0[1] + fc[1] * e0[3]) + (fc[2] * e1[1] + fc[3] * e1[3]);
      fc *= 1.f / L;
      lse = M + __builtin_amdgcn_logf(L);
    }
    float lse_hi = u2f(f2u(lse) & ~TAG_MASK), lse_lo = lse - lse_hi;
    int oa = L_OM + 2 * (tid & 3) * OM_ROW + (tid >> 2) * 4, fa = L_FM + 2 * (tid & 3) * FM_ROW + (tid >> 2) * 8;
    int odst = (ABL & 128) ? OOB : WS_O + ((bp * 8 + kvh) * 32 + (tid >> 3)) * 2048 + r16 * 128 + (tid & 7) * 16;
    int fdst = tid < 224 ? WS_F + (bp * 28 + (tid >> 3)) * 16384 + pu * 128 + (tid & 7) * 16 : OOB;
    asm volatile("" : "+v"(lse_hi), "+v"(lse_lo), "+v"(fc), "+v"(oa), "+v"(fa), "+v"(odst), "+v"(fdst));
    STAMP(6);
    LDSBAR();
    {
      float a = 0.f, c = 0.f;
#pragma unroll
      for (int ww = 0; ww < 4; ++ww) {
        a = fmaf(fc[ww], *(const float*)(lds + oa + ww * 8 * OM_ROW), a);
        c = fmaf(fc[ww], *(const float*)(lds + oa + ww * 8 * OM_ROW + OM_ROW), c);
      }
      publish(rW, odst, a, c, lse_hi, lse_lo, t);
    }
    STAMP(7);
    LDSBAR();
    {
      f32x2 s0 = {0.f, 0.f}, s1 = {0.f, 0.f};
#pragma unroll
      for (int ww = 0; ww < 4; ++ww) {
        s0 += *(const f32x2*)(lds + fa + ww * 8 * FM_ROW);
        s1 += *(const f32x2*)(lds + fa + ww * 8 * FM_ROW + FM_ROW);
      }
      publish(rW, fdst, s0.x, s0.y, s1.x, s1.y, t);
      SB();
      lds_arrive(lds, L_CNT + 12, lane);
    }
    STAMP(9);
  } else {
    const size_t head = ((size_t)(b * HKV + kvh) * SK + (SK - WIN)) * DH;
    const rsrc_t rK = mk(kc + head, WIN * DH * 2), rV = mk(vc + head, WIN * DH * 2);
    constexpr int BSTR = ILV ? 131072 : 16384;
    const int blk0 = (ILV ? j * 16384 : j * 131072) + w * 4096;
    v4u kr[32], vr[32];
    const int ko = blk0 + (lane >> 3) * 512 + (lane & 7) * 16;
#define KLOAD(i) kr[i] = ld16<KVAUX>(rK, ko, ((i) >> 2) * BSTR + ((i) & 3) * 128)
#pragma unroll
    for (int i = 0; i < KPRE; ++i) KLOAD(i);
    LDSBAR();
#pragma unroll
    for (int i = KPRE; i < 32; ++i) { THROTTLE(); KLOAD(i); if (i == QG_AT && w == 0) lds_arrive(lds, L_CNT + 8, lane); }
    const int vo = blk0 + lane * 16;
#define VLOAD(i) vr[i] = ld16<KVAUX>(rV, vo, ((i) >> 2) * BSTR + ((i) & 3) * 1024)
#define VSTEP(i) do { THROTTLE(); VLOAD(i); SB(); } while (0)
    STAMP(3);
#pragma unroll
    for (int i = 0; i < VB2; ++i) VSTEP(i);
    LDSBAR();
    STAMP(4);
    v4u pa[4];
    {
      v4u qf[2][4];
#pragma unroll
      for (int h = 0; h < 2; ++h)
#pragma unroll
        for (int t = 0; t < 4; ++t) qf[h][t] = *(const v4u*)(lds + L_Q + (h * 256 + 64 * t + 8 * (lane & 7)) * 2);
      VSTEP(VB2);
      float s[8][2];
#pragma unroll
      for (int i = 0; i < 8; i += 2) {
        float a0 = 0.f, a1 = 0.f, c0 = 0.f, c1 = 0.f;
#pragma unroll
        for (int t = 0; t < 4; ++t) {
#pragma unroll
          for (int dw = 0; dw < 4; ++dw) {
            a0 = dot2(kr[4 * i + t][dw], qf[0][t][dw], a0); c0 = dot2(kr[4 * i + 4 + t][dw], qf[0][t][dw], c0);
            a1 = dot2(kr[4 * i + t][dw], qf[1][t][dw], a1); c1 = dot2(kr[4 * i + 4 + t][dw], qf[1][t][dw], c1);
          }
          if (t == 1) VSTEP(VB2 + 1 + i);
        }
        s[i][0] = a0; s[i][1] = a1; s[i + 1][0] = c0; s[i + 1][1] = c1;
        VSTEP(VB2 + 2 + i);
      }
#pragma unroll
      for (int i = 0; i < 4; ++i)
#pragma unroll
        for (int h = 0; h < 2; ++h) {
          const float lo = s[i][h] + dpp<XOR7>(s[i][h]), hi = s[i + 4][h] + dpp<XOR7>(s[i + 4][h]);
          s[i][h] = (lane & 4) ? hi : lo;
        }
#pragma unroll
      for (int i = 0; i < 2; ++i)
#pragma unroll
        for (int h = 0; h < 2; ++h) {
          const float lo = s[i][h] + dpp<XOR2>(s[i][h]), hi = s[i + 2][h] + dpp<XOR2>(s[i + 2][h]);
          s[i][h] = (lane & 2) ? hi : lo;
        }
#pragma unroll
      for (int h = 0; h < 2; ++h) {
        const float lo = s[0][h] + dpp<XOR1>(s[0][h]), hi = s[1][h] + dpp<XOR1>(s[1][h]);
        s[0][h] = (lane & 1) ? hi : lo;
      }
      VSTEP(VB2 + 9);
      constexpr float C2 = 0.0625f * 1.4426950408889634f;
      const float z0 = s[0][0] * C2, z1 = s[0][1] * C2;
      const float m0 = x32_max(x16_max(row_max(z0))), m1 = x32_max(x16_max(row_max(z1)));
      const float p0 = __builtin_amdgcn_exp2f(z0 - m0), p1 = __builtin_amdgcn_exp2f(z1 - m1);
      const unsigned pp = pkbf(p0, p1);
      unsigned short* pl = (unsigned short*)(lds + L_P + w * 256 + ((lane >> 3) & 1) * 64 + (lane & 7) * 8 + (lane >> 4) * 2);
      pl[0] = (unsigned short)pp;
      pl[64] = (unsigned short)(pp >> 16);
      VSTEP(VB2 + 10);
      const float l01 = x16_sum(row_sum(fold32(p0, p1)));
      if ((lane & 31) == 0) *(f32x2*)(lds + L_ST + ((lane >> 5) * 4 + w) * 8) = f32x2{lane ? m1 : m0, l01};
      lds_arrive(lds, L_CNT + 4, lane);
      const bool valid = (m16 & 3) == 0 && ((m16 >> 2) & 1) == (g4 & 1);
      const int a = valid ? L_P + w * 256 + (m16 >> 3) * 128 + (g4 >> 1) * 64 : L_Z;
#pragma unroll
      for (int G = 0; G < 4; ++G) pa[G] = *(const v4u*)(lds + a + G * 16);
    }
    STAMP(5);
    f32x4 acc[8];
#pragma unroll
    for (int e = 0; e < 8; ++e) acc[e] = zero4;
    v4u tr[12], bf[8];
    const rsrc_t rO = mk(wo, NQ * DM * 2), r2 = mk(w2, DFF * DM * 2);
    const int o2 = (r16 < 14 && lane < 56) ? (16 * fs + 4 * w) * (DM * 2) + lane * 16 : OOB;
    const int oo = lane < 56 ? (kvh * 512 + 32 * r16 + 8 * w) * (DM * 2) + lane * 16 : OOB;
#define TLOAD(i) tr[i] = (i) < 4 ? ld16<WAUX>(r2, o2, (i) * (DM * 2)) : ld16<WAUX>(rO, oo, ((i) - 4) * (DM * 2))
#define TSTEP(i) do { THROTTLE(); TLOAD(i); SB(); } while (0)
#define PAIRUP(src, pr) do { _Pragma("unroll") for (int dw = 0; dw < 4; ++dw) { \
      bf[2 * dw][pr] = perm_lo((src)[2 * (pr)][dw], (src)[2 * (pr) + 1][dw]); \
      bf[2 * dw + 1][pr] = perm_hi((src)[2 * (pr)][dw], (src)[2 * (pr) + 1][dw]); } } while (0)
#define PIN_PAIR(pr) do { _Pragma("unroll") for (int e = 0; e < 8; ++e) { unsigned tmp_ = bf[e][pr]; asm volatile("" : "+v"(tmp_)); bf[e][pr] = tmp_; } SB(); } while (0)
#define PVMMA(G) do { _Pragma("unroll") for (int e = 0; e < 8; ++e) acc[e] = mfma32(pa[G], bf[e], acc[e]); } while (0)
#define PVGROUP(G) do { PAIRUP(vr + 8 * (G), 0); PAIRUP(vr + 8 * (G), 1); PAIRUP(vr + 8 * (G), 2); PAIRUP(vr + 8 * (G), 3); PVMMA(G); SB(); } while (0)
    constexpr int TIN = TPRE, TWIN = (TIN + THR + 1 < 12) ? TIN + THR + 1 : 12, V0 = VB2 + 11;
#pragma unroll
    for (int i = V0; i < 32; ++i) {
      VSTEP(i);
#pragma unroll
      for (int k = 0; k < TIN; ++k)
        if (V0 + (k * (31 - V0)) / (TIN ? TIN : 1) == i) TSTEP(k);
      if (i == V0) PVGROUP(0);
      if (i == 24) PVGROUP(1);
      if (i == 31) PVGROUP(2);
    }
#pragma unroll
    for (int k = 0; k <= THR; ++k) {
      if (TIN + k < TWIN) TSTEP(TIN + k);
      if (k == (THR > 6 ? THR - 6 : 0)) { PAIRUP(vr + 24, 0); PIN_PAIR(0); }
      if (k == (THR > 4 ? THR - 4 : 0)) { PAIRUP(vr + 24, 1); PIN_PAIR(1); }
      if (k == THR - 2) { PAIRUP(vr + 24, 2); PIN_PAIR(2); }
    }
    int oma = L_OM + w * 8 * OM_ROW + lane * 4;
    asm volatile("" : "+v"(oma));
    SB();
    PAIRUP(vr + 24, 3);
    PVMMA(3);
    STAMP(6);
#pragma unroll
    for (int e = 0; e < 8; ++e) *(float*)(lds + oma + e * OM_ROW) = acc[e][0];
    LDSBAR();
    STAMP(7);
#pragma unroll
    for (int i = TWIN; i < 12; ++i) TLOAD(i);
    SB();
    const bool diag = (m16 >> 2) == g4;
    const v4u tg = *(const v4u*)(lds + L_TAG);
    const v2u ah = *(const v2u*)(lds + ((diag && (m16 & 3) < 2) ? L_HP + (m16 & 3) * 32 + w * 8 : L_Z));
    const unsigned t = tg.x;
#pragma unroll
    for (int dw = 0; dw < 4; ++dw) {
      acc[2 * dw] = mfma16(ah, v2u{perm_lo(tr[0][dw], tr[1][dw]), perm_lo(tr[2][dw], tr[3][dw])}, zero4);
      acc[2 * dw + 1] = mfma16(ah, v2u{perm_hi(tr[0][dw], tr[1][dw]), perm_hi(tr[2][dw], tr[3][dw])}, zero4);
    }
    SB();
    int ogo = WS_O + (((bp * 8 + kvh) * 16 + r16) * 2 + (w >> 1)) * 2048 + (lane >> 2) * 128 + (w & 1) * 64 + (lane & 3) * 16;
    v4u go = ld16<AUX_SC1>(rW, ogo, 0);
    SB();
    PAIRUP(tr + 4, 0); PAIRUP(tr + 4, 1); PAIRUP(tr + 4, 2); PAIRUP(tr + 4, 3);
    PIN_PAIR(0); PIN_PAIR(1); PIN_PAIR(2); PIN_PAIR(3);
    float gate = u2f(lane < 32 ? tg.y : tg.z);
    unsigned m0 = (diag && (lane & 1) == 0) ? 0xffffffffu : 0u, m1 = (diag && (lane & 1) != 0) ? 0xffffffffu : 0u;
    int fma_ = L_FM + w * 8 * FM_ROW + lane * 8;
    asm volatile("" : "+v"(gate), "+v"(m0), "+v"(m1), "+v"(fma_));
    SB();
    STAMP(11);
    if (!wave_none(untag(go, t) != 0u)) {
      _Pragma("nounroll") for (unsigned spin = 0; spin < SPIN_CAP; ++spin) {
        REREAD(ogo);
        go = ld16<AUX_SC1>(rW, ogo, 0);
        if (wave_none(untag(go, t) != 0u)) break;
        if (PSLEEP) __builtin_amdgcn_s_sleep(PSLEEP);
      }
    }
    STAMP(8);
    {
      const float lm = u2f(go.z) + u2f(go.w);
      float M = fmx(lm, dpp<ROR4>(lm));
      M = fmx(M, dpp<ROR8>(M));
      M = x16_max(M);
      float d = __builtin_amdgcn_exp2f(lm - M);
      float a = d * u2f(go.x), c = d * u2f(go.y);
      a += dpp<ROR4>(a); c += dpp<ROR4>(c); d += dpp<ROR4>(d);
      a += dpp<ROR8>(a); c += dpp<ROR8>(c); d += dpp<ROR8>(d);
      a = x16_sum(a); c = x16_sum(c);
      d = gate * __builtin_amdgcn_rcpf(x16_sum(d));
      const unsigned op = pkbf(a * d, c * d);
      const auto sw = __builtin_amdgcn_permlane32_swap(op, op, false, false);
      const v4u b0 = {dppu<0x00>(sw[0]), dppu<0x55>(sw[0]), dppu<0xAA>(sw[0]), dppu<0xFF>(sw[0])};
      const v4u b1 = {dppu<0x00>(sw[1]), dppu<0x55>(sw[1]), dppu<0xAA>(sw[1]), dppu<0xFF>(sw[1])};
      const v4u ao = (b0 & m0) | (b1 & m1);
#pragma unroll
      for (int e = 0; e < 8; ++e) acc[e] = mfma32(ao, bf[e], acc[e]);
#pragma unroll
      for (int e = 0; e < 8; ++e) *(f32x2*)(lds + fma_ + e * FM_ROW) = f32x2{acc[e][0], acc[e][1]};
    }
    LDSBAR();
    STAMP(9);
    if (w == 0 && pu < 112) {
      int fgo = WS_F + (bp * 28 + (pu >> 2)) * 16384 + (lane >> 1) * 128 + ((pu & 3) * 2 + (lane & 1)) * 16;
      const int oo2 = (lane & 14) == 0 ? ((2 * bp + (lane >> 5)) * DM + 4 * pu + 2 * (lane & 1) + (g4 & 1)) * 2 : OOB;
      v4u gf[4];
      lds_wait(lds, L_CNT + 12, 4);
      _Pragma("nounroll") for (unsigned spin = 0; spin < SPIN_CAP; ++spin) {
        REREAD(fgo);
#pragma unroll
        for (int k = 0; k < 4; ++k) gf[k] = ld16<AUX_SC1>(rW, fgo, k * 4096);
        const unsigned miss = (untag(gf[0], t) | untag(gf[1], t)) | (untag(gf[2], t) | untag(gf[3], t));
        if (wave_none(miss != 0u)) break;
        if (PSLEEP) __builtin_amdgcn_s_sleep(PSLEEP);
      }
      float v[4];
#pragma unroll
      for (int k = 0; k < 4; ++k) v[k] = (u2f(gf[0][k]) + u2f(gf[1][k])) + (u2f(gf[2][k]) + u2f(gf[3][k]));
      float r = fold16(fold32(v[0], v[1]), fold32(v[2], v[3]));
      r += dpp<ROR8>(r); r += dpp<ROR4>(r); r += dpp<XOR2>(r);
      __builtin_amdgcn_raw_buffer_store_b16((unsigned short)pkbf(r, r), mk(out, NB * DM * 2), oo2, 0, 0);
      if (id == 0 && lane == 0) __builtin_amdgcn_raw_buffer_store_b32(t, rW, WS_E, 0, AUX_SC1);
    }
  }
}

}

extern "C" void kernel_launch(void* const* d_in, const int* in_sizes, int n_in, void* d_out, int out_size, void* d_ws, size_t ws_size,
                              hipStream_t stream) {
  (void)in_sizes; (void)n_in; (void)out_size; (void)ws_size;
  gated_mix_decode_kernel<<<256, 512, 0, stream>>>(
      (const unsigned short*)d_in[0], (const unsigned short*)d_in[1], (const unsigned short*)d_in[2], (const unsigned short*)d_in[3],
      (const unsigned short*)d_in[4], (const unsigned short*)d_in[6], (const unsigned short*)d_in[7], (const unsigned short*)d_in[8],
      (unsigned short*)d_out, (unsigned char*)d_ws);
}
```

```cpp
#include <hip/hip_runtime.h>
#include <hip/hip_bf16.h>
#include <cstdint>

namespace {

typedef unsigned v4u __attribute__((ext_vector_type(4)));
typedef unsigned v2u __attribute__((ext_vector_type(2)));
typedef unsigned v16u __attribute__((ext_vector_type(16)));
typedef float f32x4 __attribute__((ext_vector_type(4)));
typedef float f32x2 __attribute__((ext_vector_type(2)));
typedef short s16x4 __attribute__((ext_vector_type(4)));
typedef __bf16 bf16x8 __attribute__((ext_vector_type(8)));
typedef __bf16 bf16x2 __attribute__((ext_vector_type(2)));
typedef __amdgpu_buffer_rsrc_t rsrc_t;
typedef __attribute__((address_space(3))) unsigned lds_u32;

constexpr int NB = 4, DM = 448, HKV = 8, SK = 8192, DH = 256, WIN = 2048, DFF = 1792, NQ = 4096;
constexpr int AUX_SC1 = 16;
constexpr int OOB = 0x40000000;
constexpr unsigned SPIN_CAP = 200000, TAG_MASK = 1023u;

#define WAUX 2
#define KVAUX 2
#define THR 7
#define QG_AT 25
#define VB2 6
#define KPRE 2
#define CODEPF 0
#define CODE_BYTES 11264
#define ILV 1
#define ADAPT 0
#define ADAPT_INIT 2
#define PACE_A 30
#define PACE_T 160
#define PACE_D 25
#define PACE_MAX 3
#define HDRY 0
#define ODELAY 60
#define FDELAY 14
#define FSLEEP 0
#define TWN (THR + 1)
#define OPOS 2
#define TPRE 0
#define PREWARM 1
#define PSLEEP 0
#define ABL 0

constexpr int WS_E = 0;
constexpr int WS_Q = 4096;
constexpr int WS_H = WS_Q + 8 * 4 * 8192;
constexpr int WS_O = WS_H + 28 * 4 * 2048;
constexpr int WS_F = WS_O + 16 * 16 * 4096;
constexpr int WS_END = WS_F + 2 * 28 * 16384;
constexpr int WS_DIAG = 8 << 20;

constexpr int OM_ROW = 68 * 4, FM_ROW = 544;
constexpr int L_QP = 0;
constexpr int L_Q = 8192;
constexpr int L_P = 9216;
constexpr int L_Z = 10240;
constexpr int L_CNT = 10304;
constexpr int L_TAG = 10320;
constexpr int L_HP = 10368;
constexpr int L_ST = 10432;
constexpr int L_OM = 10496;
constexpr int L_FM = L_OM + 32 * OM_ROW;
constexpr int L_END = L_FM + 32 * FM_ROW;

__device__ __forceinline__ rsrc_t mk(const void* p, int bytes) { return __builtin_amdgcn_make_buffer_rsrc((void*)p, 0, bytes, 0x00020000); }
template <int AUX> __device__ __forceinline__ v4u ld16(rsrc_t r, int vo, int so) { return __builtin_amdgcn_raw_buffer_load_b128(r, vo, so, AUX); }
__device__ __forceinline__ int oob_if(bool c) { return c ? OOB : 0; }
__device__ __forceinline__ float u2f(unsigned u) { return __builtin_bit_cast(float, u); }
__device__ __forceinline__ unsigned f2u(float f) { return __builtin_bit_cast(unsigned, f); }
__device__ __forceinline__ unsigned tagged(float v, unsigned t, unsigned keep) { return (f2u(v) & keep) | t; }
__device__ __forceinline__ void publish(rsrc_t r, int vo, float a, float b, float c, float d, unsigned t, unsigned keep = ~TAG_MASK) {
  __builtin_amdgcn_raw_buffer_store_b128(v4u{tagged(a, t, keep), tagged(b, t, keep), tagged(c, t, keep), tagged(d, t, keep)}, r, vo, 0, AUX_SC1);
}
__device__ __forceinline__ unsigned untag(v4u& g, unsigned t) { g ^= t; return (g.x | g.y | g.z | g.w) & TAG_MASK; }
__device__ __forceinline__ bool wave_none(bool p) { return __builtin_amdgcn_ballot_w64(p) == 0; }
__device__ __forceinline__ float dot2(unsigned a, unsigned b, float c) {
  return __builtin_amdgcn_fdot2_f32_bf16(__builtin_bit_cast(bf16x2, a), __builtin_bit_cast(bf16x2, b), c, false);
}
__device__ __forceinline__ unsigned pkbf(float lo, float hi) {
  const bf16x2 v = {(__bf16)lo, (__bf16)hi};
  return __builtin_bit_cast(unsigned, v);
}
__device__ __forceinline__ unsigned perm_lo(unsigned a, unsigned b) { return __builtin_amdgcn_perm(b, a, 0x05040100u); }
__device__ __forceinline__ unsigned perm_hi(unsigned a, unsigned b) { return __builtin_amdgcn_perm(b, a, 0x07060302u); }
template <int CTRL> __device__ __forceinline__ unsigned dppu(unsigned x) { return (unsigned)__builtin_amdgcn_mov_dpp((int)x, CTRL, 0xf, 0xf, true); }
template <int CTRL> __device__ __forceinline__ float dpp(float x) { return u2f(dppu<CTRL>(f2u(x))); }
constexpr int XOR1 = 0xB1, XOR2 = 0x4E, XOR7 = 0x141, ROR8 = 0x128, ROR4 = 0x124;
__device__ __forceinline__ float fmx(float a, float c) { return __builtin_amdgcn_fmed3f(a, c, __builtin_inff()); }
__device__ __forceinline__ float fold16(float a, float b) {
  auto s = __builtin_amdgcn_permlane16_swap(f2u(a), f2u(b), false, false);
  return u2f(s[0]) + u2f(s[1]);
}
__device__ __forceinline__ float fold32(float a, float b) {
  auto s = __builtin_amdgcn_permlane32_swap(f2u(a), f2u(b), false, false);
  return u2f(s[0]) + u2f(s[1]);
}
__device__ __forceinline__ float x16_sum(float x) { return fold16(x, x); }
__device__ __forceinline__ float x32_sum(float x) { return fold32(x, x); }
__device__ __forceinline__ float x16_max(float x) {
  auto s = __builtin_amdgcn_permlane16_swap(f2u(x), f2u(x), false, false);
  return fmx(u2f(s[0]), u2f(s[1]));
}
__device__ __forceinline__ float x32_max(float x) {
  auto s = __builtin_amdgcn_permlane32_swap(f2u(x), f2u(x), false, false);
  return fmx(u2f(s[0]), u2f(s[1]));
}
__device__ __forceinline__ float row_sum(float x) { x += dpp<XOR1>(x); x += dpp<XOR2>(x); x += dpp<ROR4>(x); x += dpp<ROR8>(x); return x; }
__device__ __forceinline__ float row_max(float x) {
  x = fmx(x, dpp<XOR1>(x)); x = fmx(x, dpp<XOR2>(x)); x = fmx(x, dpp<ROR4>(x)); x = fmx(x, dpp<ROR8>(x)); return x;
}
__device__ __forceinline__ f32x4 mfma32(v4u a, v4u b, f32x4 c) {
  return __builtin_amdgcn_mfma_f32_16x16x32_bf16(__builtin_bit_cast(bf16x8, a), __builtin_bit_cast(bf16x8, b), c, 0, 0, 0);
}
__device__ __forceinline__ f32x4 mfma16(v2u a, v2u b, f32x4 c) {
  return __builtin_amdgcn_mfma_f32_16x16x16bf16_1k(__builtin_bit_cast(s16x4, a), __builtin_bit_cast(s16x4, b), c, 0, 0, 0);
}

#define SB() __builtin_amdgcn_sched_barrier(0)
#define REREAD(off) asm volatile("" : "+v"(off) :: "memory")
#define VMWAIT(n) __builtin_amdgcn_s_waitcnt(((n) & 15) | (7 << 4) | (15 << 8) | ((((n) >> 4) & 3) << 14))
#define THROTTLE() do { SB(); VMWAIT(THR); SB(); } while (0)
#define PACE(k) do { } while (0)
#define LDSBAR() do { SB(); __builtin_amdgcn_fence(__ATOMIC_RELEASE, "workgroup", "local"); __builtin_amdgcn_s_barrier(); \
                      __builtin_amdgcn_fence(__ATOMIC_ACQUIRE, "workgroup", "local"); SB(); } while (0)

__device__ __forceinline__ void lds_arrive(unsigned char* lds, int off, int lane) {
  __builtin_amdgcn_fence(__ATOMIC_RELEASE, "workgroup", "local");
  if (lane == 0) __hip_atomic_fetch_add((lds_u32*)(lds + off), 1u, __ATOMIC_RELAXED, __HIP_MEMORY_SCOPE_WORKGROUP);
}
__device__ __forceinline__ void lds_wait(unsigned char* lds, int off, unsigned want) {
  _Pragma("nounroll") for (unsigned spin = 0; spin < 16u * SPIN_CAP; ++spin) {
    if ((unsigned)__builtin_amdgcn_readfirstlane(__hip_atomic_load((lds_u32*)(lds + off), __ATOMIC_RELAXED, __HIP_MEMORY_SCOPE_WORKGROUP)) >= want) break;
    __builtin_amdgcn_s_sleep(1);
  }
  __builtin_amdgcn_fence(__ATOMIC_ACQUIRE, "workgroup", "local");
}

#define STAMP(k) do { } while (0)

__global__ void __launch_bounds__(512, 2)
gated_mix_decode_kernel(const unsigned short*, const unsigned short*, const unsigned short*, const unsigned short*, const unsigned short*,
                        const unsigned short*, const unsigned short*, const unsigned short*, unsigned short*, unsigned char*) {
  __shared__ __attribute__((aligned(128))) unsigned char lds[L_END];
  STAMP(0);
  v16u ka; v4u kb;
  asm volatile("s_load_dwordx16 %0, %2, 0x0\n\ts_load_dwordx4 %1, %2, 0x40\n\ts_waitcnt lgkmcnt(0)"
               : "=&s"(ka), "=&s"(kb) : "s"(__builtin_amdgcn_kernarg_segment_ptr()) : "memory");
#define KARG(T, lo, hi) ((T)(((unsigned long long)(hi) << 32) | (lo)))
  const unsigned short* x = KARG(const unsigned short*, ka[0], ka[1]);
  const unsigned short* kc = KARG(const unsigned short*, ka[2], ka[3]);
  const unsigned short* vc = KARG(const unsigned short*, ka[4], ka[5]);
  const unsigned short* wq = KARG(const unsigned short*, ka[6], ka[7]);
  const unsigned short* wo = KARG(const unsigned short*, ka[8], ka[9]);
  const unsigned short* w1 = KARG(const unsigned short*, ka[10], ka[11]);
  const unsigned short* w2 = KARG(const unsigned short*, ka[12], ka[13]);
  const unsigned short* gw = KARG(const unsigned short*, ka[14], ka[15]);
  unsigned short* out = KARG(unsigned short*, kb[0], kb[1]);
  unsigned char* ws = KARG(unsigned char*, kb[2], kb[3]);
  const int id = blockIdx.x, kvh = id & 7, b = (id >> 3) & 3, j = id >> 5;
  const int lane = threadIdx.x & 63, wv = __builtin_amdgcn_readfirstlane(threadIdx.x >> 6), w = wv & 3;
  const int bp = b >> 1, r16 = (b & 1) * 8 + j, pu = kvh * 16 + r16, fs = kvh * 14 + r16;
  const int m16 = lane & 15, g4 = lane >> 4;
  const rsrc_t rW = mk(ws, WS_END);
  const f32x4 zero4 = {0.f, 0.f, 0.f, 0.f};

  if (wv < 4) {
    const int tid = threadIdx.x;
    const rsrc_t rQ = mk(wq, DM * NQ * 2), r1 = mk(w1, DM * DFF * 2), rX = mk(x, NB * DM * 2), rG = mk(gw, DM * 2);
    v4u wqr[4], w1r[2];
    v2u xq, xh;
    {
      const int row = 32 * w + 4 * (lane >> 3);
      const int vo = ((112 * b + row) * (NQ * 2) + (kvh * 8 + j) * 128 + (lane & 7) * 16) | oob_if(row >= 112);
#pragma unroll
      for (int i = 0; i < 4; ++i) wqr[i] = ld16<WAUX>(rQ, vo, i * (NQ * 2));
      SB();
      const int rr = 2 * g4 + (m16 & 3), xrow = (m16 >> 2) * DM;
      const bool off = (m16 & 2) != 0;
      xq = __builtin_amdgcn_raw_buffer_load_b64(rX, ((xrow + 112 * b + 32 * w + 4 * rr) * 2) | oob_if(off | (32 * w + 4 * rr >= 112)), 0, 0);
      xh.x = __builtin_amdgcn_raw_buffer_load_b32(rX, ((xrow + 56 * kvh + 16 * w + 2 * rr) * 2) | oob_if(off | (16 * w + 2 * rr >= 56)), 0, 0);
      xh.y = 0u;
      SB();
      const int row1 = 16 * w + 2 * (lane >> 3);
      const int o1 = ((56 * kvh + row1) * (DFF * 2) + (4 * j + b) * 128 + (lane & 7) * 16) | oob_if((j >= 7) | (row1 >= 56));
#pragma unroll
      for (int i = 0; i < 2; ++i) w1r[i] = ld16<WAUX>(r1, o1, i * (DFF * 2));
    }
    const unsigned ep = __builtin_amdgcn_raw_buffer_load_b32(rW, WS_E, 0, AUX_SC1);
    SB();
    if (tid < 20) *(unsigned*)(lds + L_Z + tid * 4) = 0u;
    LDSBAR();
    {
      float qv[8], hv[8];
#pragma unroll
      for (int dw = 0; dw < 4; ++dw) {
        const v2u blo = {perm_lo(wqr[0][dw], wqr[1][dw]), perm_lo(wqr[2][dw], wqr[3][dw])};
        const v2u bhi = {perm_hi(wqr[0][dw], wqr[1][dw]), perm_hi(wqr[2][dw], wqr[3][dw])};
        const f32x4 dl = mfma16(xq, blo, zero4), dh = mfma16(xq, bhi, zero4);
        const float sl = (lane & 8) ? dl[1] : dl[0], sh = (lane & 8) ? dh[1] : dh[0];
        qv[2 * dw] = sl + dpp<ROR8>(sl);
        qv[2 * dw + 1] = sh + dpp<ROR8>(sh);
      }
#pragma unroll
      for (int dw = 0; dw < 4; ++dw) {
        const v2u blo = {perm_lo(w1r[0][dw], w1r[1][dw]), 0u}, bhi = {perm_hi(w1r[0][dw], w1r[1][dw]), 0u};
        const f32x4 dl = mfma16(xh, blo, zero4), dh = mfma16(xh, bhi, zero4);
        const float sl = (lane & 8) ? dl[1] : dl[0], sh = (lane & 8) ? dh[1] : dh[0];
        hv[2 * dw] = sl + dpp<ROR8>(sl);
        hv[2 * dw + 1] = sh + dpp<ROR8>(sh);
      }
      const bool up = (lane & 8) != 0;
      f32x4* p = (f32x4*)(lds + L_QP + ((w * 4 + g4) * 128 + 8 * m16) * 4);
      p[0] = up ? f32x4{hv[0], hv[1], hv[2], hv[3]} : f32x4{qv[0], qv[1], qv[2], qv[3]};
      p[1] = up ? f32x4{hv[4], hv[5], hv[6], hv[7]} : f32x4{qv[4], qv[5], qv[6], qv[7]};
    }
    lds_arrive(lds, L_CNT, lane);
    const unsigned t = ((unsigned)__builtin_amdgcn_readfirstlane(ep) + 1u) & TAG_MASK;
    STAMP(1);
    lds_wait(lds, L_CNT, 4);
    {
      f32x4 s = zero4;
#pragma unroll
      for (int ww = 0; ww < 4; ++ww) s += *(const f32x4*)(lds + L_QP + ((ww * 4 + w) * 128 + 4 * (lane & 31)) * 4);
      const int dst = lane < 16 ? WS_Q + (kvh * 4 + w) * 8192 + j * 1024 + b * 256 + lane * 16
                    : (lane < 32 && j < 7) ? WS_H + ((4 * j + b) * 4 + w) * 2048 + kvh * 256 + m16 * 16 : OOB;
      publish(rW, (ABL & 8) ? OOB : dst, s[0], s[1], s[2], s[3], t);
    }
    SB();
    STAMP(2);
    STAMP(3);
    lds_wait(lds, L_CNT + 8, 1);
    STAMP(5);
    {
      const int go_ = (lane * 16) | oob_if((lane >= 56) | (w != 3) | ((ABL & 8192) != 0));
      const v4u xg0 = ld16<0>(rX, go_, 2 * bp * DM * 2), xg1 = ld16<0>(rX, go_, (2 * bp + 1) * DM * 2), gg = ld16<0>(rG, go_, 0);
      SB();
      int qgo = (ABL & (2 | 16384)) ? OOB : WS_Q + (kvh * 4 + b) * 8192 + (2 * w + (lane >> 5)) * 1024 + (g4 & 1) * 256 + m16 * 16;
      const bool hon = w == 3 && r16 < 14;
      int hgo = (hon && !(ABL & (2 | 4096))) ? WS_H + ((fs >> 2) * 4 + 2 * bp + (lane >> 5)) * 2048 + ((lane >> 2) & 7) * 256 + (fs & 3) * 64 + (lane & 3) * 16 : OOB;
      v4u g0, g1, gh;
      _Pragma("nounroll") for (unsigned spin = 0; spin < SPIN_CAP; ++spin) {
        REREAD(qgo); REREAD(hgo);
        g0 = ld16<AUX_SC1>(rW, qgo, 0);
        g1 = ld16<AUX_SC1>(rW, qgo, 512);
        gh = ld16<AUX_SC1>(rW, hgo, 0);
        const unsigned mq = untag(g0, t) | untag(g1, t), mh = untag(gh, t);
        if ((ABL & 1) || wave_none((mq | (hon ? mh : 0u)) != 0u)) break;
        __builtin_amdgcn_s_sleep(8);
      }
      float q[4];
#pragma unroll
      for (int k = 0; k < 4; ++k) q[k] = x16_sum(u2f(g0[k]) + u2f(g1[k]));
      if ((g4 & 1) == 0) *(v2u*)(lds + L_Q + ((2 * w + (lane >> 5)) * 64 + 4 * m16) * 2) = v2u{pkbf(q[0], q[1]), pkbf(q[2], q[3])};
      if (w == 3) {
        float d0 = 0.f, d1 = 0.f;
#pragma unroll
        for (int k = 0; k < 4; ++k) { d0 = dot2(xg0[k], gg[k], d0); d1 = dot2(xg1[k], gg[k], d1); }
        const float gate = 1.f / (1.f + __expf(-x16_sum(row_sum(fold32(d0, d1)))));
        if ((lane & 31) == 0) *(float*)(lds + L_TAG + 4 + 4 * (lane >> 5)) = gate;
        if (lane == 1) *(unsigned*)(lds + L_TAG) = t;
        float h[4];
#pragma unroll
        for (int k = 0; k < 4; ++k) {
          float a = hon ? u2f(gh[k]) : 0.f;
          a += dpp<ROR4>(a);
          a += dpp<ROR8>(a);
          h[k] = fmaxf(x16_sum(a), 0.f) * (1.f - gate);
        }
        if ((lane & 28) == 0) *(v2u*)(lds + L_HP + (lane >> 5) * 32 + (lane & 3) * 8) = v2u{pkbf(h[0], h[1]), pkbf(h[2], h[3])};
      }
    }
    STAMP(4);
    LDSBAR();
    if (PREWARM) {
      const int po = WS_O + ((bp * 8 + kvh) * 16 + r16) * 4096 + (lane & 31) * 128;
      const int pf = WS_F + (bp * 28 + (pu >> 2)) * 16384 + (w * 32 + (lane & 31)) * 128;
      const unsigned pw = __builtin_amdgcn_raw_buffer_load_b32(rW, lane < 32 ? (w == 0 ? po : OOB) : (((PREWARM & 2) && pu < 112) ? pf : OOB), 0, AUX_SC1);
      asm volatile("" :: "v"(pw));
    }
    lds_wait(lds, L_CNT + 4, 4);
    float lse;
    f32x4 fc;
    {
      const int h = tid >> 7;
      const f32x4 e0 = *(const f32x4*)(lds + L_ST + h * 32), e1 = *(const f32x4*)(lds + L_ST + h * 32 + 16);
      const float M = fmx(fmx(e0[0], e0[2]), fmx(e1[0], e1[2]));
      fc = f32x4{__builtin_amdgcn_exp2f(e0[0] - M), __builtin_amdgcn_exp2f(e0[2] - M), __builtin_amdgcn_exp2f(e1[0] - M), __builtin_amdgcn_exp2f(e1[2] - M)};
      const float L = (fc[0] * e0[1] + fc[1] * e0[3]) + (fc[2] * e1[1] + fc[3] * e1[3]);
      fc *= 1.f / L;
      lse = M + __builtin_amdgcn_logf(L);
    }
    float lse_hi = u2f(f2u(lse) & ~TAG_MASK), lse_lo = lse - lse_hi;
    int oa = L_OM + 2 * (tid & 3) * OM_ROW + (tid >> 2) * 4, fa = L_FM + 2 * (tid & 3) * FM_ROW + (tid >> 2) * 8;
    int odst = (ABL & 128) ? OOB : WS_O + ((bp * 8 + kvh) * 32 + (tid >> 3)) * 2048 + r16 * 128 + (tid & 7) * 16;
    int fdst = tid < 224 ? WS_F + (bp * 28 + (tid >> 3)) * 16384 + pu * 128 + (tid & 7) * 16 : OOB;
    asm volatile("" : "+v"(lse_hi), "+v"(lse_lo), "+v"(fc), "+v"(oa), "+v"(fa), "+v"(odst), "+v"(fdst));
    STAMP(6);
    int pass = HDRY ? 0 : 1;
    unsigned tv = t, keep = ~TAG_MASK;
    asm volatile("" : "+s"(pass), "+v"(tv), "+s"(keep));
    _Pragma("nounroll") for (; pass < 2; ++pass) {
      const int live = pass ? 0 : OOB;
      if (pass) LDSBAR();
      {
        float a = 0.f, c = 0.f;
#pragma unroll
        for (int ww = 0; ww < 4; ++ww) {
          a = fmaf(fc[ww], *(const float*)(lds + oa + ww * 8 * OM_ROW), a);
          c = fmaf(fc[ww], *(const float*)(lds + oa + ww * 8 * OM_ROW + OM_ROW), c);
        }
        publish(rW, odst | live, a, c, lse_hi, lse_lo, tv, keep);
      }
      STAMP(7);
      if (pass) LDSBAR();
      {
        f32x2 s0 = {0.f, 0.f}, s1 = {0.f, 0.f};
#pragma unroll
        for (int ww = 0; ww < 4; ++ww) {
          s0 += *(const f32x2*)(lds + fa + ww * 8 * FM_ROW);
          s1 += *(const f32x2*)(lds + fa + ww * 8 * FM_ROW + FM_ROW);
        }
        publish(rW, fdst | live, s0.x, s0.y, s1.x, s1.y, tv, keep);
        SB();
        if (pass) lds_arrive(lds, L_CNT + 12, lane);
      }
    }
    STAMP(9);
  } else {
    const size_t head = ((size_t)(b * HKV + kvh) * SK + (SK - WIN)) * DH;
    const rsrc_t rK = mk(kc + head, WIN * DH * 2), rV = mk(vc + head, WIN * DH * 2);
    constexpr int BSTR = ILV ? 131072 : 16384;
    const int blk0 = ILV == 2 ? (j & 1) * 4096 + w * 8192 + (j >> 1) * 32768 : (ILV ? j * 16384 : j * 131072) + w * 4096;
    v4u kr[32], vr[32];
    const int ko = blk0 + (lane >> 3) * 512 + (lane & 7) * 16;
#define KLOAD(i) kr[i] = ld16<KVAUX>(rK, ko, ((i) >> 2) * BSTR + ((i) & 3) * 128)
#pragma unroll
    for (int i = 0; i < KPRE; ++i) KLOAD(i);
    LDSBAR();
#pragma unroll
    for (int i = KPRE; i < 32; ++i) { THROTTLE(); KLOAD(i); if (i >= 11 && (i & 3) == 3) PACE(i); if (i == QG_AT && w == 0) lds_arrive(lds, L_CNT + 8, lane); }
    const int vo = blk0 + lane * 16;
#define VLOAD(i) vr[i] = ld16<KVAUX>(rV, vo, ((i) >> 2) * BSTR + ((i) & 3) * 1024)
#define VSTEP(i) do { THROTTLE(); VLOAD(i); SB(); if (((i) & 3) == 3) PACE(32 + (i)); } while (0)
    STAMP(3);
#pragma unroll
    for (int i = 0; i < VB2; ++i) VSTEP(i);
    LDSBAR();
    STAMP(4);
    v4u pa[4];
    {
      v4u qf[2][4];
#pragma unroll
      for (int h = 0; h < 2; ++h)
#pragma unroll
        for (int t = 0; t < 4; ++t) qf[h][t] = *(const v4u*)(lds + L_Q + (h * 256 + 64 * t + 8 * (lane & 7)) * 2);
      VSTEP(VB2);
      float s[8][2];
#pragma unroll
      for (int i = 0; i < 8; i += 2) {
        float a0 = 0.f, a1 = 0.f, c0 = 0.f, c1 = 0.f;
#pragma unroll
        for (int t = 0; t < 4; ++t) {
#pragma unroll
          for (int dw = 0; dw < 4; ++dw) {
            a0 = dot2(kr[4 * i + t][dw], qf[0][t][dw], a0); c0 = dot2(kr[4 * i + 4 + t][dw], qf[0][t][dw], c0);
            a1 = dot2(kr[4 * i + t][dw], qf[1][t][dw], a1); c1 = dot2(kr[4 * i + 4 + t][dw], qf[1][t][dw], c1);
          }
          if (t == 1) VSTEP(VB2 + 1 + i);
        }
        s[i][0] = a0; s[i][1] = a1; s[i + 1][0] = c0; s[i + 1][1] = c1;
        VSTEP(VB2 + 2 + i);
      }
#pragma unroll
      for (int i = 0; i < 4; ++i)
#pragma unroll
        for (int h = 0; h < 2; ++h) {
          const float lo = s[i][h] + dpp<XOR7>(s[i][h]), hi = s[i + 4][h] + dpp<XOR7>(s[i + 4][h]);
          s[i][h] = (lane & 4) ? hi : lo;
        }
#pragma unroll
      for (int i = 0; i < 2; ++i)
#pragma unroll
        for (int h = 0; h < 2; ++h) {
          const float lo = s[i][h] + dpp<XOR2>(s[i][h]), hi = s[i + 2][h] + dpp<XOR2>(s[i + 2][h]);
          s[i][h] = (lane & 2) ? hi : lo;
        }
#pragma unroll
      for (int h = 0; h < 2; ++h) {
        const float lo = s[0][h] + dpp<XOR1>(s[0][h]), hi = s[1][h] + dpp<XOR1>(s[1][h]);
        s[0][h] = (lane & 1) ? hi : lo;
      }
      VSTEP(VB2 + 9);
      constexpr float C2 = 0.0625f * 1.4426950408889634f;
      const float z0 = s[0][0] * C2, z1 = s[0][1] * C2;
      const float m0 = x32_max(x16_max(row_max(z0))), m1 = x32_max(x16_max(row_max(z1)));
      const float p0 = __builtin_amdgcn_exp2f(z0 - m0), p1 = __builtin_amdgcn_exp2f(z1 - m1);
      const unsigned pp = pkbf(p0, p1);
      unsigned short* pl = (unsigned short*)(lds + L_P + w * 256 + ((lane >> 3) & 1) * 64 + (lane & 7) * 8 + (lane >> 4) * 2);
      pl[0] = (unsigned short)pp;
      pl[64] = (unsigned short)(pp >> 16);
      VSTEP(VB2 + 10);
      const float l01 = x16_sum(row_sum(fold32(p0, p1)));
      if ((lane & 31) == 0) *(f32x2*)(lds + L_ST + ((lane >> 5) * 4 + w) * 8) = f32x2{lane ? m1 : m0, l01};
      lds_arrive(lds, L_CNT + 4, lane);
      const bool valid = (m16 & 3) == 0 && ((m16 >> 2) & 1) == (g4 & 1);
      const int a = valid ? L_P + w * 256 + (m16 >> 3) * 128 + (g4 >> 1) * 64 : L_Z;
#pragma unroll
      for (int G = 0; G < 4; ++G) pa[G] = *(const v4u*)(lds + a + G * 16);
    }
    STAMP(5);
    f32x4 acc[8];
#pragma unroll
    for (int e = 0; e < 8; ++e) acc[e] = zero4;
    v4u tr[12], bf[8];
    const rsrc_t rO = mk(wo, NQ * DM * 2), r2 = mk(w2, DFF * DM * 2);
    const int o2 = (r16 < 14 && lane < 56) ? (16 * fs + 4 * w) * (DM * 2) + lane * 16 : OOB;
    const int oo = lane < 56 ? (kvh * 512 + 32 * r16 + 8 * w) * (DM * 2) + lane * 16 : OOB;
#define TLOAD(i) tr[i] = (i) < 4 ? ld16<WAUX>(r2, o2, (i) * (DM * 2)) : ld16<WAUX>(rO, oo, ((i) - 4) * (DM * 2))
#define TSTEP(i) do { THROTTLE(); TLOAD(i); SB(); } while (0)
#define PAIRUP(src, pr) do { _Pragma("unroll") for (int dw = 0; dw < 4; ++dw) { \
      bf[2 * dw][pr] = perm_lo((src)[2 * (pr)][dw], (src)[2 * (pr) + 1][dw]); \
      bf[2 * dw + 1][pr] = perm_hi((src)[2 * (pr)][dw], (src)[2 * (pr) + 1][dw]); } } while (0)
#define PIN_PAIR(pr) do { _Pragma("unroll") for (int e = 0; e < 8; ++e) { unsigned tmp_ = bf[e][pr]; asm volatile("" : "+v"(tmp_)); bf[e][pr] = tmp_; } SB(); } while (0)
#define PVMMA(G) do { _Pragma("unroll") for (int e = 0; e < 8; ++e) acc[e] = mfma32(pa[G], bf[e], acc[e]); } while (0)
#define PVGROUP(G) do { PAIRUP(vr + 8 * (G), 0); PAIRUP(vr + 8 * (G), 1); PAIRUP(vr + 8 * (G), 2); PAIRUP(vr + 8 * (G), 3); PVMMA(G); SB(); } while (0)
    constexpr int TIN = TPRE, TWIN = (TIN + TWN < 12) ? TIN + TWN : 12, V0 = VB2 + 11, KOFF = THR + 1 - TWN;
#pragma unroll
    for (int i = V0; i < 32; ++i) {
      VSTEP(i);
#pragma unroll
      for (int k = 0; k < TIN; ++k)
        if (V0 + (k * (31 - V0)) / (TIN ? TIN : 1) == i) TSTEP(k);
      if (i == V0) PVGROUP(0);
      if (i == 24) PVGROUP(1);
      if (i == 31) PVGROUP(2);
    }
#pragma unroll
    for (int k = 0; k <= THR; ++k) {
      if (k >= KOFF && TIN + k - KOFF < TWIN) { SB(); VMWAIT(THR - KOFF); SB(); TLOAD(TIN + k - KOFF); SB(); }
      if (k == (THR > 6 ? THR - 6 : 0)) { PAIRUP(vr + 24, 0); PIN_PAIR(0); }
      if (k == (THR > 4 ? THR - 4 : 0)) { PAIRUP(vr + 24, 1); PIN_PAIR(1); }
      if (k == THR - 2) { PAIRUP(vr + 24, 2); PIN_PAIR(2); }
    }
    int oma = L_OM + w * 8 * OM_ROW + lane * 4;
    asm volatile("" : "+v"(oma));
    SB();
    PAIRUP(vr + 24, 3);
    PVMMA(3);
    STAMP(6);
#pragma unroll
    for (int e = 0; e < 8; ++e) *(float*)(lds + oma + e * OM_ROW) = acc[e][0];
    LDSBAR();
    const unsigned tb3 = ODELAY > 0 ? (unsigned)__builtin_amdgcn_s_memrealtime() : 0u;
    STAMP(7);
#pragma unroll
    for (int i = TWIN; i < 12; ++i) TLOAD(i);
    SB();
    const bool diag = (m16 >> 2) == g4;
    const v4u tg = *(const v4u*)(lds + L_TAG);
    const v2u ah = *(const v2u*)(lds + ((diag && (m16 & 3) < 2) ? L_HP + (m16 & 3) * 32 + w * 8 : L_Z));
    const unsigned t = tg.x;
#pragma unroll
    for (int dw = 0; dw < 4; ++dw) {
      acc[2 * dw] = mfma16(ah, v2u{perm_lo(tr[0][dw], tr[1][dw]), perm_lo(tr[2][dw], tr[3][dw])}, zero4);
      acc[2 * dw + 1] = mfma16(ah, v2u{perm_hi(tr[0][dw], tr[1][dw]), perm_hi(tr[2][dw], tr[3][dw])}, zero4);
    }
    SB();
    int ogo = WS_O + (((bp * 8 + kvh) * 16 + r16) * 2 + (w >> 1)) * 2048 + (lane >> 2) * 128 + (w & 1) * 64 + (lane & 3) * 16;
    float gate = u2f(lane < 32 ? tg.y : tg.z);
    unsigned m0 = (diag && (lane & 1) == 0) ? 0xffffffffu : 0u, m1 = (diag && (lane & 1) != 0) ? 0xffffffffu : 0u;
    int fma_ = L_FM + w * 8 * FM_ROW + lane * 8;
    asm volatile("" : "+v"(gate), "+v"(m0), "+v"(m1), "+v"(fma_), "+v"(ogo));
    SB();
    if (OPOS >= 1) { PAIRUP(tr + 4, 0); PIN_PAIR(0); }
    if (OPOS >= 2) { PAIRUP(tr + 4, 1); PIN_PAIR(1); }
    if (OPOS >= 3) { PAIRUP(tr + 4, 2); PIN_PAIR(2); }
    if (OPOS >= 4) { PAIRUP(tr + 4, 3); PIN_PAIR(3); }
    if (ODELAY > 0) while ((int)((unsigned)__builtin_amdgcn_s_memrealtime() - tb3) < ODELAY) {}
    SB();
    v4u go = ld16<AUX_SC1>(rW, ogo, 0);
    SB();
    if (OPOS < 1) { PAIRUP(tr + 4, 0); PIN_PAIR(0); }
    if (OPOS < 2) { PAIRUP(tr + 4, 1); PIN_PAIR(1); }
    if (OPOS < 3) { PAIRUP(tr + 4, 2); PIN_PAIR(2); }
    if (OPOS < 4) { PAIRUP(tr + 4, 3); PIN_PAIR(3); }
    STAMP(11);
    if (!wave_none(untag(go, t) != 0u)) {
      _Pragma("nounroll") for (unsigned spin = 0; spin < SPIN_CAP; ++spin) {
        if (PSLEEP) __builtin_amdgcn_s_sleep(PSLEEP);
        REREAD(ogo);
        go = ld16<AUX_SC1>(rW, ogo, 0);
        if (wave_none(untag(go, t) != 0u)) break;
      }
    }
    STAMP(8);
    {
      const float lm = u2f(go.z) + u2f(go.w);
      float M = fmx(lm, dpp<ROR4>(lm));
      M = fmx(M, dpp<ROR8>(M));
      M = x16_max(M);
      float d = __builtin_amdgcn_exp2f(lm - M);
      float a = d * u2f(go.x), c = d * u2f(go.y);
      a += dpp<ROR4>(a); c += dpp<ROR4>(c); d += dpp<ROR4>(d);
      a += dpp<ROR8>(a); c += dpp<ROR8>(c); d += dpp<ROR8>(d);
      a = x16_sum(a); c = x16_sum(c);
      d = gate * __builtin_amdgcn_rcpf(x16_sum(d));
      const unsigned op = pkbf(a * d, c * d);
      const auto sw = __builtin_amdgcn_permlane32_swap(op, op, false, false);
      const v4u b0 = {dppu<0x00>(sw[0]), dppu<0x55>(sw[0]), dppu<0xAA>(sw[0]), dppu<0xFF>(sw[0])};
      const v4u b1 = {dppu<0x00>(sw[1]), dppu<0x55>(sw[1]), dppu<0xAA>(sw[1]), dppu<0xFF>(sw[1])};
      const v4u ao = (b0 & m0) | (b1 & m1);
#pragma unroll
      for (int e = 0; e < 8; ++e) acc[e] = mfma32(ao, bf[e], acc[e]);
#pragma unroll
      for (int e = 0; e < 8; ++e) *(f32x2*)(lds + fma_ + e * FM_ROW) = f32x2{acc[e][0], acc[e][1]};
    }
    LDSBAR();
    STAMP(9);
    if (w == 0 && pu < 112) {
      int fgo = WS_F + (bp * 28 + (pu >> 2)) * 16384 + (lane >> 1) * 128 + ((pu & 3) * 2 + (lane & 1)) * 16;
      const int oo2 = (lane & 14) == 0 ? ((2 * bp + (lane >> 5)) * DM + 4 * pu + 2 * (lane & 1) + (g4 & 1)) * 2 : OOB;
      v4u gf[4];
      lds_wait(lds, L_CNT + 12, 4);
      if (FDELAY) __builtin_amdgcn_s_sleep(FDELAY);
      _Pragma("nounroll") for (unsigned spin = 0; spin < SPIN_CAP; ++spin) {
        REREAD(fgo);
#pragma unroll
        for (int k = 0; k < 4; ++k) gf[k] = ld16<AUX_SC1>(rW, fgo, k * 4096);
        const unsigned miss = (untag(gf[0], t) | untag(gf[1], t)) | (untag(gf[2], t) | untag(gf[3], t));
        if (wave_none(miss != 0u)) break;
        if (FSLEEP) __builtin_amdgcn_s_sleep(FSLEEP);
      }
      float v[4];
#pragma unroll
      for (int k = 0; k < 4; ++k) v[k] = (u2f(gf[0][k]) + u2f(gf[1][k])) + (u2f(gf[2][k]) + u2f(gf[3][k]));
      float r = fold16(fold32(v[0], v[1]), fold32(v[2], v[3]));
      r += dpp<ROR8>(r); r += dpp<ROR4>(r); r += dpp<XOR2>(r);
      __builtin_amdgcn_raw_buffer_store_b16((unsigned short)pkbf(r, r), mk(out, NB * DM * 2), oo2, 0, 0);
      if (id == 0 && lane == 0) __builtin_amdgcn_raw_buffer_store_b32(t, rW, WS_E, 0, AUX_SC1);
    }
  }
}

}

extern "C" void kernel_launch(void* const* d_in, const int* in_sizes, int n_in, void* d_out, int out_size, void* d_ws, size_t ws_size,
                              hipStream_t stream) {
  (void)in_sizes; (void)n_in; (void)out_size; (void)ws_size;
  gated_mix_decode_kernel<<<256, 512, 0, stream>>>(
      (const unsigned short*)d_in[0], (const unsigned short*)d_in[1], (const unsigned short*)d_in[2], (const unsigned short*)d_in[3],
      (const unsigned short*)d_in[4], (const unsigned short*)d_in[6], (const unsigned short*)d_in[7], (const unsigned short*)d_in[8],
      (unsigned short*)d_out, (unsigned char*)d_ws);
}
```

```cpp
#include <hip/hip_runtime.h>
#include <hip/hip_bf16.h>
#include <cstdint>

namespace {

typedef unsigned v4u __attribute__((ext_vector_type(4)));
typedef unsigned v2u __attribute__((ext_vector_type(2)));
typedef unsigned v16u __attribute__((ext_vector_type(16)));
typedef float f32x4 __attribute__((ext_vector_type(4)));
typedef float f32x2 __attribute__((ext_vector_type(2)));
typedef short s16x4 __attribute__((ext_vector_type(4)));
typedef __bf16 bf16x8 __attribute__((ext_vector_type(8)));
typedef __bf16 bf16x2 __attribute__((ext_vector_type(2)));
typedef __amdgpu_buffer_rsrc_t rsrc_t;
typedef __attribute__((address_space(3))) unsigned lds_u32;

constexpr int NB = 4, DM = 448, HKV = 8, SK = 8192, DH = 256, WIN = 2048, DFF = 1792, NQ = 4096;
constexpr int AUX_SC1 = 16;
constexpr int OOB = 0x40000000;
constexpr unsigned SPIN_CAP = 200000, TAG_MASK = 1023u;

#define WAUX 2
#define KVAUX 2
#define THR 5
#define QG_AT 25
#define VB2 4
#define KPRE 2
#define CODEPF 1
#define CODE_BYTES 11264
#define ILV 1
#define ADAPT 0
#define ADAPT_INIT 2
#define PACE_A 30
#define PACE_T 160
#define PACE_D 25
#define PACE_MAX 3
#define HDRY 0
#define ODELAY 60
#define FDELAY 14
#define FSLEEP 0
#define TWN 8
#define OPOS 2
#define STAUX 16
#define QLAUX 16
#define PWAUX 16
#define TLAUX 16
#define KB0 10
#define KBURST 8
#define JSKIP 3
#define SLOTX 1
#define TPRE 0
#define PREWARM 1
#define PSLEEP 0
#define ABL 0

constexpr int WS_E = 0;
constexpr int WS_Q = 4096;
constexpr int WS_H = WS_Q + 8 * 4 * 8192;
constexpr int WS_O = WS_H + 28 * 4 * 2048;
constexpr int WS_F = WS_O + 16 * 16 * 4096;
constexpr int WS_END = WS_F + 2 * 28 * 16384;
constexpr int WS_DIAG = 8 << 20;

constexpr int OM_ROW = 68 * 4, FM_ROW = 544;
constexpr int L_QP = 0;
constexpr int L_Q = 8192;
constexpr int L_P = 9216;
constexpr int L_Z = 10240;
constexpr int L_CNT = 10304;
constexpr int L_TAG = 10320;
constexpr int L_HP = 10368;
constexpr int L_ST = 10432;
constexpr int L_OM = 10496;
constexpr int L_FM = L_OM + 32 * OM_ROW;
constexpr int L_END = L_FM + 32 * FM_ROW;

__device__ __forceinline__ rsrc_t mk(const void* p, int bytes) { return __builtin_amdgcn_make_buffer_rsrc((void*)p, 0, bytes, 0x00020000); }
template <int AUX> __device__ __forceinline__ v4u ld16(rsrc_t r, int vo, int so) { return __builtin_amdgcn_raw_buffer_load_b128(r, vo, so, AUX); }
__device__ __forceinline__ int oob_if(bool c) { return c ? OOB : 0; }
__device__ __forceinline__ float u2f(unsigned u) { return __builtin_bit_cast(float, u); }
__device__ __forceinline__ unsigned f2u(float f) { return __builtin_bit_cast(unsigned, f); }
__device__ __forceinline__ unsigned tagged(float v, unsigned t, unsigned keep) { return (f2u(v) & keep) | t; }
__device__ __forceinline__ void publish(rsrc_t r, int vo, float a, float b, float c, float d, unsigned t, unsigned keep = ~TAG_MASK) {
  __builtin_amdgcn_raw_buffer_store_b128(v4u{tagged(a, t, keep), tagged(b, t, keep), tagged(c, t, keep), tagged(d, t, keep)}, r, vo, 0, STAUX);
}
__device__ __forceinline__ unsigned untag(v4u& g, unsigned t) { g ^= t; return (g.x | g.y | g.z | g.w) & TAG_MASK; }
__device__ __forceinline__ bool wave_none(bool p) { return __builtin_amdgcn_ballot_w64(p) == 0; }
__device__ __forceinline__ float dot2(unsigned a, unsigned b, float c) {
  return __builtin_amdgcn_fdot2_f32_bf16(__builtin_bit_cast(bf16x2, a), __builtin_bit_cast(bf16x2, b), c, false);
}
__device__ __forceinline__ unsigned pkbf(float lo, float hi) {
  const bf16x2 v = {(__bf16)lo, (__bf16)hi};
  return __builtin_bit_cast(unsigned, v);
}
__device__ __forceinline__ unsigned perm_lo(unsigned a, unsigned b) { return __builtin_amdgcn_perm(b, a, 0x05040100u); }
__device__ __forceinline__ unsigned perm_hi(unsigned a, unsigned b) { return __builtin_amdgcn_perm(b, a, 0x07060302u); }
template <int CTRL> __device__ __forceinline__ unsigned dppu(unsigned x) { return (unsigned)__builtin_amdgcn_mov_dpp((int)x, CTRL, 0xf, 0xf, true); }
template <int CTRL> __device__ __forceinline__ float dpp(float x) { return u2f(dppu<CTRL>(f2u(x))); }
constexpr int XOR1 = 0xB1, XOR2 = 0x4E, XOR7 = 0x141, ROR8 = 0x128, ROR4 = 0x124;
__device__ __forceinline__ float fmx(float a, float c) { return __builtin_amdgcn_fmed3f(a, c, __builtin_inff()); }
__device__ __forceinline__ float fold16(float a, float b) {
  auto s = __builtin_amdgcn_permlane16_swap(f2u(a), f2u(b), false, false);
  return u2f(s[0]) + u2f(s[1]);
}
__device__ __forceinline__ float fold32(float a, float b) {
  auto s = __builtin_amdgcn_permlane32_swap(f2u(a), f2u(b), false, false);
  return u2f(s[0]) + u2f(s[1]);
}
__device__ __forceinline__ float x16_sum(float x) { return fold16(x, x); }
__device__ __forceinline__ float x32_sum(float x) { return fold32(x, x); }
__device__ __forceinline__ float x16_max(float x) {
  auto s = __builtin_amdgcn_permlane16_swap(f2u(x), f2u(x), false, false);
  return fmx(u2f(s[0]), u2f(s[1]));
}
__device__ __forceinline__ float x32_max(float x) {
  auto s = __builtin_amdgcn_permlane32_swap(f2u(x), f2u(x), false, false);
  return fmx(u2f(s[0]), u2f(s[1]));
}
__device__ __forceinline__ float row_sum(float x) { x += dpp<XOR1>(x); x += dpp<XOR2>(x); x += dpp<ROR4>(x); x += dpp<ROR8>(x); return x; }
__device__ __forceinline__ float row_max(float x) {
  x = fmx(x, dpp<XOR1>(x)); x = fmx(x, dpp<XOR2>(x)); x = fmx(x, dpp<ROR4>(x)); x = fmx(x, dpp<ROR8>(x)); return x;
}
__device__ __forceinline__ f32x4 mfma32(v4u a, v4u b, f32x4 c) {
  return __builtin_amdgcn_mfma_f32_16x16x32_bf16(__builtin_bit_cast(bf16x8, a), __builtin_bit_cast(bf16x8, b), c, 0, 0, 0);
}
__device__ __forceinline__ f32x4 mfma16(v2u a, v2u b, f32x4 c) {
  return __builtin_amdgcn_mfma_f32_16x16x16bf16_1k(__builtin_bit_cast(s16x4, a), __builtin_bit_cast(s16x4, b), c, 0, 0, 0);
}

#define SB() __builtin_amdgcn_sched_barrier(0)
#define REREAD(off) asm volatile("" : "+v"(off) :: "memory")
#define VMWAIT(n) __builtin_amdgcn_s_waitcnt(((n) & 15) | (7 << 4) | (15 << 8) | ((((n) >> 4) & 3) << 14))
#define THROTTLE() do { SB(); VMWAIT(THR); SB(); } while (0)
#define PACE(k) do { } while (0)
#define LDSBAR() do { SB(); __builtin_amdgcn_fence(__ATOMIC_RELEASE, "workgroup", "local"); __builtin_amdgcn_s_barrier(); \
                      __builtin_amdgcn_fence(__ATOMIC_ACQUIRE, "workgroup", "local"); SB(); } while (0)

__device__ __forceinline__ void lds_arrive(unsigned char* lds, int off, int lane) {
  __builtin_amdgcn_fence(__ATOMIC_RELEASE, "workgroup", "local");
  if (lane == 0) __hip_atomic_fetch_add((lds_u32*)(lds + off), 1u, __ATOMIC_RELAXED, __HIP_MEMORY_SCOPE_WORKGROUP);
}
__device__ __forceinline__ void lds_wait(unsigned char* lds, int off, unsigned want) {
  _Pragma("nounroll") for (unsigned spin = 0; spin < 16u * SPIN_CAP; ++spin) {
    if ((unsigned)__builtin_amdgcn_readfirstlane(__hip_atomic_load((lds_u32*)(lds + off), __ATOMIC_RELAXED, __HIP_MEMORY_SCOPE_WORKGROUP)) >= want) break;
    __builtin_amdgcn_s_sleep(1);
  }
  __builtin_amdgcn_fence(__ATOMIC_ACQUIRE, "workgroup", "local");
}

#define STAMP(k) do { } while (0)

__global__ void __launch_bounds__(512, 2)
gated_mix_decode_kernel(const unsigned short*, const unsigned short*, const unsigned short*, const unsigned short*, const unsigned short*,
                        const unsigned short*, const unsigned short*, const unsigned short*, unsigned short*, unsigned char*) {
  __shared__ __attribute__((aligned(128))) unsigned char lds[L_END];
  STAMP(0);
  v16u ka; v4u kb;
  asm volatile("s_load_dwordx16 %0, %2, 0x0\n\ts_load_dwordx4 %1, %2, 0x40" : "=&s"(ka), "=&s"(kb) : "s"(__builtin_amdgcn_kernarg_segment_ptr()) : "memory");
  SB();
  unsigned cpf;
  {
    unsigned long long pc;
    asm volatile("s_getpc_b64 %0" : "=s"(pc));
    const int line = ((blockIdx.x >> 3) & 3) * 24 + threadIdx.x;
    cpf = __builtin_amdgcn_raw_buffer_load_b32(mk((const void*)pc, CODE_BYTES), (line * 128) | oob_if(threadIdx.x >= 24), 0, 0);
    SB();
  }
  const int id = blockIdx.x, kvh = id & 7, b = (id >> 3) & 3, j = id >> 5;
  const int lane = threadIdx.x & 63, wv = __builtin_amdgcn_readfirstlane(threadIdx.x >> 6), w = wv & 3;
  const bool helper = wv < 4;
  const int blk0 = j * 16384 + w * 4096;
  int fo;
  {
    const int base = helper ? (112 * b + 32 * w) * (NQ * 2) + (kvh * 8 + j) * 128 : blk0;
    const int rstr = helper ? 4 * NQ * 2 : 512, rlim = wv == 3 ? 4 : 8;
    fo = (base + (lane >> 3) * rstr + (lane & 7) * 16) | oob_if((lane >> 3) >= rlim);
    asm volatile("" : "+v"(fo));
  }
  asm volatile("s_waitcnt lgkmcnt(0)" : "+s"(ka), "+s"(kb) :: "memory");
#define KARG(T, lo, hi) ((T)(((unsigned long long)(hi) << 32) | (lo)))
  const unsigned short* x = KARG(const unsigned short*, ka[0], ka[1]);
  const unsigned short* kc = KARG(const unsigned short*, ka[2], ka[3]);
  const unsigned short* vc = KARG(const unsigned short*, ka[4], ka[5]);
  const unsigned short* wq = KARG(const unsigned short*, ka[6], ka[7]);
  const unsigned short* wo = KARG(const unsigned short*, ka[8], ka[9]);
  const unsigned short* w1 = KARG(const unsigned short*, ka[10], ka[11]);
  const unsigned short* w2 = KARG(const unsigned short*, ka[12], ka[13]);
  const unsigned short* gw = KARG(const unsigned short*, ka[14], ka[15]);
  unsigned short* out = KARG(unsigned short*, kb[0], kb[1]);
  unsigned char* ws = KARG(unsigned char*, kb[2], kb[3]);
  const size_t head = ((size_t)(id & 31) * SK + (SK - WIN)) * DH;
  const rsrc_t rF = mk(helper ? wq : kc + head, helper ? DM * NQ * 2 : WIN * DH * 2);
  const int fstep = helper ? NQ * 2 : 128;
  v4u first[4];
#pragma unroll
  for (int i = 0; i < 4; ++i) first[i] = ld16<2>(rF, fo, i * fstep);
  SB();
  const int j1 = j == 7 ? JSKIP : j;
  const int bp = b >> 1, r16 = (b & 1) * 8 + j, pu = kvh * 16 + r16, fs = kvh * 14 + r16;
  const int m16 = lane & 15, g4 = lane >> 4;
  const rsrc_t rW = mk(ws, WS_END);
  const f32x4 zero4 = {0.f, 0.f, 0.f, 0.f};

  if (helper) {
    const int tid = threadIdx.x;
    const rsrc_t r1 = mk(w1, DM * DFF * 2), rX = mk(x, NB * DM * 2), rG = mk(gw, DM * 2);
    const v4u (&wqr)[4] = first;
    v4u w1r[2];
    v2u xq, xh;
    {
      const int rr = 2 * g4 + (m16 & 3), xrow = (m16 >> 2) * DM;
      const bool off = (m16 & 2) != 0;
      xq = __builtin_amdgcn_raw_buffer_load_b64(rX, ((xrow + 112 * b + 32 * w + 4 * rr) * 2) | oob_if(off | (32 * w + 4 * rr >= 112)), 0, 0);
      xh.x = __builtin_amdgcn_raw_buffer_load_b32(rX, ((xrow + 56 * kvh + 16 * w + 2 * rr) * 2) | oob_if(off | (16 * w + 2 * rr >= 56)), 0, 0);
      xh.y = 0u;
    }
    const unsigned ep = __builtin_amdgcn_raw_buffer_load_b32(rW, WS_E, 0, AUX_SC1);
    SB();
    {
      const int row1 = 16 * w + 2 * (lane >> 3);
      const int o1 = ((56 * kvh + row1) * (DFF * 2) + (4 * j1 + b) * 128 + (lane & 7) * 16) | oob_if((j == JSKIP) | (row1 >= 56));
#pragma unroll
      for (int i = 0; i < 2; ++i) w1r[i] = ld16<WAUX>(r1, o1, i * (DFF * 2));
    }
    SB();
    STAMP(11);
    if (tid < 20) *(unsigned*)(lds + L_Z + tid * 4) = 0u;
    LDSBAR();
    {
      float qv[8], hv[8];
#pragma unroll
      for (int dw = 0; dw < 4; ++dw) {
        const v2u blo = {perm_lo(wqr[0][dw], wqr[1][dw]), perm_lo(wqr[2][dw], wqr[3][dw])};
        const v2u bhi = {perm_hi(wqr[0][dw], wqr[1][dw]), perm_hi(wqr[2][dw], wqr[3][dw])};
        const f32x4 dl = mfma16(xq, blo, zero4), dh = mfma16(xq, bhi, zero4);
        const float sl = (lane & 8) ? dl[1] : dl[0], sh = (lane & 8) ? dh[1] : dh[0];
        qv[2 * dw] = sl + dpp<ROR8>(sl);
        qv[2 * dw + 1] = sh + dpp<ROR8>(sh);
      }
#pragma unroll
      for (int dw = 0; dw < 4; ++dw) {
        const v2u blo = {perm_lo(w1r[0][dw], w1r[1][dw]), 0u}, bhi = {perm_hi(w1r[0][dw], w1r[1][dw]), 0u};
        const f32x4 dl = mfma16(xh, blo, zero4), dh = mfma16(xh, bhi, zero4);
        const float sl = (lane & 8) ? dl[1] : dl[0], sh = (lane & 8) ? dh[1] : dh[0];
        hv[2 * dw] = sl + dpp<ROR8>(sl);
        hv[2 * dw + 1] = sh + dpp<ROR8>(sh);
      }
      const bool up = (lane & 8) != 0;
      f32x4* p = (f32x4*)(lds + L_QP + ((w * 4 + g4) * 128 + 8 * m16) * 4);
      p[0] = up ? f32x4{hv[0], hv[1], hv[2], hv[3]} : f32x4{qv[0], qv[1], qv[2], qv[3]};
      p[1] = up ? f32x4{hv[4], hv[5], hv[6], hv[7]} : f32x4{qv[4], qv[5], qv[6], qv[7]};
    }
    lds_arrive(lds, L_CNT, lane);
    asm volatile("" :: "v"(cpf));
    const unsigned t = ((unsigned)__builtin_amdgcn_readfirstlane(ep) + 1u) & TAG_MASK;
    STAMP(1);
    lds_wait(lds, L_CNT, 4);
    {
      f32x4 s = zero4;
#pragma unroll
      for (int ww = 0; ww < 4; ++ww) s += *(const f32x4*)(lds + L_QP + ((ww * 4 + w) * 128 + 4 * (lane & 31)) * 4);
      const int dst = lane < 16 ? WS_Q + (kvh * 4 + w) * 8192 + j * 1024 + b * 256 + lane * 16
                    : (lane < 32 && j != JSKIP) ? WS_H + ((4 * j1 + b) * 4 + w) * 2048 + kvh * 256 + m16 * 16 : OOB;
      publish(rW, (ABL & 8) ? OOB : dst, s[0], s[1], s[2], s[3], t);
    }
    SB();
    STAMP(2);
    STAMP(3);
    lds_wait(lds, L_CNT + 8, 1);
    STAMP(5);
    {
      const int go_ = (lane * 16) | oob_if((lane >= 56) | (w != 3) | ((ABL & 8192) != 0));
      const v4u xg0 = ld16<0>(rX, go_, 2 * bp * DM * 2), xg1 = ld16<0>(rX, go_, (2 * bp + 1) * DM * 2), gg = ld16<0>(rG, go_, 0);
      SB();
      int qgo = (ABL & (2 | 16384)) ? OOB : WS_Q + (kvh * 4 + b) * 8192 + (2 * w + (lane >> 5)) * 1024 + (g4 & 1) * 256 + m16 * 16;
      const bool hon = w == 3 && r16 < 14;
      int hgo = (hon && !(ABL & (2 | 4096))) ? WS_H + ((fs >> 2) * 4 + 2 * bp + (lane >> 5)) * 2048 + ((lane >> 2) & 7) * 256 + (fs & 3) * 64 + (lane & 3) * 16 : OOB;
      v4u g0, g1, gh;
      _Pragma("nounroll") for (unsigned spin = 0; spin < SPIN_CAP; ++spin) {
        REREAD(qgo); REREAD(hgo);
        g0 = ld16<QLAUX>(rW, qgo, 0);
        g1 = ld16<QLAUX>(rW, qgo, 512);
        gh = ld16<QLAUX>(rW, hgo, 0);
        const unsigned mq = untag(g0, t) | untag(g1, t), mh = untag(gh, t);
        if ((ABL & 1) || wave_none((mq | (hon ? mh : 0u)) != 0u)) break;
        __builtin_amdgcn_s_sleep(8);
      }
      float q[4];
#pragma unroll
      for (int k = 0; k < 4; ++k) q[k] = x16_sum(u2f(g0[k]) + u2f(g1[k]));
      if ((g4 & 1) == 0) *(v2u*)(lds + L_Q + ((2 * w + (lane >> 5)) * 64 + 4 * m16) * 2) = v2u{pkbf(q[0], q[1]), pkbf(q[2], q[3])};
      if (w == 3) {
        float d0 = 0.f, d1 = 0.f;
#pragma unroll
        for (int k = 0; k < 4; ++k) { d0 = dot2(xg0[k], gg[k], d0); d1 = dot2(xg1[k], gg[k], d1); }
        const float gate = 1.f / (1.f + __expf(-x16_sum(row_sum(fold32(d0, d1)))));
        if ((lane & 31) == 0) *(float*)(lds + L_TAG + 4 + 4 * (lane >> 5)) = gate;
        if (lane == 1) *(unsigned*)(lds + L_TAG) = t;
        float h[4];
#pragma unroll
        for (int k = 0; k < 4; ++k) {
          float a = hon ? u2f(gh[k]) : 0.f;
          a += dpp<ROR4>(a);
          a += dpp<ROR8>(a);
          h[k] = fmaxf(x16_sum(a), 0.f) * (1.f - gate);
        }
        if ((lane & 28) == 0) *(v2u*)(lds + L_HP + (lane >> 5) * 32 + (lane & 3) * 8) = v2u{pkbf(h[0], h[1]), pkbf(h[2], h[3])};
      }
    }
    STAMP(4);
    LDSBAR();
    if (PREWARM) {
      const int po = WS_O + ((bp * 8 + kvh) * 16 + r16) * 4096 + (lane & 31) * 128;
      const int pf = WS_F + (bp * 28 + (pu >> 2)) * 16384 + (w * 32 + (lane & 31)) * 128;
      const unsigned pw = __builtin_amdgcn_raw_buffer_load_b32(rW, lane < 32 ? (w == 0 ? po : OOB) : (((PREWARM & 2) && pu < 112) ? pf : OOB), 0, PWAUX);
      asm volatile("" :: "v"(pw));
    }
    lds_wait(lds, L_CNT + 4, 4);
    float lse;
    f32x4 fc;
    {
      const int h = tid >> 7;
      const f32x4 e0 = *(const f32x4*)(lds + L_ST + h * 32), e1 = *(const f32x4*)(lds + L_ST + h * 32 + 16);
      const float M = fmx(fmx(e0[0], e0[2]), fmx(e1[0], e1[2]));
      fc = f32x4{__builtin_amdgcn_exp2f(e0[0] - M), __builtin_amdgcn_exp2f(e0[2] - M), __builtin_amdgcn_exp2f(e1[0] - M), __builtin_amdgcn_exp2f(e1[2] - M)};
      const float L = (fc[0] * e0[1] + fc[1] * e0[3]) + (fc[2] * e1[1] + fc[3] * e1[3]);
      fc *= 1.f / L;
      lse = M + __builtin_amdgcn_logf(L);
    }
    float lse_hi = u2f(f2u(lse) & ~TAG_MASK), lse_lo = lse - lse_hi;
    int oa = L_OM + 2 * (tid & 3) * OM_ROW + (tid >> 2) * 4, fa = L_FM + 2 * (tid & 3) * FM_ROW + (tid >> 2) * 8;
    int odst = (ABL & 128) ? OOB : WS_O + ((bp * 8 + kvh) * 32 + (tid >> 3)) * 2048 + (r16 ^ (SLOTX ? ((tid >> 3) & 7) : 0)) * 128 + (tid & 7) * 16;
    int fdst = tid < 224 ? WS_F + (bp * 28 + (tid >> 3)) * 16384 + (pu ^ (SLOTX ? ((tid >> 3) & 7) : 0)) * 128 + (tid & 7) * 16 : OOB;
    asm volatile("" : "+v"(lse_hi), "+v"(lse_lo), "+v"(fc), "+v"(oa), "+v"(fa), "+v"(odst), "+v"(fdst));
    STAMP(6);
    int pass = HDRY ? 0 : 1;
    unsigned tv = t, keep = ~TAG_MASK;
    asm volatile("" : "+s"(pass), "+v"(tv), "+s"(keep));
    _Pragma("nounroll") for (; pass < 2; ++pass) {
      const int live = pass ? 0 : OOB;
      if (pass) LDSBAR();
      {
        float a = 0.f, c = 0.f;
#pragma unroll
        for (int ww = 0; ww < 4; ++ww) {
          a = fmaf(fc[ww], *(const float*)(lds + oa + ww * 8 * OM_ROW), a);
          c = fmaf(fc[ww], *(const float*)(lds + oa + ww * 8 * OM_ROW + OM_ROW), c);
        }
        publish(rW, odst | live, a, c, lse_hi, lse_lo, tv, keep);
      }
      STAMP(7);
      if (pass) LDSBAR();
      {
        f32x2 s0 = {0.f, 0.f}, s1 = {0.f, 0.f};
#pragma unroll
        for (int ww = 0; ww < 4; ++ww) {
          s0 += *(const f32x2*)(lds + fa + ww * 8 * FM_ROW);
          s1 += *(const f32x2*)(lds + fa + ww * 8 * FM_ROW + FM_ROW);
        }
        publish(rW, fdst | live, s0.x, s0.y, s1.x, s1.y, tv, keep);
        SB();
        if (pass) lds_arrive(lds, L_CNT + 12, lane);
      }
    }
    STAMP(9);
  } else {
    const rsrc_t rV = mk(vc + head, WIN * DH * 2);
    constexpr int BSTR = 131072;
    v4u kr[32], vr[32];
#define KLOAD(i) kr[i] = ld16<KVAUX>(rF, fo, ((i) >> 2) * BSTR + ((i) & 3) * 128)
#pragma unroll
    for (int i = 0; i < 4; ++i) kr[i] = first[i];
    STAMP(1);
#pragma unroll
    for (int i = 4; i < 32; ++i) {
      if (i < 8 && i >= KBURST) { SB(); VMWAIT(KBURST - 1); SB(); } else if (i >= 8) THROTTLE();
      KLOAD(i);
      if (i >= 11 && (i & 3) == 3) PACE(i);
      if (i == KB0) { LDSBAR(); STAMP(2); }
      if (i == QG_AT && w == 0) lds_arrive(lds, L_CNT + 8, lane);
    }
    const int vo = blk0 + lane * 16;
#define VLOAD(i) vr[i] = ld16<KVAUX>(rV, vo, ((i) >> 2) * BSTR + ((i) & 3) * 1024)
#define VSTEP(i) do { THROTTLE(); VLOAD(i); SB(); if (((i) & 3) == 3) PACE(32 + (i)); } while (0)
    asm volatile("" :: "v"(cpf));
    STAMP(3);
#pragma unroll
    for (int i = 0; i < VB2; ++i) VSTEP(i);
    LDSBAR();
    STAMP(4);
    v4u pa[4];
    {
      v4u qf[2][4];
#pragma unroll
      for (int h = 0; h < 2; ++h)
#pragma unroll
        for (int t = 0; t < 4; ++t) qf[h][t] = *(const v4u*)(lds + L_Q + (h * 256 + 64 * t + 8 * (lane & 7)) * 2);
      VSTEP(VB2);
      float s[8][2];
#pragma unroll
      for (int i = 0; i < 8; i += 2) {
        float a0 = 0.f, a1 = 0.f, c0 = 0.f, c1 = 0.f;
#pragma unroll
        for (int t = 0; t < 4; ++t) {
#pragma unroll
          for (int dw = 0; dw < 4; ++dw) {
            a0 = dot2(kr[4 * i + t][dw], qf[0][t][dw], a0); c0 = dot2(kr[4 * i + 4 + t][dw], qf[0][t][dw], c0);
            a1 = dot2(kr[4 * i + t][dw], qf[1][t][dw], a1); c1 = dot2(kr[4 * i + 4 + t][dw], qf[1][t][dw], c1);
          }
          if (t == 1) VSTEP(VB2 + 1 + i);
        }
        s[i][0] = a0; s[i][1] = a1; s[i + 1][0] = c0; s[i + 1][1] = c1;
        VSTEP(VB2 + 2 + i);
      }
#pragma unroll
      for (int i = 0; i < 4; ++i)
#pragma unroll
        for (int h = 0; h < 2; ++h) {
          const float lo = s[i][h] + dpp<XOR7>(s[i][h]), hi = s[i + 4][h] + dpp<XOR7>(s[i + 4][h]);
          s[i][h] = (lane & 4) ? hi : lo;
        }
#pragma unroll
      for (int i = 0; i < 2; ++i)
#pragma unroll
        for (int h = 0; h < 2; ++h) {
          const float lo = s[i][h] + dpp<XOR2>(s[i][h]), hi = s[i + 2][h] + dpp<XOR2>(s[i + 2][h]);
          s[i][h] = (lane & 2) ? hi : lo;
        }
#pragma unroll
      for (int h = 0; h < 2; ++h) {
        const float lo = s[0][h] + dpp<XOR1>(s[0][h]), hi = s[1][h] + dpp<XOR1>(s[1][h]);
        s[0][h] = (lane & 1) ? hi : lo;
      }
      VSTEP(VB2 + 9);
      constexpr float C2 = 0.0625f * 1.4426950408889634f;
      const float z0 = s[0][0] * C2, z1 = s[0][1] * C2;
      const float m0 = x32_max(x16_max(row_max(z0))), m1 = x32_max(x16_max(row_max(z1)));
      const float p0 = __builtin_amdgcn_exp2f(z0 - m0), p1 = __builtin_amdgcn_exp2f(z1 - m1);
      const unsigned pp = pkbf(p0, p1);
      unsigned short* pl = (unsigned short*)(lds + L_P + w * 256 + ((lane >> 3) & 1) * 64 + (lane & 7) * 8 + (lane >> 4) * 2);
      pl[0] = (unsigned short)pp;
      pl[64] = (unsigned short)(pp >> 16);
      VSTEP(VB2 + 10);
      const float l01 = x16_sum(row_sum(fold32(p0, p1)));
      if ((lane & 31) == 0) *(f32x2*)(lds + L_ST + ((lane >> 5) * 4 + w) * 8) = f32x2{lane ? m1 : m0, l01};
      lds_arrive(lds, L_CNT + 4, lane);
      const bool valid = (m16 & 3) == 0 && ((m16 >> 2) & 1) == (g4 & 1);
      const int a = valid ? L_P + w * 256 + (m16 >> 3) * 128 + (g4 >> 1) * 64 : L_Z;
#pragma unroll
      for (int G = 0; G < 4; ++G) pa[G] = *(const v4u*)(lds + a + G * 16);
    }
    STAMP(5);
    f32x4 acc[8];
#pragma unroll
    for (int e = 0; e < 8; ++e) acc[e] = zero4;
    v4u tr[12], bf[8];
    const rsrc_t rO = mk(wo, NQ * DM * 2), r2 = mk(w2, DFF * DM * 2);
    const int o2 = (r16 < 14 && lane < 56) ? (16 * fs + 4 * w) * (DM * 2) + lane * 16 : OOB;
    const int oo = lane < 56 ? (kvh * 512 + 32 * r16 + 8 * w) * (DM * 2) + lane * 16 : OOB;
#define TLOAD(i) tr[i] = (i) < 4 ? ld16<WAUX>(r2, o2, (i) * (DM * 2)) : ld16<WAUX>(rO, oo, ((i) - 4) * (DM * 2))
#define TSTEP(i) do { THROTTLE(); TLOAD(i); SB(); } while (0)
#define PAIRUP(src, pr) do { _Pragma("unroll") for (int dw = 0; dw < 4; ++dw) { \
      bf[2 * dw][pr] = perm_lo((src)[2 * (pr)][dw], (src)[2 * (pr) + 1][dw]); \
      bf[2 * dw + 1][pr] = perm_hi((src)[2 * (pr)][dw], (src)[2 * (pr) + 1][dw]); } } while (0)
#define PIN_PAIR(pr) do { _Pragma("unroll") for (int e = 0; e < 8; ++e) { unsigned tmp_ = bf[e][pr]; asm volatile("" : "+v"(tmp_)); bf[e][pr] = tmp_; } SB(); } while (0)
#define PVMMA(G) do { _Pragma("unroll") for (int e = 0; e < 8; ++e) acc[e] = mfma32(pa[G], bf[e], acc[e]); } while (0)
#define PVGROUP(G) do { PAIRUP(vr + 8 * (G), 0); PAIRUP(vr + 8 * (G), 1); PAIRUP(vr + 8 * (G), 2); PAIRUP(vr + 8 * (G), 3); PVMMA(G); SB(); } while (0)
    constexpr int TIN = TPRE, TWIN = (TIN + TWN < 12) ? TIN + TWN : 12, V0 = VB2 + 11;
#pragma unroll
    for (int i = V0; i < 32; ++i) {
      VSTEP(i);
#pragma unroll
      for (int k = 0; k < TIN; ++k)
        if (V0 + (k * (31 - V0)) / (TIN ? TIN : 1) == i) TSTEP(k);
      if (i == V0) PVGROUP(0);
      if (i == 24) PVGROUP(1);
      if (i == 31) PVGROUP(2);
    }
#pragma unroll
    for (int k = 0; k < 8; ++k) {
      if (k + TWN >= 8 && TIN + k + TWN - 8 < TWIN) { SB(); VMWAIT(TWN - 1); SB(); TLOAD(TIN + k + TWN - 8); SB(); }
      if (k == 1) { PAIRUP(vr + 24, 0); PIN_PAIR(0); }
      if (k == 3) { PAIRUP(vr + 24, 1); PIN_PAIR(1); }
      if (k == 5) { PAIRUP(vr + 24, 2); PIN_PAIR(2); }
    }
    int oma = L_OM + w * 8 * OM_ROW + lane * 4;
    asm volatile("" : "+v"(oma));
    SB();
    PAIRUP(vr + 24, 3);
    PVMMA(3);
    STAMP(6);
#pragma unroll
    for (int e = 0; e < 8; ++e) *(float*)(lds + oma + e * OM_ROW) = acc[e][0];
    LDSBAR();
    const unsigned tb3 = ODELAY > 0 ? (unsigned)__builtin_amdgcn_s_memrealtime() : 0u;
    STAMP(7);
#pragma unroll
    for (int i = TWIN; i < 12; ++i) TLOAD(i);
    SB();
    const bool diag = (m16 >> 2) == g4;
    const v4u tg = *(const v4u*)(lds + L_TAG);
    const v2u ah = *(const v2u*)(lds + ((diag && (m16 & 3) < 2) ? L_HP + (m16 & 3) * 32 + w * 8 : L_Z));
    const unsigned t = tg.x;
#pragma unroll
    for (int dw = 0; dw < 4; ++dw) {
      acc[2 * dw] = mfma16(ah, v2u{perm_lo(tr[0][dw], tr[1][dw]), perm_lo(tr[2][dw], tr[3][dw])}, zero4);
      acc[2 * dw + 1] = mfma16(ah, v2u{perm_hi(tr[0][dw], tr[1][dw]), perm_hi(tr[2][dw], tr[3][dw])}, zero4);
    }
    SB();
    int ogo = WS_O + (((bp * 8 + kvh) * 16 + r16) * 2 + (w >> 1)) * 2048 + (lane >> 2) * 128 + (w & 1) * 64 + (lane & 3) * 16;
    float gate = u2f(lane < 32 ? tg.y : tg.z);
    unsigned m0 = (diag && (lane & 1) == 0) ? 0xffffffffu : 0u, m1 = (diag && (lane & 1) != 0) ? 0xffffffffu : 0u;
    int fma_ = L_FM + w * 8 * FM_ROW + lane * 8;
    asm volatile("" : "+v"(gate), "+v"(m0), "+v"(m1), "+v"(fma_), "+v"(ogo));
    SB();
    if (OPOS >= 1) { PAIRUP(tr + 4, 0); PIN_PAIR(0); }
    if (OPOS >= 2) { PAIRUP(tr + 4, 1); PIN_PAIR(1); }
    if (OPOS >= 3) { PAIRUP(tr + 4, 2); PIN_PAIR(2); }
    if (OPOS >= 4) { PAIRUP(tr + 4, 3); PIN_PAIR(3); }
    if (ODELAY > 0) while ((int)((unsigned)__builtin_amdgcn_s_memrealtime() - tb3) < ODELAY) {}
    SB();
    v4u go = ld16<TLAUX>(rW, ogo, 0);
    SB();
    if (OPOS < 1) { PAIRUP(tr + 4, 0); PIN_PAIR(0); }
    if (OPOS < 2) { PAIRUP(tr + 4, 1); PIN_PAIR(1); }
    if (OPOS < 3) { PAIRUP(tr + 4, 2); PIN_PAIR(2); }
    if (OPOS < 4) { PAIRUP(tr + 4, 3); PIN_PAIR(3); }
    STAMP(11);
    if (!wave_none(untag(go, t) != 0u)) {
      _Pragma("nounroll") for (unsigned spin = 0; spin < SPIN_CAP; ++spin) {
        if (PSLEEP) __builtin_amdgcn_s_sleep(PSLEEP);
        REREAD(ogo);
        go = ld16<TLAUX>(rW, ogo, 0);
        if (wave_none(untag(go, t) != 0u)) break;
      }
    }
    STAMP(8);
    {
      const float lm = u2f(go.z) + u2f(go.w);
      float M = fmx(lm, dpp<ROR4>(lm));
      M = fmx(M, dpp<ROR8>(M));
      M = x16_max(M);
      float d = __builtin_amdgcn_exp2f(lm - M);
      float a = d * u2f(go.x), c = d * u2f(go.y);
      a += dpp<ROR4>(a); c += dpp<ROR4>(c); d += dpp<ROR4>(d);
      a += dpp<ROR8>(a); c += dpp<ROR8>(c); d += dpp<ROR8>(d);
      a = x16_sum(a); c = x16_sum(c);
      d = gate * __builtin_amdgcn_rcpf(x16_sum(d));
      const unsigned op = pkbf(a * d, c * d);
      const auto sw = __builtin_amdgcn_permlane32_swap(op, op, false, false);
      const v4u b0 = {dppu<0x00>(sw[0]), dppu<0x55>(sw[0]), dppu<0xAA>(sw[0]), dppu<0xFF>(sw[0])};
      const v4u b1 = {dppu<0x00>(sw[1]), dppu<0x55>(sw[1]), dppu<0xAA>(sw[1]), dppu<0xFF>(sw[1])};
      const v4u ao = (b0 & m0) | (b1 & m1);
#pragma unroll
      for (int e = 0; e < 8; ++e) acc[e] = mfma32(ao, bf[e], acc[e]);
#pragma unroll
      for (int e = 0; e < 8; ++e) *(f32x2*)(lds + fma_ + e * FM_ROW) = f32x2{acc[e][0], acc[e][1]};
    }
    LDSBAR();
    STAMP(9);
    if (w == 0 && pu < 112) {
      int fgo = WS_F + (bp * 28 + (pu >> 2)) * 16384 + (lane >> 1) * 128 + ((pu & 3) * 2 + (lane & 1)) * 16;
      const int oo2 = (lane & 14) == 0 ? ((2 * bp + (lane >> 5)) * DM + 4 * pu + 2 * (lane & 1) + (g4 & 1)) * 2 : OOB;
      v4u gf[4];
      lds_wait(lds, L_CNT + 12, 4);
      if (FDELAY) __builtin_amdgcn_s_sleep(FDELAY);
      _Pragma("nounroll") for (unsigned spin = 0; spin < SPIN_CAP; ++spin) {
        REREAD(fgo);
#pragma unroll
        for (int k = 0; k < 4; ++k) gf[k] = ld16<TLAUX>(rW, fgo, k * 4096);
        const unsigned miss = (untag(gf[0], t) | untag(gf[1], t)) | (untag(gf[2], t) | untag(gf[3], t));
        if (wave_none(miss != 0u)) break;
        if (FSLEEP) __builtin_amdgcn_s_sleep(FSLEEP);
      }
      float v[4];
#pragma unroll
      for (int k = 0; k < 4; ++k) v[k] = (u2f(gf[0][k]) + u2f(gf[1][k])) + (u2f(gf[2][k]) + u2f(gf[3][k]));
      float r = fold16(fold32(v[0], v[1]), fold32(v[2], v[3]));
      r += dpp<ROR8>(r); r += dpp<ROR4>(r); r += dpp<XOR2>(r);
      __builtin_amdgcn_raw_buffer_store_b16((unsigned short)pkbf(r, r), mk(out, NB * DM * 2), oo2, 0, 0);
      if (id == 0 && lane == 0) __builtin_amdgcn_raw_buffer_store_b32(t, rW, WS_E, 0, AUX_SC1);
    }
  }
}

}

extern "C" void kernel_launch(void* const* d_in, const int* in_sizes, int n_in, void* d_out, int out_size, void* d_ws, size_t ws_size,
                              hipStream_t stream) {
  (void)in_sizes; (void)n_in; (void)out_size; (void)ws_size;
  gated_mix_decode_kernel<<<256, 512, 0, stream>>>(
      (const unsigned short*)d_in[0], (const unsigned short*)d_in[1], (const unsigned short*)d_in[2], (const unsigned short*)d_in[3],
      (const unsigned short*)d_in[4], (const unsigned short*)d_in[6], (const unsigned short*)d_in[7], (const unsigned short*)d_in[8],
      (unsigned short*)d_out, (unsigned char*)d_ws);
}
```

```cpp
#include <hip/hip_runtime.h>
#include <hip/hip_bf16.h>
#include <cstdint>

namespace {

typedef unsigned v4u __attribute__((ext_vector_type(4)));
typedef unsigned v2u __attribute__((ext_vector_type(2)));
typedef unsigned v16u __attribute__((ext_vector_type(16)));
typedef float f32x4 __attribute__((ext_vector_type(4)));
typedef float f32x2 __attribute__((ext_vector_type(2)));
typedef short s16x4 __attribute__((ext_vector_type(4)));
typedef __bf16 bf16x8 __attribute__((ext_vector_type(8)));
typedef __bf16 bf16x2 __attribute__((ext_vector_type(2)));
typedef __amdgpu_buffer_rsrc_t rsrc_t;
typedef __attribute__((address_space(3))) unsigned lds_u32;

constexpr int NB = 4, DM = 448, HKV = 8, SK = 8192, DH = 256, WIN = 2048, DFF = 1792, NQ = 4096;
constexpr int AUX_SC1 = 16;
constexpr int OOB = 0x40000000;
constexpr unsigned SPIN_CAP = 200000, TAG_MASK = 1023u;

#define WAUX 2
#define KVAUX 2
#define THR 5
#define QG_AT 25
#define VB2 4
#define KPRE 2
#define CODEPF 1
#define CODE_BYTES 11264
#define ILV 1
#define ADAPT 0
#define ADAPT_INIT 2
#define PACE_A 30
#define PACE_T 160
#define PACE_D 25
#define PACE_MAX 3
#define HDRY 0
#define ODELAY 60
#define FDELAY 14
#define FSLEEP 0
#define TWN 8
#define OPOS 2
#define STAUX 16
#define QLAUX 16
#define PWAUX 16
#define TLAUX 16
#define KB0 10
#define KBURST 8
#define JSKIP 3
#define SLOTX 1
#define TLBW 2
#define TPRE 0
#define PREWARM 1
#define PSLEEP 0
#define ABL 0

constexpr int WS_E = 0;
constexpr int WS_Q = 4096;
constexpr int WS_H = WS_Q + 8 * 4 * 8192;
constexpr int WS_O = WS_H + 28 * 4 * 2048 + 4096;
constexpr int WS_F = WS_O + 16 * 16 * 4096;
constexpr int WS_END = WS_F + 2 * 28 * 16384;
constexpr int WS_DIAG = 8 << 20;

constexpr int OM_ROW = 68 * 4, FM_ROW = 544;
constexpr int L_QP = 0;
constexpr int L_Q = 8192;
constexpr int L_P = 9216;
constexpr int L_Z = 10240;
constexpr int L_CNT = 10304;
constexpr int L_TAG = 10320;
constexpr int L_HP = 10368;
constexpr int L_ST = 10432;
constexpr int L_OM = 10496;
constexpr int L_FM = L_OM + 32 * OM_ROW;
constexpr int L_END = L_FM + 32 * FM_ROW;

__device__ __forceinline__ rsrc_t mk(const void* p, int bytes) { return __builtin_amdgcn_make_buffer_rsrc((void*)p, 0, bytes, 0x00020000); }
template <int AUX> __device__ __forceinline__ v4u ld16(rsrc_t r, int vo, int so) { return __builtin_amdgcn_raw_buffer_load_b128(r, vo, so, AUX); }
__device__ __forceinline__ int oob_if(bool c) { return c ? OOB : 0; }
__device__ __forceinline__ float u2f(unsigned u) { return __builtin_bit_cast(float, u); }
__device__ __forceinline__ unsigned f2u(float f) { return __builtin_bit_cast(unsigned, f); }
__device__ __forceinline__ unsigned tagged(float v, unsigned t, unsigned keep) { return (f2u(v) & keep) | t; }
__device__ __forceinline__ void publish(rsrc_t r, int vo, float a, float b, float c, float d, unsigned t, unsigned keep = ~TAG_MASK) {
  __builtin_amdgcn_raw_buffer_store_b128(v4u{tagged(a, t, keep), tagged(b, t, keep), tagged(c, t, keep), tagged(d, t, keep)}, r, vo, 0, STAUX);
}
__device__ __forceinline__ unsigned untag(v4u& g, unsigned t) { g ^= t; return (g.x | g.y | g.z | g.w) & TAG_MASK; }
__device__ __forceinline__ bool wave_none(bool p) { return __builtin_amdgcn_ballot_w64(p) == 0; }
__device__ __forceinline__ float dot2(unsigned a, unsigned b, float c) {
  return __builtin_amdgcn_fdot2_f32_bf16(__builtin_bit_cast(bf16x2, a), __builtin_bit_cast(bf16x2, b), c, false);
}
__device__ __forceinline__ unsigned pkbf(float lo, float hi) {
  const bf16x2 v = {(__bf16)lo, (__bf16)hi};
  return __builtin_bit_cast(unsigned, v);
}
__device__ __forceinline__ unsigned perm_lo(unsigned a, unsigned b) { return __builtin_amdgcn_perm(b, a, 0x05040100u); }
__device__ __forceinline__ unsigned perm_hi(unsigned a, unsigned b) { return __builtin_amdgcn_perm(b, a, 0x07060302u); }
template <int CTRL> __device__ __forceinline__ unsigned dppu(unsigned x) { return (unsigned)__builtin_amdgcn_mov_dpp((int)x, CTRL, 0xf, 0xf, true); }
template <int CTRL> __device__ __forceinline__ float dpp(float x) { return u2f(dppu<CTRL>(f2u(x))); }
constexpr int XOR1 = 0xB1, XOR2 = 0x4E, XOR7 = 0x141, ROR8 = 0x128, ROR4 = 0x124;
__device__ __forceinline__ float fmx(float a, float c) { return __builtin_amdgcn_fmed3f(a, c, __builtin_inff()); }
__device__ __forceinline__ float fold16(float a, float b) {
  auto s = __builtin_amdgcn_permlane16_swap(f2u(a), f2u(b), false, false);
  return u2f(s[0]) + u2f(s[1]);
}
__device__ __forceinline__ float fold32(float a, float b) {
  auto s = __builtin_amdgcn_permlane32_swap(f2u(a), f2u(b), false, false);
  return u2f(s[0]) + u2f(s[1]);
}
__device__ __forceinline__ float x16_sum(float x) { return fold16(x, x); }
__device__ __forceinline__ float x32_sum(float x) { return fold32(x, x); }
__device__ __forceinline__ float x16_max(float x) {
  auto s = __builtin_amdgcn_permlane16_swap(f2u(x), f2u(x), false, false);
  return fmx(u2f(s[0]), u2f(s[1]));
}
__device__ __forceinline__ float x32_max(float x) {
  auto s = __builtin_amdgcn_permlane32_swap(f2u(x), f2u(x), false, false);
  return fmx(u2f(s[0]), u2f(s[1]));
}
__device__ __forceinline__ float row_sum(float x) { x += dpp<XOR1>(x); x += dpp<XOR2>(x); x += dpp<ROR4>(x); x += dpp<ROR8>(x); return x; }
__device__ __forceinline__ float row_max(float x) {
  x = fmx(x, dpp<XOR1>(x)); x = fmx(x, dpp<XOR2>(x)); x = fmx(x, dpp<ROR4>(x)); x = fmx(x, dpp<ROR8>(x)); return x;
}
__device__ __forceinline__ f32x4 mfma32(v4u a, v4u b, f32x4 c) {
  return __builtin_amdgcn_mfma_f32_16x16x32_bf16(__builtin_bit_cast(bf16x8, a), __builtin_bit_cast(bf16x8, b), c, 0, 0, 0);
}
__device__ __forceinline__ f32x4 mfma16(v2u a, v2u b, f32x4 c) {
  return __builtin_amdgcn_mfma_f32_16x16x16bf16_1k(__builtin_bit_cast(s16x4, a), __builtin_bit_cast(s16x4, b), c, 0, 0, 0);
}

#define SB() __builtin_amdgcn_sched_barrier(0)
#define REREAD(off) asm volatile("" : "+v"(off) :: "memory")
#define VMWAIT(n) __builtin_amdgcn_s_waitcnt(((n) & 15) | (7 << 4) | (15 << 8) | ((((n) >> 4) & 3) << 14))
#define THROTTLE() do { SB(); VMWAIT(THR); SB(); } while (0)
#define PACE(k) do { } while (0)
#define LDSBAR() do { SB(); __builtin_amdgcn_fence(__ATOMIC_RELEASE, "workgroup", "local"); __builtin_amdgcn_s_barrier(); \
                      __builtin_amdgcn_fence(__ATOMIC_ACQUIRE, "workgroup", "local"); SB(); } while (0)

__device__ __forceinline__ void lds_arrive(unsigned char* lds, int off, int lane) {
  __builtin_amdgcn_fence(__ATOMIC_RELEASE, "workgroup", "local");
  if (lane == 0) __hip_atomic_fetch_add((lds_u32*)(lds + off), 1u, __ATOMIC_RELAXED, __HIP_MEMORY_SCOPE_WORKGROUP);
}
__device__ __forceinline__ void lds_wait(unsigned char* lds, int off, unsigned want) {
  _Pragma("nounroll") for (unsigned spin = 0; spin < 16u * SPIN_CAP; ++spin) {
    if ((unsigned)__builtin_amdgcn_readfirstlane(__hip_atomic_load((lds_u32*)(lds + off), __ATOMIC_RELAXED, __HIP_MEMORY_SCOPE_WORKGROUP)) >= want) break;
    __builtin_amdgcn_s_sleep(1);
  }
  __builtin_amdgcn_fence(__ATOMIC_ACQUIRE, "workgroup", "local");
}

#define STAMP(k) do { } while (0)

struct TouchEntry { const void* p; unsigned stride, zero; };
struct Touch { TouchEntry e[32]; };

__global__ void __launch_bounds__(512, 2)
gated_mix_decode_kernel(const unsigned short*, const unsigned short*, const unsigned short*, const unsigned short*, const unsigned short*,
                        const unsigned short*, const unsigned short*, const unsigned short*, unsigned short*, unsigned char*, Touch) {
  __shared__ __attribute__((aligned(128))) unsigned char lds[L_END];
  STAMP(0);
  v16u ka; v4u kb;
  v4u tp;
  asm volatile("s_load_dwordx4 %2, %3, %4 offset:0x50\n\ts_load_dwordx16 %0, %3, 0x0\n\ts_load_dwordx4 %1, %3, 0x40"
               : "=&s"(ka), "=&s"(kb), "=&s"(tp) : "s"(__builtin_amdgcn_kernarg_segment_ptr()), "s"((blockIdx.x >> 3) * 16u) : "memory");
  SB();
  unsigned cpf;
  {
    unsigned long long pc;
    asm volatile("s_getpc_b64 %0" : "=s"(pc));
    const int line = ((blockIdx.x >> 3) & 3) * 24 + threadIdx.x;
    cpf = __builtin_amdgcn_raw_buffer_load_b32(mk((const void*)pc, CODE_BYTES), (line * 128) | oob_if(threadIdx.x >= 24), 0, 0);
    SB();
  }
  const int id = blockIdx.x, kvh = id & 7, b = (id >> 3) & 3, j = id >> 5;
  const int lane = threadIdx.x & 63, wv = __builtin_amdgcn_readfirstlane(threadIdx.x >> 6), w = wv & 3;
  const bool helper = wv < 4;
  const int blk0 = j * 16384 + w * 4096;
  int fo;
  {
    const int base = helper ? (112 * b + 32 * w) * (NQ * 2) + (kvh * 8 + j) * 128 : blk0;
    const int rstr = helper ? 4 * NQ * 2 : 512, rlim = wv == 3 ? 4 : 8;
    fo = (base + (lane >> 3) * rstr + (lane & 7) * 16) | oob_if((lane >> 3) >= rlim);
    asm volatile("" : "+v"(fo));
  }
  asm volatile("s_waitcnt lgkmcnt(0)" : "+s"(ka), "+s"(kb), "+s"(tp) :: "memory");
  const unsigned tw = __builtin_amdgcn_raw_buffer_load_b32(mk((const void*)(((unsigned long long)tp.y << 32) | tp.x), OOB), oob_if(threadIdx.x != 0), tp.z * kvh, 2);
  SB();
#define KARG(T, lo, hi) ((T)(((unsigned long long)(hi) << 32) | (lo)))
  const unsigned short* x = KARG(const unsigned short*, ka[0], ka[1]);
  const unsigned short* kc = KARG(const unsigned short*, ka[2], ka[3]);
  const unsigned short* vc = KARG(const unsigned short*, ka[4], ka[5]);
  const unsigned short* wq = KARG(const unsigned short*, ka[6], ka[7]);
  const unsigned short* wo = KARG(const unsigned short*, ka[8], ka[9]);
  const unsigned short* w1 = KARG(const unsigned short*, ka[10], ka[11]);
  const unsigned short* w2 = KARG(const unsigned short*, ka[12], ka[13]);
  const unsigned short* gw = KARG(const unsigned short*, ka[14], ka[15]);
  unsigned short* out = KARG(unsigned short*, kb[0], kb[1]);
  unsigned char* ws = KARG(unsigned char*, kb[2], kb[3]);
  const size_t head = ((size_t)(id & 31) * SK + (SK - WIN)) * DH;
  const rsrc_t rF = mk(helper ? wq : kc + head, helper ? DM * NQ * 2 : WIN * DH * 2);
  const int fstep = helper ? NQ * 2 : 128;
  v4u first[4];
#pragma unroll
  for (int i = 0; i < 4; ++i) first[i] = ld16<2>(rF, fo, i * fstep);
  SB();
  const int j1 = j == 7 ? JSKIP : j;
  const int bp = b >> 1, r16 = (b & 1) * 8 + j, pu = kvh * 16 + r16, fs = kvh * 14 + r16;
  const int m16 = lane & 15, g4 = lane >> 4;
  const rsrc_t rW = mk(ws, WS_END);
  const f32x4 zero4 = {0.f, 0.f, 0.f, 0.f};

  if (helper) {
    const int tid = threadIdx.x;
    const rsrc_t r1 = mk(w1, DM * DFF * 2), rX = mk(x, NB * DM * 2), rG = mk(gw, DM * 2);
    const v4u (&wqr)[4] = first;
    v4u w1r[2];
    v2u xq, xh;
    {
      const int rr = 2 * g4 + (m16 & 3), xrow = (m16 >> 2) * DM;
      const bool off = (m16 & 2) != 0;
      xq = __builtin_amdgcn_raw_buffer_load_b64(rX, ((xrow + 112 * b + 32 * w + 4 * rr) * 2) | oob_if(off | (32 * w + 4 * rr >= 112)), 0, 0);
      xh.x = __builtin_amdgcn_raw_buffer_load_b32(rX, ((xrow + 56 * kvh + 16 * w + 2 * rr) * 2) | oob_if(off | (16 * w + 2 * rr >= 56)), 0, 0);
      xh.y = 0u;
    }
    const unsigned ep = __builtin_amdgcn_raw_buffer_load_b32(rW, WS_E, 0, AUX_SC1);
    SB();
    {
      const int row1 = 16 * w + 2 * (lane >> 3);
      const int o1 = ((56 * kvh + row1) * (DFF * 2) + (4 * j1 + b) * 128 + (lane & 7) * 16) | oob_if((j == JSKIP) | (row1 >= 56));
#pragma unroll
      for (int i = 0; i < 2; ++i) w1r[i] = ld16<WAUX>(r1, o1, i * (DFF * 2));
    }
    SB();
    STAMP(11);
    if (tid < 20) *(unsigned*)(lds + L_Z + tid * 4) = 0u;
    LDSBAR();
    {
      float qv[8], hv[8];
#pragma unroll
      for (int dw = 0; dw < 4; ++dw) {
        const v2u blo = {perm_lo(wqr[0][dw], wqr[1][dw]), perm_lo(wqr[2][dw], wqr[3][dw])};
        const v2u bhi = {perm_hi(wqr[0][dw], wqr[1][dw]), perm_hi(wqr[2][dw], wqr[3][dw])};
        const f32x4 dl = mfma16(xq, blo, zero4), dh = mfma16(xq, bhi, zero4);
        const float sl = (lane & 8) ? dl[1] : dl[0], sh = (lane & 8) ? dh[1] : dh[0];
        qv[2 * dw] = sl + dpp<ROR8>(sl);
        qv[2 * dw + 1] = sh + dpp<ROR8>(sh);
      }
#pragma unroll
      for (int dw = 0; dw < 4; ++dw) {
        const v2u blo = {perm_lo(w1r[0][dw], w1r[1][dw]), 0u}, bhi = {perm_hi(w1r[0][dw], w1r[1][dw]), 0u};
        const f32x4 dl = mfma16(xh, blo, zero4), dh = mfma16(xh, bhi, zero4);
        const float sl = (lane & 8) ? dl[1] : dl[0], sh = (lane & 8) ? dh[1] : dh[0];
        hv[2 * dw] = sl + dpp<ROR8>(sl);
        hv[2 * dw + 1] = sh + dpp<ROR8>(sh);
      }
      const bool up = (lane & 8) != 0;
      f32x4* p = (f32x4*)(lds + L_QP + ((w * 4 + g4) * 128 + 8 * m16) * 4);
      p[0] = up ? f32x4{hv[0], hv[1], hv[2], hv[3]} : f32x4{qv[0], qv[1], qv[2], qv[3]};
      p[1] = up ? f32x4{hv[4], hv[5], hv[6], hv[7]} : f32x4{qv[4], qv[5], qv[6], qv[7]};
    }
    lds_arrive(lds, L_CNT, lane);
    asm volatile("" :: "v"(tw));
    asm volatile("" :: "v"(cpf));
    const unsigned t = ((unsigned)__builtin_amdgcn_readfirstlane(ep) + 1u) & TAG_MASK;
    STAMP(1);
    lds_wait(lds, L_CNT, 4);
    {
      f32x4 s = zero4;
#pragma unroll
      for (int ww = 0; ww < 4; ++ww) s += *(const f32x4*)(lds + L_QP + ((ww * 4 + w) * 128 + 4 * (lane & 31)) * 4);
      const int dst = lane < 16 ? WS_Q + (kvh * 4 + w) * 8192 + j * 1024 + b * 256 + lane * 16
                    : (lane < 32 && j != JSKIP) ? WS_H + ((4 * j1 + b) * 4 + w) * 2048 + kvh * 256 + m16 * 16 : OOB;
      publish(rW, (ABL & 8) ? OOB : dst, s[0], s[1], s[2], s[3], t);
    }
    SB();
    STAMP(2);
    STAMP(3);
    lds_wait(lds, L_CNT + 8, 1);
    STAMP(5);
    {
      const int go_ = (lane * 16) | oob_if((lane >= 56) | (w != 3) | ((ABL & 8192) != 0));
      const v4u xg0 = ld16<0>(rX, go_, 2 * bp * DM * 2), xg1 = ld16<0>(rX, go_, (2 * bp + 1) * DM * 2), gg = ld16<0>(rG, go_, 0);
      SB();
      int qgo = (ABL & (2 | 16384)) ? OOB : WS_Q + (kvh * 4 + b) * 8192 + (2 * w + (lane >> 5)) * 1024 + (g4 & 1) * 256 + m16 * 16;
      const bool hon = w == 3 && r16 < 14;
      int hgo = (hon && !(ABL & (2 | 4096))) ? WS_H + ((fs >> 2) * 4 + 2 * bp + (lane >> 5)) * 2048 + ((lane >> 2) & 7) * 256 + (fs & 3) * 64 + (lane & 3) * 16 : OOB;
      v4u g0, g1, gh;
      _Pragma("nounroll") for (unsigned spin = 0; spin < SPIN_CAP; ++spin) {
        REREAD(qgo); REREAD(hgo);
        g0 = ld16<QLAUX>(rW, qgo, 0);
        g1 = ld16<QLAUX>(rW, qgo, 512);
        gh = ld16<QLAUX>(rW, hgo, 0);
        const unsigned mq = untag(g0, t) | untag(g1, t), mh = untag(gh, t);
        if ((ABL & 1) || wave_none((mq | (hon ? mh : 0u)) != 0u)) break;
        __builtin_amdgcn_s_sleep(8);
      }
      float q[4];
#pragma unroll
      for (int k = 0; k < 4; ++k) q[k] = x16_sum(u2f(g0[k]) + u2f(g1[k]));
      if ((g4 & 1) == 0) *(v2u*)(lds + L_Q + ((2 * w + (lane >> 5)) * 64 + 4 * m16) * 2) = v2u{pkbf(q[0], q[1]), pkbf(q[2], q[3])};
      if (w == 3) {
        float d0 = 0.f, d1 = 0.f;
#pragma unroll
        for (int k = 0; k < 4; ++k) { d0 = dot2(xg0[k], gg[k], d0); d1 = dot2(xg1[k], gg[k], d1); }
        const float gate = 1.f / (1.f + __expf(-x16_sum(row_sum(fold32(d0, d1)))));
        if ((lane & 31) == 0) *(float*)(lds + L_TAG + 4 + 4 * (lane >> 5)) = gate;
        if (lane == 1) *(unsigned*)(lds + L_TAG) = t;
        float h[4];
#pragma unroll
        for (int k = 0; k < 4; ++k) {
          float a = hon ? u2f(gh[k]) : 0.f;
          a += dpp<ROR4>(a);
          a += dpp<ROR8>(a);
          h[k] = fmaxf(x16_sum(a), 0.f) * (1.f - gate);
        }
        if ((lane & 28) == 0) *(v2u*)(lds + L_HP + (lane >> 5) * 32 + (lane & 3) * 8) = v2u{pkbf(h[0], h[1]), pkbf(h[2], h[3])};
      }
    }
    STAMP(4);
    LDSBAR();
    if (PREWARM) {
      const int po = WS_O + ((bp * 8 + kvh) * 16 + r16) * 4096 + (lane & 31) * 128;
      const int pf = WS_F + (bp * 28 + (pu >> 2)) * 16384 + (w * 32 + (lane & 31)) * 128;
      const unsigned pw = __builtin_amdgcn_raw_buffer_load_b32(rW, lane < 32 ? (w == 0 ? po : OOB) : (((PREWARM & 2) && pu < 112) ? pf : OOB), 0, PWAUX);
      asm volatile("" :: "v"(pw));
    }
    lds_wait(lds, L_CNT + 4, 4);
    float lse;
    f32x4 fc;
    {
      const int h = tid >> 7;
      const f32x4 e0 = *(const f32x4*)(lds + L_ST + h * 32), e1 = *(const f32x4*)(lds + L_ST + h * 32 + 16);
      const float M = fmx(fmx(e0[0], e0[2]), fmx(e1[0], e1[2]));
      fc = f32x4{__builtin_amdgcn_exp2f(e0[0] - M), __builtin_amdgcn_exp2f(e0[2] - M), __builtin_amdgcn_exp2f(e1[0] - M), __builtin_amdgcn_exp2f(e1[2] - M)};
      const float L = (fc[0] * e0[1] + fc[1] * e0[3]) + (fc[2] * e1[1] + fc[3] * e1[3]);
      fc *= 1.f / L;
      lse = M + __builtin_amdgcn_logf(L);
    }
    float lse_hi = u2f(f2u(lse) & ~TAG_MASK), lse_lo = lse - lse_hi;
    int oa = L_OM + 2 * (tid & 3) * OM_ROW + (tid >> 2) * 4, fa = L_FM + 2 * (tid & 3) * FM_ROW + (tid >> 2) * 8;
    int odst = (ABL & 128) ? OOB : WS_O + ((bp * 8 + kvh) * 32 + (tid >> 3)) * 2048 + (r16 ^ (SLOTX ? ((tid >> 3) & 7) : 0)) * 128 + (tid & 7) * 16;
    int fdst = tid < 224 ? WS_F + (bp * 28 + (tid >> 3)) * 16384 + (pu ^ (SLOTX ? ((tid >> 3) & 7) : 0)) * 128 + (tid & 7) * 16 : OOB;
    asm volatile("" : "+v"(lse_hi), "+v"(lse_lo), "+v"(fc), "+v"(oa), "+v"(fa), "+v"(odst), "+v"(fdst));
    STAMP(6);
    int pass = HDRY ? 0 : 1;
    unsigned tv = t, keep = ~TAG_MASK;
    asm volatile("" : "+s"(pass), "+v"(tv), "+s"(keep));
    _Pragma("nounroll") for (; pass < 2; ++pass) {
      const int live = pass ? 0 : OOB;
      if (pass) LDSBAR();
      {
        float a = 0.f, c = 0.f;
#pragma unroll
        for (int ww = 0; ww < 4; ++ww) {
          a = fmaf(fc[ww], *(const float*)(lds + oa + ww * 8 * OM_ROW), a);
          c = fmaf(fc[ww], *(const float*)(lds + oa + ww * 8 * OM_ROW + OM_ROW), c);
        }
        publish(rW, odst | live, a, c, lse_hi, lse_lo, tv, keep);
      }
      STAMP(7);
      if (pass) LDSBAR();
      {
        f32x2 s0 = {0.f, 0.f}, s1 = {0.f, 0.f};
#pragma unroll
        for (int ww = 0; ww < 4; ++ww) {
          s0 += *(const f32x2*)(lds + fa + ww * 8 * FM_ROW);
          s1 += *(const f32x2*)(lds + fa + ww * 8 * FM_ROW + FM_ROW);
        }
        publish(rW, fdst | live, s0.x, s0.y, s1.x, s1.y, tv, keep);
        SB();
        if (pass) lds_arrive(lds, L_CNT + 12, lane);
      }
    }
    STAMP(9);
  } else {
    const rsrc_t rV = mk(vc + head, WIN * DH * 2);
    constexpr int BSTR = 131072;
    v4u kr[32], vr[32];
#define KLOAD(i) kr[i] = ld16<KVAUX>(rF, fo, ((i) >> 2) * BSTR + ((i) & 3) * 128)
#pragma unroll
    for (int i = 0; i < 4; ++i) kr[i] = first[i];
    STAMP(1);
#pragma unroll
    for (int i = 4; i < 32; ++i) {
      if (i < 8 && i >= KBURST) { SB(); VMWAIT(KBURST - 1); SB(); } else if (i >= 8) THROTTLE();
      KLOAD(i);
      if (i >= 11 && (i & 3) == 3) PACE(i);
      if (i == KB0) { LDSBAR(); STAMP(2); }
      if (i == QG_AT && w == 0) lds_arrive(lds, L_CNT + 8, lane);
    }
    const int vo = blk0 + lane * 16;
#define VLOAD(i) vr[i] = ld16<KVAUX>(rV, vo, ((i) >> 2) * BSTR + ((i) & 3) * 1024)
#define VSTEP(i) do { THROTTLE(); VLOAD(i); SB(); if (((i) & 3) == 3) PACE(32 + (i)); } while (0)
    asm volatile("" :: "v"(cpf));
    asm volatile("" :: "v"(tw));
    STAMP(3);
#pragma unroll
    for (int i = 0; i < VB2; ++i) VSTEP(i);
    LDSBAR();
    STAMP(4);
    v4u pa[4];
    {
      v4u qf[2][4];
#pragma unroll
      for (int h = 0; h < 2; ++h)
#pragma unroll
        for (int t = 0; t < 4; ++t) qf[h][t] = *(const v4u*)(lds + L_Q + (h * 256 + 64 * t + 8 * (lane & 7)) * 2);
      VSTEP(VB2);
      float s[8][2];
#pragma unroll
      for (int i = 0; i < 8; i += 2) {
        float a0 = 0.f, a1 = 0.f, c0 = 0.f, c1 = 0.f;
#pragma unroll
        for (int t = 0; t < 4; ++t) {
#pragma unroll
          for (int dw = 0; dw < 4; ++dw) {
            a0 = dot2(kr[4 * i + t][dw], qf[0][t][dw], a0); c0 = dot2(kr[4 * i + 4 + t][dw], qf[0][t][dw], c0);
            a1 = dot2(kr[4 * i + t][dw], qf[1][t][dw], a1); c1 = dot2(kr[4 * i + 4 + t][dw], qf[1][t][dw], c1);
          }
          if (t == 1) VSTEP(VB2 + 1 + i);
        }
        s[i][0] = a0; s[i][1] = a1; s[i + 1][0] = c0; s[i + 1][1] = c1;
        VSTEP(VB2 + 2 + i);
      }
#pragma unroll
      for (int i = 0; i < 4; ++i)
#pragma unroll
        for (int h = 0; h < 2; ++h) {
          const float lo = s[i][h] + dpp<XOR7>(s[i][h]), hi = s[i + 4][h] + dpp<XOR7>(s[i + 4][h]);
          s[i][h] = (lane & 4) ? hi : lo;
        }
#pragma unroll
      for (int i = 0; i < 2; ++i)
#pragma unroll
        for (int h = 0; h < 2; ++h) {
          const float lo = s[i][h] + dpp<XOR2>(s[i][h]), hi = s[i + 2][h] + dpp<XOR2>(s[i + 2][h]);
          s[i][h] = (lane & 2) ? hi : lo;
        }
#pragma unroll
      for (int h = 0; h < 2; ++h) {
        const float lo = s[0][h] + dpp<XOR1>(s[0][h]), hi = s[1][h] + dpp<XOR1>(s[1][h]);
        s[0][h] = (lane & 1) ? hi : lo;
      }
      VSTEP(VB2 + 9);
      constexpr float C2 = 0.0625f * 1.4426950408889634f;
      const float z0 = s[0][0] * C2, z1 = s[0][1] * C2;
      const float m0 = x32_max(x16_max(row_max(z0))), m1 = x32_max(x16_max(row_max(z1)));
      const float p0 = __builtin_amdgcn_exp2f(z0 - m0), p1 = __builtin_amdgcn_exp2f(z1 - m1);
      const unsigned pp = pkbf(p0, p1);
      unsigned short* pl = (unsigned short*)(lds + L_P + w * 256 + ((lane >> 3) & 1) * 64 + (lane & 7) * 8 + (lane >> 4) * 2);
      pl[0] = (unsigned short)pp;
      pl[64] = (unsigned short)(pp >> 16);
      VSTEP(VB2 + 10);
      const float l01 = x16_sum(row_sum(fold32(p0, p1)));
      if ((lane & 31) == 0) *(f32x2*)(lds + L_ST + ((lane >> 5) * 4 + w) * 8) = f32x2{lane ? m1 : m0, l01};
      lds_arrive(lds, L_CNT + 4, lane);
      const bool valid = (m16 & 3) == 0 && ((m16 >> 2) & 1) == (g4 & 1);
      const int a = valid ? L_P + w * 256 + (m16 >> 3) * 128 + (g4 >> 1) * 64 : L_Z;
#pragma unroll
      for (int G = 0; G < 4; ++G) pa[G] = *(const v4u*)(lds + a + G * 16);
    }
    STAMP(5);
    f32x4 acc[8];
#pragma unroll
    for (int e = 0; e < 8; ++e) acc[e] = zero4;
    v4u tr[12], bf[8];
    const rsrc_t rO = mk(wo, NQ * DM * 2), r2 = mk(w2, DFF * DM * 2);
    const int o2 = (r16 < 14 && lane < 56) ? (16 * fs + 4 * w) * (DM * 2) + lane * 16 : OOB;
    const int oo = lane < 56 ? (kvh * 512 + 32 * r16 + 8 * w) * (DM * 2) + lane * 16 : OOB;
#define TLOAD(i) tr[i] = (i) < 4 ? ld16<WAUX>(r2, o2, (i) * (DM * 2)) : ld16<WAUX>(rO, oo, ((i) - 4) * (DM * 2))
#define TSTEP(i) do { THROTTLE(); TLOAD(i); SB(); } while (0)
#define PAIRUP(src, pr) do { _Pragma("unroll") for (int dw = 0; dw < 4; ++dw) { \
      bf[2 * dw][pr] = perm_lo((src)[2 * (pr)][dw], (src)[2 * (pr) + 1][dw]); \
      bf[2 * dw + 1][pr] = perm_hi((src)[2 * (pr)][dw], (src)[2 * (pr) + 1][dw]); } } while (0)
#define PIN_PAIR(pr) do { _Pragma("unroll") for (int e = 0; e < 8; ++e) { unsigned tmp_ = bf[e][pr]; asm volatile("" : "+v"(tmp_)); bf[e][pr] = tmp_; } SB(); } while (0)
#define PVMMA(G) do { _Pragma("unroll") for (int e = 0; e < 8; ++e) acc[e] = mfma32(pa[G], bf[e], acc[e]); } while (0)
#define PVGROUP(G) do { PAIRUP(vr + 8 * (G), 0); PAIRUP(vr + 8 * (G), 1); PAIRUP(vr + 8 * (G), 2); PAIRUP(vr + 8 * (G), 3); PVMMA(G); SB(); } while (0)
    constexpr int TIN = TPRE, TWIN = (TIN + TWN < 12) ? TIN + TWN : 12, V0 = VB2 + 11;
#pragma unroll
    for (int i = V0; i < 32; ++i) {
      VSTEP(i);
#pragma unroll
      for (int k = 0; k < TIN; ++k)
        if (V0 + (k * (31 - V0)) / (TIN ? TIN : 1) == i) TSTEP(k);
      if (i == V0) PVGROUP(0);
      if (i == 24) PVGROUP(1);
      if (i == 31) PVGROUP(2);
    }
#pragma unroll
    for (int k = 0; k < 8; ++k) {
      if (k + TWN >= 8 && TIN + k + TWN - 8 < TWIN) { SB(); VMWAIT(TWN - 1); SB(); TLOAD(TIN + k + TWN - 8); SB(); }
      if (k == 1) { PAIRUP(vr + 24, 0); PIN_PAIR(0); }
      if (k == 3) { PAIRUP(vr + 24, 1); PIN_PAIR(1); }
      if (k == 5) { PAIRUP(vr + 24, 2); PIN_PAIR(2); }
    }
    int oma = L_OM + w * 8 * OM_ROW + lane * 4;
    asm volatile("" : "+v"(oma));
    SB();
    PAIRUP(vr + 24, 3);
    PVMMA(3);
    STAMP(6);
#pragma unroll
    for (int e = 0; e < 8; ++e) *(float*)(lds + oma + e * OM_ROW) = acc[e][0];
    LDSBAR();
    const unsigned tb3 = ODELAY > 0 ? (unsigned)__builtin_amdgcn_s_memrealtime() : 0u;
    STAMP(7);
#pragma unroll
    for (int i = TWIN; i < 12; ++i) TLOAD(i);
    SB();
    const bool diag = (m16 >> 2) == g4;
    const v4u tg = *(const v4u*)(lds + L_TAG);
    const v2u ah = *(const v2u*)(lds + ((diag && (m16 & 3) < 2) ? L_HP + (m16 & 3) * 32 + w * 8 : L_Z));
    const unsigned t = tg.x;
#pragma unroll
    for (int dw = 0; dw < 4; ++dw) {
      acc[2 * dw] = mfma16(ah, v2u{perm_lo(tr[0][dw], tr[1][dw]), perm_lo(tr[2][dw], tr[3][dw])}, zero4);
      acc[2 * dw + 1] = mfma16(ah, v2u{perm_hi(tr[0][dw], tr[1][dw]), perm_hi(tr[2][dw], tr[3][dw])}, zero4);
    }
    SB();
    int ogo = WS_O + (((bp * 8 + kvh) * 16 + r16) * 2 + (w >> 1)) * 2048 + (lane >> 2) * 128 + (w & 1) * 64 + (lane & 3) * 16;
    float gate = u2f(lane < 32 ? tg.y : tg.z);
    unsigned m0 = (diag && (lane & 1) == 0) ? 0xffffffffu : 0u, m1 = (diag && (lane & 1) != 0) ? 0xffffffffu : 0u;
    int fma_ = L_FM + w * 8 * FM_ROW + lane * 8;
    asm volatile("" : "+v"(gate), "+v"(m0), "+v"(m1), "+v"(fma_), "+v"(ogo));
    SB();
    if (OPOS >= 1) { PAIRUP(tr + 4, 0); PIN_PAIR(0); }
    if (OPOS >= 2) { PAIRUP(tr + 4, 1); PIN_PAIR(1); }
    if (OPOS >= 3) { PAIRUP(tr + 4, 2); PIN_PAIR(2); }
    if (OPOS >= 4) { PAIRUP(tr + 4, 3); PIN_PAIR(3); }
    if (ODELAY > 0) while ((int)((unsigned)__builtin_amdgcn_s_memrealtime() - tb3) < ODELAY) {}
    SB();
    v4u go = ld16<TLAUX>(rW, ogo, 0);
    SB();
    if (OPOS < 1) { PAIRUP(tr + 4, 0); PIN_PAIR(0); }
    if (OPOS < 2) { PAIRUP(tr + 4, 1); PIN_PAIR(1); }
    if (OPOS < 3) { PAIRUP(tr + 4, 2); PIN_PAIR(2); }
    if (OPOS < 4) { PAIRUP(tr + 4, 3); PIN_PAIR(3); }
    STAMP(11);
    if (!wave_none(untag(go, t) != 0u)) {
      _Pragma("nounroll") for (unsigned spin = 0; spin < SPIN_CAP; ++spin) {
        if (PSLEEP) __builtin_amdgcn_s_sleep(PSLEEP);
        REREAD(ogo);
        go = ld16<TLAUX>(rW, ogo, 0);
        if (wave_none(untag(go, t) != 0u)) break;
      }
    }
    STAMP(8);
    {
      const float lm = u2f(go.z) + u2f(go.w);
      float M = fmx(lm, dpp<ROR4>(lm));
      M = fmx(M, dpp<ROR8>(M));
      M = x16_max(M);
      float d = __builtin_amdgcn_exp2f(lm - M);
      float a = d * u2f(go.x), c = d * u2f(go.y);
      a += dpp<ROR4>(a); c += dpp<ROR4>(c); d += dpp<ROR4>(d);
      a += dpp<ROR8>(a); c += dpp<ROR8>(c); d += dpp<ROR8>(d);
      a = x16_sum(a); c = x16_sum(c);
      d = gate * __builtin_amdgcn_rcpf(x16_sum(d));
      const unsigned op = pkbf(a * d, c * d);
      const auto sw = __builtin_amdgcn_permlane32_swap(op, op, false, false);
      const v4u b0 = {dppu<0x00>(sw[0]), dppu<0x55>(sw[0]), dppu<0xAA>(sw[0]), dppu<0xFF>(sw[0])};
      const v4u b1 = {dppu<0x00>(sw[1]), dppu<0x55>(sw[1]), dppu<0xAA>(sw[1]), dppu<0xFF>(sw[1])};
      const v4u ao = (b0 & m0) | (b1 & m1);
#pragma unroll
      for (int e = 0; e < 8; ++e) acc[e] = mfma32(ao, bf[e], acc[e]);
#pragma unroll
      for (int e = 0; e < 8; ++e) *(f32x2*)(lds + fma_ + e * FM_ROW) = f32x2{acc[e][0], acc[e][1]};
    }
    LDSBAR();
    STAMP(9);
    if (w == 0 && pu < 112) {
      int fgo = WS_F + (bp * 28 + (pu >> 2)) * 16384 + (lane >> 1) * 128 + ((pu & 3) * 2 + (lane & 1)) * 16;
      const int oo2 = (lane & 14) == 0 ? ((2 * bp + (lane >> 5)) * DM + 4 * pu + 2 * (lane & 1) + (g4 & 1)) * 2 : OOB;
      v4u gf[4];
      lds_wait(lds, L_CNT + 12, 4);
      if (FDELAY) __builtin_amdgcn_s_sleep(FDELAY);
      _Pragma("nounroll") for (unsigned spin = 0; spin < SPIN_CAP; ++spin) {
        REREAD(fgo);
#pragma unroll
        for (int k = 0; k < 4; ++k) gf[k] = ld16<TLAUX>(rW, fgo, k * 4096);
        const unsigned miss = (untag(gf[0], t) | untag(gf[1], t)) | (untag(gf[2], t) | untag(gf[3], t));
        if (wave_none(miss != 0u)) break;
        if (FSLEEP) __builtin_amdgcn_s_sleep(FSLEEP);
      }
      float v[4];
#pragma unroll
      for (int k = 0; k < 4; ++k) v[k] = (u2f(gf[0][k]) + u2f(gf[1][k])) + (u2f(gf[2][k]) + u2f(gf[3][k]));
      float r = fold16(fold32(v[0], v[1]), fold32(v[2], v[3]));
      r += dpp<ROR8>(r); r += dpp<ROR4>(r); r += dpp<XOR2>(r);
      __builtin_amdgcn_raw_buffer_store_b16((unsigned short)pkbf(r, r), mk(out, NB * DM * 2), oo2, 0, 0);
      if (id == 0 && lane == 0) __builtin_amdgcn_raw_buffer_store_b32(t, rW, WS_E, 0, AUX_SC1);
    }
  }
}

}

extern "C" void kernel_launch(void* const* d_in, const int* in_sizes, int n_in, void* d_out, int out_size, void* d_ws, size_t ws_size,
                              hipStream_t stream) {
  (void)in_sizes; (void)n_in; (void)out_size; (void)ws_size;
  Touch tt;
  {
    const char* const kc = (const char*)d_in[1]; const char* const vc = (const char*)d_in[2];
    const char* const wq = (const char*)d_in[3]; const char* const wo = (const char*)d_in[4];
    const char* const w1 = (const char*)d_in[6]; const char* const w2 = (const char*)d_in[7];
    constexpr unsigned HEAD = SK * DH * 2, W0 = (SK - WIN) * DH * 2, WB = WIN * DH * 2, WQB = DM * NQ * 2, W1B = DM * DFF * 2;
    for (int n = 0; n < 16; ++n)
      tt.e[n] = TouchEntry{((n >= 4 && n < 12) ? vc : kc) + (size_t)(n & 3) * HKV * HEAD + W0 + ((n >> 2) & 1 ? 0 : WB - 128), HEAD, 0u};
    const char* const rest[16] = {wq, wq + WQB / 2, wq + WQB - 1024, w1, w1 + W1B - 1024, w2, w2 + W1B - 1024, wo, wo + WQB / 8 - 128,
                                  (const char*)d_in[0], (const char*)d_in[8], (const char*)d_out, (const char*)d_ws + 1024,
                                  (const char*)d_ws + WS_O - 1024, (const char*)d_ws + WS_END, vc + W0 + WB / 2};
    const unsigned step[16] = {128, 128, 128, 128, 128, 128, 128, WQB / 8, WQB / 8, 128, 112, 128, 128, 128, 128, HEAD};
    for (int n = 0; n < 16; ++n) tt.e[16 + n] = TouchEntry{rest[n], step[n], 0u};
  }
  gated_mix_decode_kernel<<<256, 512, 0, stream>>>(
      (const unsigned short*)d_in[0], (const unsigned short*)d_in[1], (const unsigned short*)d_in[2], (const unsigned short*)d_in[3],
      (const unsigned short*)d_in[4], (const unsigned short*)d_in[6], (const unsigned short*)d_in[7], (const unsigned short*)d_in[8],
      (unsigned short*)d_out, (unsigned char*)d_ws, tt);
}
```

```cpp
#define AMP_K 1
#define AMP_K0 0
#define AMP_A 10
#define AMP_ID 130
#define AMP_W 4
#include <hip/hip_runtime.h>
#include <hip/hip_bf16.h>
#include <cstdint>

namespace {

typedef unsigned v4u __attribute__((ext_vector_type(4)));
typedef unsigned v2u __attribute__((ext_vector_type(2)));
typedef unsigned v16u __attribute__((ext_vector_type(16)));
typedef float f32x4 __attribute__((ext_vector_type(4)));
typedef float f32x2 __attribute__((ext_vector_type(2)));
typedef short s16x4 __attribute__((ext_vector_type(4)));
typedef __bf16 bf16x8 __attribute__((ext_vector_type(8)));
typedef __bf16 bf16x2 __attribute__((ext_vector_type(2)));
typedef __amdgpu_buffer_rsrc_t rsrc_t;
typedef __attribute__((address_space(3))) unsigned lds_u32;

constexpr int NB = 4, DM = 448, HKV = 8, SK = 8192, DH = 256, WIN = 2048, DFF = 1792, NQ = 4096;
constexpr int AUX_SC1 = 16;
constexpr int OOB = 0x40000000;
constexpr unsigned SPIN_CAP = 200000, TAG_MASK = 1023u;

#define WAUX 2
#define KVAUX 2
#define THR 4
#define QG_AT 23
#define VB2 4
#define KPRE 2
#define CODEPF 1
#define CODE_BYTES 12288
#define ILV 1
#define ADAPT 1
#define ADAPT_INIT 2
#define PACE_A 80
#define PACE_T 160
#define PACE_D 25
#define PACE_MAX 3
#define HDRY 0
#define ODELAY 60
#define FDELAY 14
#define FSLEEP 0
#define TWN 8
#define OPOS 2
#define STAUX 16
#define QLAUX 16
#define PWAUX 16
#define TLAUX 16
#define KB0 10
#define KBURST 8
#define JSKIP 3
#define SLOTX 1
#define TLBW 2
#define MIDT 0
#define LAGW 10
#define HFIRST 1
#define TPRE 0
#define PREWARM 1
#define PSLEEP 0
#define ABL 0

constexpr int WS_E = 0;
constexpr int WS_Q = 4096;
constexpr int WS_H = WS_Q + 8 * 4 * 8192;
constexpr int WS_O = WS_H + 28 * 4 * 2048 + 4096;
constexpr int WS_F = WS_O + 16 * 16 * 4096;
constexpr int WS_END = WS_F + 2 * 28 * 16384;
constexpr int WS_DIAG = 8 << 20;

constexpr int OM_ROW = 68 * 4, FM_ROW = 544;
constexpr int L_QP = 0;
constexpr int L_Q = 8192;
constexpr int L_P = 9216;
constexpr int L_Z = 10240;
constexpr int L_CNT = 10304;
constexpr int L_TAG = 10320;
constexpr int L_HP = 10368;
constexpr int L_ST = 10432;
constexpr int L_OM = 10496;
constexpr int L_FM = L_OM + 32 * OM_ROW;
constexpr int L_END = L_FM + 32 * FM_ROW;

__device__ __forceinline__ rsrc_t mk(const void* p, int bytes) { return __builtin_amdgcn_make_buffer_rsrc((void*)p, 0, bytes, 0x00020000); }
template <int AUX> __device__ __forceinline__ v4u ld16(rsrc_t r, int vo, int so) { return __builtin_amdgcn_raw_buffer_load_b128(r, vo, so, AUX); }
__device__ __forceinline__ int oob_if(bool c) { return c ? OOB : 0; }
__device__ __forceinline__ float u2f(unsigned u) { return __builtin_bit_cast(float, u); }
__device__ __forceinline__ unsigned f2u(float f) { return __builtin_bit_cast(unsigned, f); }
__device__ __forceinline__ unsigned tagged(float v, unsigned t, unsigned keep) { return (f2u(v) & keep) | t; }
__device__ __forceinline__ void publish(rsrc_t r, int vo, float a, float b, float c, float d, unsigned t, unsigned keep = ~TAG_MASK) {
  __builtin_amdgcn_raw_buffer_store_b128(v4u{tagged(a, t, keep), tagged(b, t, keep), tagged(c, t, keep), tagged(d, t, keep)}, r, vo, 0, STAUX);
}
__device__ __forceinline__ unsigned untag(v4u& g, unsigned t) { g ^= t; return (g.x | g.y | g.z | g.w) & TAG_MASK; }
__device__ __forceinline__ bool wave_none(bool p) { return __builtin_amdgcn_ballot_w64(p) == 0; }
__device__ __forceinline__ float dot2(unsigned a, unsigned b, float c) {
  return __builtin_amdgcn_fdot2_f32_bf16(__builtin_bit_cast(bf16x2, a), __builtin_bit_cast(bf16x2, b), c, false);
}
__device__ __forceinline__ unsigned pkbf(float lo, float hi) {
  const bf16x2 v = {(__bf16)lo, (__bf16)hi};
  return __builtin_bit_cast(unsigned, v);
}
__device__ __forceinline__ unsigned perm_lo(unsigned a, unsigned b) { return __builtin_amdgcn_perm(b, a, 0x05040100u); }
__device__ __forceinline__ unsigned perm_hi(unsigned a, unsigned b) { return __builtin_amdgcn_perm(b, a, 0x07060302u); }
template <int CTRL> __device__ __forceinline__ unsigned dppu(unsigned x) { return (unsigned)__builtin_amdgcn_mov_dpp((int)x, CTRL, 0xf, 0xf, true); }
template <int CTRL> __device__ __forceinline__ float dpp(float x) { return u2f(dppu<CTRL>(f2u(x))); }
constexpr int XOR1 = 0xB1, XOR2 = 0x4E, XOR7 = 0x141, ROR8 = 0x128, ROR4 = 0x124;
__device__ __forceinline__ float fmx(float a, float c) { return __builtin_amdgcn_fmed3f(a, c, __builtin_inff()); }
__device__ __forceinline__ float fold16(float a, float b) {
  auto s = __builtin_amdgcn_permlane16_swap(f2u(a), f2u(b), false, false);
  return u2f(s[0]) + u2f(s[1]);
}
__device__ __forceinline__ float fold32(float a, float b) {
  auto s = __builtin_amdgcn_permlane32_swap(f2u(a), f2u(b), false, false);
  return u2f(s[0]) + u2f(s[1]);
}
__device__ __forceinline__ float x16_sum(float x) { return fold16(x, x); }
__device__ __forceinline__ float x32_sum(float x) { return fold32(x, x); }
__device__ __forceinline__ float x16_max(float x) {
  auto s = __builtin_amdgcn_permlane16_swap(f2u(x), f2u(x), false, false);
  return fmx(u2f(s[0]), u2f(s[1]));
}
__device__ __forceinline__ float x32_max(float x) {
  auto s = __builtin_amdgcn_permlane32_swap(f2u(x), f2u(x), false, false);
  return fmx(u2f(s[0]), u2f(s[1]));
}
__device__ __forceinline__ float row_sum(float x) { x += dpp<XOR1>(x); x += dpp<XOR2>(x); x += dpp<ROR4>(x); x += dpp<ROR8>(x); return x; }
__device__ __forceinline__ float row_max(float x) {
  x = fmx(x, dpp<XOR1>(x)); x = fmx(x, dpp<XOR2>(x)); x = fmx(x, dpp<ROR4>(x)); x = fmx(x, dpp<ROR8>(x)); return x;
}
__device__ __forceinline__ f32x4 mfma32(v4u a, v4u b, f32x4 c) {
  return __builtin_amdgcn_mfma_f32_16x16x32_bf16(__builtin_bit_cast(bf16x8, a), __builtin_bit_cast(bf16x8, b), c, 0, 0, 0);
}
__device__ __forceinline__ f32x4 mfma16(v2u a, v2u b, f32x4 c) {
  return __builtin_amdgcn_mfma_f32_16x16x16bf16_1k(__builtin_bit_cast(s16x4, a), __builtin_bit_cast(s16x4, b), c, 0, 0, 0);
}

#define SB() __builtin_amdgcn_sched_barrier(0)
#define REREAD(off) asm volatile("" : "+v"(off) :: "memory")
#define VMWAIT(n) __builtin_amdgcn_s_waitcnt(((n) & 15) | (7 << 4) | (15 << 8) | ((((n) >> 4) & 3) << 14))
#define THROTTLE() do { SB(); VMWAIT(LAGW - 1); \
    asm volatile("s_cmp_gt_i32 %0, 0\n\ts_cbranch_scc1 1f\n\ts_waitcnt vmcnt(%1)\n1:" :: "s"(lag), "i"(THR) : "scc"); SB(); } while (0)
#define PACE(k) do { lag = (int)((unsigned)__builtin_amdgcn_s_memrealtime() - tstart) - (PACE_A + (PACE_T * (k)) / 10); SB(); } while (0)
#define LDSBAR() do { SB(); __builtin_amdgcn_fence(__ATOMIC_RELEASE, "workgroup", "local"); __builtin_amdgcn_s_barrier(); \
                      __builtin_amdgcn_fence(__ATOMIC_ACQUIRE, "workgroup", "local"); SB(); } while (0)

__device__ __forceinline__ void lds_arrive(unsigned char* lds, int off, int lane) {
  __builtin_amdgcn_fence(__ATOMIC_RELEASE, "workgroup", "local");
  if (lane == 0) __hip_atomic_fetch_add((lds_u32*)(lds + off), 1u, __ATOMIC_RELAXED, __HIP_MEMORY_SCOPE_WORKGROUP);
}
__device__ __forceinline__ void lds_wait(unsigned char* lds, int off, unsigned want) {
  _Pragma("nounroll") for (unsigned spin = 0; spin < 16u * SPIN_CAP; ++spin) {
    if ((unsigned)__builtin_amdgcn_readfirstlane(__hip_atomic_load((lds_u32*)(lds + off), __ATOMIC_RELAXED, __HIP_MEMORY_SCOPE_WORKGROUP)) >= want) break;
    __builtin_amdgcn_s_sleep(1);
  }
  __builtin_amdgcn_fence(__ATOMIC_ACQUIRE, "workgroup", "local");
}

#define STAMP(k) do { if ((k) == AMP_K0 || (k) == AMP_K) { SB(); stamp[k] = __builtin_amdgcn_s_memrealtime(); SB(); } } while (0)

struct TouchEntry { const void* p; unsigned stride, zero; };
struct Touch { TouchEntry e[32]; };

__global__ void __launch_bounds__(512, 2)
gated_mix_decode_kernel(const unsigned short*, const unsigned short*, const unsigned short*, const unsigned short*, const unsigned short*,
                        const unsigned short*, const unsigned short*, const unsigned short*, unsigned short*, unsigned char*, Touch) {
  __shared__ __attribute__((aligned(128))) unsigned char lds[L_END];
  unsigned long long stamp[12];
#pragma unroll
  for (int k = 0; k < 12; ++k) stamp[k] = 0;
  STAMP(0);
  const unsigned tstart = (unsigned)__builtin_amdgcn_s_memrealtime();
  v16u ka; v4u kb;
  v4u tp;
  asm volatile("s_load_dwordx4 %2, %3, %4 offset:0x50\n\ts_load_dwordx16 %0, %3, 0x0\n\ts_load_dwordx4 %1, %3, 0x40"
               : "=&s"(ka), "=&s"(kb), "=&s"(tp) : "s"(__builtin_amdgcn_kernarg_segment_ptr()), "s"((blockIdx.x >> 3) * 16u) : "memory");
  SB();
  unsigned cpf;
  {
    unsigned long long pc;
    asm volatile("s_getpc_b64 %0" : "=s"(pc));
    constexpr int PER = (CODE_BYTES / 128 + 3) / 4;
    const int line = ((blockIdx.x >> 3) & 3) * PER + threadIdx.x;
    cpf = __builtin_amdgcn_raw_buffer_load_b32(mk((const void*)pc, CODE_BYTES), (line * 128) | oob_if(threadIdx.x >= PER), 0, 0);
    SB();
  }
  const int id = blockIdx.x, kvh = id & 7, b = (id >> 3) & 3, j = id >> 5;
  const int lane = threadIdx.x & 63, wv = __builtin_amdgcn_readfirstlane(threadIdx.x >> 6), w = wv & 3;
  const bool helper = wv < 4;
  const int blk0 = j * 16384 + w * 4096;
  int fo;
  {
    const int base = helper ? (112 * b + 32 * w) * (NQ * 2) + (kvh * 8 + j) * 128 : blk0;
    const int rstr = helper ? 4 * NQ * 2 : 512, rlim = wv == 3 ? 4 : 8;
    fo = (base + (lane >> 3) * rstr + (lane & 7) * 16) | oob_if((lane >> 3) >= rlim);
    asm volatile("" : "+v"(fo));
  }
  asm volatile("s_waitcnt lgkmcnt(0)" : "+s"(ka), "+s"(kb), "+s"(tp) :: "memory");
  const unsigned tw = __builtin_amdgcn_raw_buffer_load_b32(mk((const void*)(((unsigned long long)tp.y << 32) | tp.x), OOB), oob_if(threadIdx.x != 0), tp.z * kvh, 2);
  SB();
#define KARG(T, lo, hi) ((T)(((unsigned long long)(hi) << 32) | (lo)))
  const unsigned short* x = KARG(const unsigned short*, ka[0], ka[1]);
  const unsigned short* kc = KARG(const unsigned short*, ka[2], ka[3]);
  const unsigned short* vc = KARG(const unsigned short*, ka[4], ka[5]);
  const unsigned short* wq = KARG(const unsigned short*, ka[6], ka[7]);
  const unsigned short* wo = KARG(const unsigned short*, ka[8], ka[9]);
  const unsigned short* w1 = KARG(const unsigned short*, ka[10], ka[11]);
  const unsigned short* w2 = KARG(const unsigned short*, ka[12], ka[13]);
  const unsigned short* gw = KARG(const unsigned short*, ka[14], ka[15]);
  unsigned short* out = KARG(unsigned short*, kb[0], kb[1]);
  unsigned char* ws = KARG(unsigned char*, kb[2], kb[3]);
  const size_t head = ((size_t)(id & 31) * SK + (SK - WIN)) * DH;
  const rsrc_t rF = mk(helper ? wq : kc + head, helper ? DM * NQ * 2 : WIN * DH * 2);
  const int fstep = helper ? NQ * 2 : 128;
  v4u first[4];
#pragma unroll
  for (int i = 0; i < 4; ++i) first[i] = ld16<2>(rF, fo, i * fstep);
  SB();
  const int j1 = j == 7 ? JSKIP : j;
  const int bp = b >> 1, r16 = (b & 1) * 8 + j, pu = kvh * 16 + r16, fs = kvh * 14 + r16;
  const int m16 = lane & 15, g4 = lane >> 4;
  const rsrc_t rW = mk(ws, WS_END);
  const f32x4 zero4 = {0.f, 0.f, 0.f, 0.f};

  if (__builtin_expect(helper, HFIRST)) {
    const int tid = threadIdx.x;
    const rsrc_t r1 = mk(w1, DM * DFF * 2), rX = mk(x, NB * DM * 2), rG = mk(gw, DM * 2);
    const v4u (&wqr)[4] = first;
    v4u w1r[2];
    v2u xq, xh;
    {
      const int rr = 2 * g4 + (m16 & 3), xrow = (m16 >> 2) * DM;
      const bool off = (m16 & 2) != 0;
      xq = __builtin_amdgcn_raw_buffer_load_b64(rX, ((xrow + 112 * b + 32 * w + 4 * rr) * 2) | oob_if(off | (32 * w + 4 * rr >= 112)), 0, 0);
      xh.x = __builtin_amdgcn_raw_buffer_load_b32(rX, ((xrow + 56 * kvh + 16 * w + 2 * rr) * 2) | oob_if(off | (16 * w + 2 * rr >= 56)), 0, 0);
      xh.y = 0u;
    }
    const unsigned ep = __builtin_amdgcn_raw_buffer_load_b32(rW, WS_E, 0, AUX_SC1);
    SB();
    {
      const int row1 = 16 * w + 2 * (lane >> 3);
      const int o1 = ((56 * kvh + row1) * (DFF * 2) + (4 * j1 + b) * 128 + (lane & 7) * 16) | oob_if((j == JSKIP) | (row1 >= 56));
#pragma unroll
      for (int i = 0; i < 2; ++i) w1r[i] = ld16<WAUX>(r1, o1, i * (DFF * 2));
    }
    SB();
    STAMP(11);
    if (tid < 20) *(unsigned*)(lds + L_Z + tid * 4) = 0u;
    LDSBAR();
    {
      float qv[8], hv[8];
#pragma unroll
      for (int dw = 0; dw < 4; ++dw) {
        const v2u blo = {perm_lo(wqr[0][dw], wqr[1][dw]), perm_lo(wqr[2][dw], wqr[3][dw])};
        const v2u bhi = {perm_hi(wqr[0][dw], wqr[1][dw]), perm_hi(wqr[2][dw], wqr[3][dw])};
        const f32x4 dl = mfma16(xq, blo, zero4), dh = mfma16(xq, bhi, zero4);
        const float sl = (lane & 8) ? dl[1] : dl[0], sh = (lane & 8) ? dh[1] : dh[0];
        qv[2 * dw] = sl + dpp<ROR8>(sl);
        qv[2 * dw + 1] = sh + dpp<ROR8>(sh);
      }
#pragma unroll
      for (int dw = 0; dw < 4; ++dw) {
        const v2u blo = {perm_lo(w1r[0][dw], w1r[1][dw]), 0u}, bhi = {perm_hi(w1r[0][dw], w1r[1][dw]), 0u};
        const f32x4 dl = mfma16(xh, blo, zero4), dh = mfma16(xh, bhi, zero4);
        const float sl = (lane & 8) ? dl[1] : dl[0], sh = (lane & 8) ? dh[1] : dh[0];
        hv[2 * dw] = sl + dpp<ROR8>(sl);
        hv[2 * dw + 1] = sh + dpp<ROR8>(sh);
      }
      const bool up = (lane & 8) != 0;
      f32x4* p = (f32x4*)(lds + L_QP + ((w * 4 + g4) * 128 + 8 * m16) * 4);
      p[0] = up ? f32x4{hv[0], hv[1], hv[2], hv[3]} : f32x4{qv[0], qv[1], qv[2], qv[3]};
      p[1] = up ? f32x4{hv[4], hv[5], hv[6], hv[7]} : f32x4{qv[4], qv[5], qv[6], qv[7]};
    }
    lds_arrive(lds, L_CNT, lane);
    asm volatile("" :: "v"(tw));
    asm volatile("" :: "v"(cpf));
    const unsigned t = ((unsigned)__builtin_amdgcn_readfirstlane(ep) + 1u) & TAG_MASK;
    STAMP(1);
    lds_wait(lds, L_CNT, 4);
    {
      f32x4 s = zero4;
#pragma unroll
      for (int ww = 0; ww < 4; ++ww) s += *(const f32x4*)(lds + L_QP + ((ww * 4 + w) * 128 + 4 * (lane & 31)) * 4);
      const int dst = lane < 16 ? WS_Q + (kvh * 4 + w) * 8192 + j * 1024 + b * 256 + lane * 16
                    : (lane < 32 && j != JSKIP) ? WS_H + ((4 * j1 + b) * 4 + w) * 2048 + kvh * 256 + m16 * 16 : OOB;
      publish(rW, (ABL & 8) ? OOB : dst, s[0], s[1], s[2], s[3], t);
    }
    SB();
    STAMP(2);
    STAMP(3);
    lds_wait(lds, L_CNT + 8, 1);
    STAMP(5);
    {
      const int go_ = (lane * 16) | oob_if((lane >= 56) | (w != 3) | ((ABL & 8192) != 0));
      const v4u xg0 = ld16<0>(rX, go_, 2 * bp * DM * 2), xg1 = ld16<0>(rX, go_, (2 * bp + 1) * DM * 2), gg = ld16<0>(rG, go_, 0);
      SB();
      int qgo = (ABL & (2 | 16384)) ? OOB : WS_Q + (kvh * 4 + b) * 8192 + (2 * w + (lane >> 5)) * 1024 + (g4 & 1) * 256 + m16 * 16;
      const bool hon = w == 3 && r16 < 14;
      int hgo = (hon && !(ABL & (2 | 4096))) ? WS_H + ((fs >> 2) * 4 + 2 * bp + (lane >> 5)) * 2048 + ((lane >> 2) & 7) * 256 + (fs & 3) * 64 + (lane & 3) * 16 : OOB;
      v4u g0, g1, gh;
      _Pragma("nounroll") for (unsigned spin = 0; spin < SPIN_CAP; ++spin) {
        REREAD(qgo); REREAD(hgo);
        g0 = ld16<QLAUX>(rW, qgo, 0);
        g1 = ld16<QLAUX>(rW, qgo, 512);
        gh = ld16<QLAUX>(rW, hgo, 0);
        const unsigned mq = untag(g0, t) | untag(g1, t), mh = untag(gh, t);
        if ((ABL & 1) || wave_none((mq | (hon ? mh : 0u)) != 0u)) break;
        __builtin_amdgcn_s_sleep(8);
      }
      float q[4];
#pragma unroll
      for (int k = 0; k < 4; ++k) q[k] = x16_sum(u2f(g0[k]) + u2f(g1[k]));
      if ((g4 & 1) == 0) *(v2u*)(lds + L_Q + ((2 * w + (lane >> 5)) * 64 + 4 * m16) * 2) = v2u{pkbf(q[0], q[1]), pkbf(q[2], q[3])};
      if (w == 3) {
        float d0 = 0.f, d1 = 0.f;
#pragma unroll
        for (int k = 0; k < 4; ++k) { d0 = dot2(xg0[k], gg[k], d0); d1 = dot2(xg1[k], gg[k], d1); }
        const float gate = 1.f / (1.f + __expf(-x16_sum(row_sum(fold32(d0, d1)))));
        if ((lane & 31) == 0) *(float*)(lds + L_TAG + 4 + 4 * (lane >> 5)) = gate;
        if (lane == 1) *(unsigned*)(lds + L_TAG) = t;
        float h[4];
#pragma unroll
        for (int k = 0; k < 4; ++k) {
          float a = hon ? u2f(gh[k]) : 0.f;
          a += dpp<ROR4>(a);
          a += dpp<ROR8>(a);
          h[k] = fmaxf(x16_sum(a), 0.f) * (1.f - gate);
        }
        if ((lane & 28) == 0) *(v2u*)(lds + L_HP + (lane >> 5) * 32 + (lane & 3) * 8) = v2u{pkbf(h[0], h[1]), pkbf(h[2], h[3])};
      }
    }
    STAMP(4);
    LDSBAR();
    if (PREWARM) {
      const int po = WS_O + ((bp * 8 + kvh) * 16 + r16) * 4096 + (lane & 31) * 128;
      const int pf = WS_F + (bp * 28 + (pu >> 2)) * 16384 + (w * 32 + (lane & 31)) * 128;
      const unsigned pw = __builtin_amdgcn_raw_buffer_load_b32(rW, lane < 32 ? (w == 0 ? po : OOB) : (((PREWARM & 2) && pu < 112) ? pf : OOB), 0, PWAUX);
      asm volatile("" :: "v"(pw));
    }
    if (MIDT && w == 1) {
      const unsigned char* p = lane == 0 ? (const unsigned char*)(wo + (kvh * 512 + 32 * r16) * DM)
                             : lane == 1 ? (const unsigned char*)(w2 + (r16 < 14 ? 16 * fs : 0) * DM)
                             : lane == 2 ? (const unsigned char*)out + (id & 15) * 128 : ws + WS_END + 4096 + id * 128;
      unsigned mt = 0;
      if (lane < 4) mt = __builtin_nontemporal_load((const __attribute__((address_space(1))) unsigned*)p);
      asm volatile("" :: "v"(mt));
    }
    lds_wait(lds, L_CNT + 4, 4);
    float lse;
    f32x4 fc;
    {
      const int h = tid >> 7;
      const f32x4 e0 = *(const f32x4*)(lds + L_ST + h * 32), e1 = *(const f32x4*)(lds + L_ST + h * 32 + 16);
      const float M = fmx(fmx(e0[0], e0[2]), fmx(e1[0], e1[2]));
      fc = f32x4{__builtin_amdgcn_exp2f(e0[0] - M), __builtin_amdgcn_exp2f(e0[2] - M), __builtin_amdgcn_exp2f(e1[0] - M), __builtin_amdgcn_exp2f(e1[2] - M)};
      const float L = (fc[0] * e0[1] + fc[1] * e0[3]) + (fc[2] * e1[1] + fc[3] * e1[3]);
      fc *= 1.f / L;
      lse = M + __builtin_amdgcn_logf(L);
    }
    float lse_hi = u2f(f2u(lse) & ~TAG_MASK), lse_lo = lse - lse_hi;
    int oa = L_OM + 2 * (tid & 3) * OM_ROW + (tid >> 2) * 4, fa = L_FM + 2 * (tid & 3) * FM_ROW + (tid >> 2) * 8;
    int odst = (ABL & 128) ? OOB : WS_O + ((bp * 8 + kvh) * 32 + (tid >> 3)) * 2048 + (r16 ^ (SLOTX ? ((tid >> 3) & 7) : 0)) * 128 + (tid & 7) * 16;
    int fdst = tid < 224 ? WS_F + (bp * 28 + (tid >> 3)) * 16384 + (pu ^ (SLOTX ? ((tid >> 3) & 7) : 0)) * 128 + (tid & 7) * 16 : OOB;
    asm volatile("" : "+v"(lse_hi), "+v"(lse_lo), "+v"(fc), "+v"(oa), "+v"(fa), "+v"(odst), "+v"(fdst));
    STAMP(6);
    int pass = HDRY ? 0 : 1;
    unsigned tv = t, keep = ~TAG_MASK;
    asm volatile("" : "+s"(pass), "+v"(tv), "+s"(keep));
    _Pragma("nounroll") for (; pass < 2; ++pass) {
      const int live = pass ? 0 : OOB;
      if (pass) LDSBAR();
      {
        float a = 0.f, c = 0.f;
#pragma unroll
        for (int ww = 0; ww < 4; ++ww) {
          a = fmaf(fc[ww], *(const float*)(lds + oa + ww * 8 * OM_ROW), a);
          c = fmaf(fc[ww], *(const float*)(lds + oa + ww * 8 * OM_ROW + OM_ROW), c);
        }
        publish(rW, odst | live, a, c, lse_hi, lse_lo, tv, keep);
      }
      STAMP(7);
      if (pass) LDSBAR();
      {
        f32x2 s0 = {0.f, 0.f}, s1 = {0.f, 0.f};
#pragma unroll
        for (int ww = 0; ww < 4; ++ww) {
          s0 += *(const f32x2*)(lds + fa + ww * 8 * FM_ROW);
          s1 += *(const f32x2*)(lds + fa + ww * 8 * FM_ROW + FM_ROW);
        }
        publish(rW, fdst | live, s0.x, s0.y, s1.x, s1.y, tv, keep);
        SB();
        if (pass) lds_arrive(lds, L_CNT + 12, lane);
      }
    }
    STAMP(9);
  } else {
    const rsrc_t rV = mk(vc + head, WIN * DH * 2);
    constexpr int BSTR = 131072;
    v4u kr[32], vr[32];
    int lag = 0;
#define KLOAD(i) kr[i] = ld16<KVAUX>(rF, fo, ((i) >> 2) * BSTR + ((i) & 3) * 128)
#pragma unroll
    for (int i = 0; i < 4; ++i) kr[i] = first[i];
    STAMP(1);
#pragma unroll
    for (int i = 4; i < 32; ++i) {
      if (i < 8 && i >= KBURST) { SB(); VMWAIT(KBURST - 1); SB(); } else if (i >= 12) THROTTLE(); else if (i >= 8) { SB(); VMWAIT(THR); SB(); }
      KLOAD(i);
      if (i >= 11 && (i & 3) == 3) PACE(i);
      if (i == KB0) { LDSBAR(); STAMP(2); }
      if (i == QG_AT && w == 0) lds_arrive(lds, L_CNT + 8, lane);
    }
    const int vo = blk0 + lane * 16;
#define VLOAD(i) vr[i] = ld16<KVAUX>(rV, vo, ((i) >> 2) * BSTR + ((i) & 3) * 1024)
#define VSTEP(i) do { THROTTLE(); VLOAD(i); SB(); if (((i) & 3) == 3) PACE(32 + (i)); } while (0)
    asm volatile("" :: "v"(cpf));
    asm volatile("" :: "v"(tw));
    STAMP(3);
#pragma unroll
    for (int i = 0; i < VB2; ++i) VSTEP(i);
    LDSBAR();
    STAMP(4);
    v4u pa[4];
    {
      v4u qf[2][4];
#pragma unroll
      for (int h = 0; h < 2; ++h)
#pragma unroll
        for (int t = 0; t < 4; ++t) qf[h][t] = *(const v4u*)(lds + L_Q + (h * 256 + 64 * t + 8 * (lane & 7)) * 2);
      VSTEP(VB2);
      float s[8][2];
#pragma unroll
      for (int i = 0; i < 8; i += 2) {
        float a0 = 0.f, a1 = 0.f, c0 = 0.f, c1 = 0.f;
#pragma unroll
        for (int t = 0; t < 4; ++t) {
#pragma unroll
          for (int dw = 0; dw < 4; ++dw) {
            a0 = dot2(kr[4 * i + t][dw], qf[0][t][dw], a0); c0 = dot2(kr[4 * i + 4 + t][dw], qf[0][t][dw], c0);
            a1 = dot2(kr[4 * i + t][dw], qf[1][t][dw], a1); c1 = dot2(kr[4 * i + 4 + t][dw], qf[1][t][dw], c1);
          }
          if (t == 1) VSTEP(VB2 + 1 + i);
        }
        s[i][0] = a0; s[i][1] = a1; s[i + 1][0] = c0; s[i + 1][1] = c1;
        VSTEP(VB2 + 2 + i);
      }
#pragma unroll
      for (int i = 0; i < 4; ++i)
#pragma unroll
        for (int h = 0; h < 2; ++h) {
          const float lo = s[i][h] + dpp<XOR7>(s[i][h]), hi = s[i + 4][h] + dpp<XOR7>(s[i + 4][h]);
          s[i][h] = (lane & 4) ? hi : lo;
        }
#pragma unroll
      for (int i = 0; i < 2; ++i)
#pragma unroll
        for (int h = 0; h < 2; ++h) {
          const float lo = s[i][h] + dpp<XOR2>(s[i][h]), hi = s[i + 2][h] + dpp<XOR2>(s[i + 2][h]);
          s[i][h] = (lane & 2) ? hi : lo;
        }
#pragma unroll
      for (int h = 0; h < 2; ++h) {
        const float lo = s[0][h] + dpp<XOR1>(s[0][h]), hi = s[1][h] + dpp<XOR1>(s[1][h]);
        s[0][h] = (lane & 1) ? hi : lo;
      }
      VSTEP(VB2 + 9);
      constexpr float C2 = 0.0625f * 1.4426950408889634f;
      const float z0 = s[0][0] * C2, z1 = s[0][1] * C2;
      const float m0 = x32_max(x16_max(row_max(z0))), m1 = x32_max(x16_max(row_max(z1)));
      const float p0 = __builtin_amdgcn_exp2f(z0 - m0), p1 = __builtin_amdgcn_exp2f(z1 - m1);
      const unsigned pp = pkbf(p0, p1);
      unsigned short* pl = (unsigned short*)(lds + L_P + w * 256 + ((lane >> 3) & 1) * 64 + (lane & 7) * 8 + (lane >> 4) * 2);
      pl[0] = (unsigned short)pp;
      pl[64] = (unsigned short)(pp >> 16);
      VSTEP(VB2 + 10);
      const float l01 = x16_sum(row_sum(fold32(p0, p1)));
      if ((lane & 31) == 0) *(f32x2*)(lds + L_ST + ((lane >> 5) * 4 + w) * 8) = f32x2{lane ? m1 : m0, l01};
      lds_arrive(lds, L_CNT + 4, lane);
      const bool valid = (m16 & 3) == 0 && ((m16 >> 2) & 1) == (g4 & 1);
      const int a = valid ? L_P + w * 256 + (m16 >> 3) * 128 + (g4 >> 1) * 64 : L_Z;
#pragma unroll
      for (int G = 0; G < 4; ++G) pa[G] = *(const v4u*)(lds + a + G * 16);
    }
    STAMP(5);
    f32x4 acc[8];
#pragma unroll
    for (int e = 0; e < 8; ++e) acc[e] = zero4;
    v4u tr[12], bf[8];
    const rsrc_t rO = mk(wo, NQ * DM * 2), r2 = mk(w2, DFF * DM * 2);
    const int o2 = (r16 < 14 && lane < 56) ? (16 * fs + 4 * w) * (DM * 2) + lane * 16 : OOB;
    const int oo = lane < 56 ? (kvh * 512 + 32 * r16 + 8 * w) * (DM * 2) + lane * 16 : OOB;
#define TLOAD(i) tr[i] = (i) < 4 ? ld16<WAUX>(r2, o2, (i) * (DM * 2)) : ld16<WAUX>(rO, oo, ((i) - 4) * (DM * 2))
#define TSTEP(i) do { THROTTLE(); TLOAD(i); SB(); } while (0)
#define PAIRUP(src, pr) do { _Pragma("unroll") for (int dw = 0; dw < 4; ++dw) { \
      bf[2 * dw][pr] = perm_lo((src)[2 * (pr)][dw], (src)[2 * (pr) + 1][dw]); \
      bf[2 * dw + 1][pr] = perm_hi((src)[2 * (pr)][dw], (src)[2 * (pr) + 1][dw]); } } while (0)
#define PIN_PAIR(pr) do { _Pragma("unroll") for (int e = 0; e < 8; ++e) { unsigned tmp_ = bf[e][pr]; asm volatile("" : "+v"(tmp_)); bf[e][pr] = tmp_; } SB(); } while (0)
#define PVMMA(G) do { _Pragma("unroll") for (int e = 0; e < 8; ++e) acc[e] = mfma32(pa[G], bf[e], acc[e]); } while (0)
#define PVGROUP(G) do { PAIRUP(vr + 8 * (G), 0); PAIRUP(vr + 8 * (G), 1); PAIRUP(vr + 8 * (G), 2); PAIRUP(vr + 8 * (G), 3); PVMMA(G); SB(); } while (0)
    constexpr int TIN = TPRE, TWIN = (TIN + TWN < 12) ? TIN + TWN : 12, V0 = VB2 + 11;
#pragma unroll
    for (int i = V0; i < 32; ++i) {
      VSTEP(i);
#pragma unroll
      for (int k = 0; k < TIN; ++k)
        if (V0 + (k * (31 - V0)) / (TIN ? TIN : 1) == i) TSTEP(k);
      if (i == V0) PVGROUP(0);
      if (i == 24) PVGROUP(1);
      if (i == 31) PVGROUP(2);
    }
#pragma unroll
    for (int k = 0; k < 8; ++k) {
      if (k + TWN >= 8 && TIN + k + TWN - 8 < TWIN) { SB(); VMWAIT(TWN - 1); SB(); TLOAD(TIN + k + TWN - 8); SB(); }
      if (k == 1) { PAIRUP(vr + 24, 0); PIN_PAIR(0); }
      if (k == 3) { PAIRUP(vr + 24, 1); PIN_PAIR(1); }
      if (k == 5) { PAIRUP(vr + 24, 2); PIN_PAIR(2); }
    }
    int oma = L_OM + w * 8 * OM_ROW + lane * 4;
    asm volatile("" : "+v"(oma));
    SB();
    PAIRUP(vr + 24, 3);
    PVMMA(3);
    STAMP(6);
#pragma unroll
    for (int e = 0; e < 8; ++e) *(float*)(lds + oma + e * OM_ROW) = acc[e][0];
    LDSBAR();
    const unsigned tb3 = ODELAY > 0 ? (unsigned)__builtin_amdgcn_s_memrealtime() : 0u;
    STAMP(7);
#pragma unroll
    for (int i = TWIN; i < 12; ++i) TLOAD(i);
    SB();
    const bool diag = (m16 >> 2) == g4;
    const v4u tg = *(const v4u*)(lds + L_TAG);
    const v2u ah = *(const v2u*)(lds + ((diag && (m16 & 3) < 2) ? L_HP + (m16 & 3) * 32 + w * 8 : L_Z));
    const unsigned t = tg.x;
#pragma unroll
    for (int dw = 0; dw < 4; ++dw) {
      acc[2 * dw] = mfma16(ah, v2u{perm_lo(tr[0][dw], tr[1][dw]), perm_lo(tr[2][dw], tr[3][dw])}, zero4);
      acc[2 * dw + 1] = mfma16(ah, v2u{perm_hi(tr[0][dw], tr[1][dw]), perm_hi(tr[2][dw], tr[3][dw])}, zero4);
    }
    SB();
    int ogo = WS_O + (((bp * 8 + kvh) * 16 + r16) * 2 + (w >> 1)) * 2048 + (lane >> 2) * 128 + (w & 1) * 64 + (lane & 3) * 16;
    float gate = u2f(lane < 32 ? tg.y : tg.z);
    unsigned m0 = (diag && (lane & 1) == 0) ? 0xffffffffu : 0u, m1 = (diag && (lane & 1) != 0) ? 0xffffffffu : 0u;
    int fma_ = L_FM + w * 8 * FM_ROW + lane * 8;
    asm volatile("" : "+v"(gate), "+v"(m0), "+v"(m1), "+v"(fma_), "+v"(ogo));
    SB();
    if (OPOS >= 1) { PAIRUP(tr + 4, 0); PIN_PAIR(0); }
    if (OPOS >= 2) { PAIRUP(tr + 4, 1); PIN_PAIR(1); }
    if (OPOS >= 3) { PAIRUP(tr + 4, 2); PIN_PAIR(2); }
    if (OPOS >= 4) { PAIRUP(tr + 4, 3); PIN_PAIR(3); }
    if (ODELAY > 0) while ((int)((unsigned)__builtin_amdgcn_s_memrealtime() - tb3) < ODELAY) {}
    SB();
    v4u go = ld16<TLAUX>(rW, ogo, 0);
    SB();
    if (OPOS < 1) { PAIRUP(tr + 4, 0); PIN_PAIR(0); }
    if (OPOS < 2) { PAIRUP(tr + 4, 1); PIN_PAIR(1); }
    if (OPOS < 3) { PAIRUP(tr + 4, 2); PIN_PAIR(2); }
    if (OPOS < 4) { PAIRUP(tr + 4, 3); PIN_PAIR(3); }
    STAMP(11);
    if (!wave_none(untag(go, t) != 0u)) {
      _Pragma("nounroll") for (unsigned spin = 0; spin < SPIN_CAP; ++spin) {
        if (PSLEEP) __builtin_amdgcn_s_sleep(PSLEEP);
        REREAD(ogo);
        go = ld16<TLAUX>(rW, ogo, 0);
        if (wave_none(untag(go, t) != 0u)) break;
      }
    }
    STAMP(8);
    {
      const float lm = u2f(go.z) + u2f(go.w);
      float M = fmx(lm, dpp<ROR4>(lm));
      M = fmx(M, dpp<ROR8>(M));
      M = x16_max(M);
      float d = __builtin_amdgcn_exp2f(lm - M);
      float a = d * u2f(go.x), c = d * u2f(go.y);
      a += dpp<ROR4>(a); c += dpp<ROR4>(c); d += dpp<ROR4>(d);
      a += dpp<ROR8>(a); c += dpp<ROR8>(c); d += dpp<ROR8>(d);
      a = x16_sum(a); c = x16_sum(c);
      d = gate * __builtin_amdgcn_rcpf(x16_sum(d));
      const unsigned op = pkbf(a * d, c * d);
      const auto sw = __builtin_amdgcn_permlane32_swap(op, op, false, false);
      const v4u b0 = {dppu<0x00>(sw[0]), dppu<0x55>(sw[0]), dppu<0xAA>(sw[0]), dppu<0xFF>(sw[0])};
      const v4u b1 = {dppu<0x00>(sw[1]), dppu<0x55>(sw[1]), dppu<0xAA>(sw[1]), dppu<0xFF>(sw[1])};
      const v4u ao = (b0 & m0) | (b1 & m1);
#pragma unroll
      for (int e = 0; e < 8; ++e) acc[e] = mfma32(ao, bf[e], acc[e]);
#pragma unroll
      for (int e = 0; e < 8; ++e) *(f32x2*)(lds + fma_ + e * FM_ROW) = f32x2{acc[e][0], acc[e][1]};
    }
    LDSBAR();
    STAMP(9);
    if (w == 0 && pu < 112) {
      int fgo = WS_F + (bp * 28 + (pu >> 2)) * 16384 + (lane >> 1) * 128 + ((pu & 3) * 2 + (lane & 1)) * 16;
      const int oo2 = (lane & 14) == 0 ? ((2 * bp + (lane >> 5)) * DM + 4 * pu + 2 * (lane & 1) + (g4 & 1)) * 2 : OOB;
      v4u gf[4];
      lds_wait(lds, L_CNT + 12, 4);
      if (FDELAY) __builtin_amdgcn_s_sleep(FDELAY);
      _Pragma("nounroll") for (unsigned spin = 0; spin < SPIN_CAP; ++spin) {
        REREAD(fgo);
#pragma unroll
        for (int k = 0; k < 4; ++k) gf[k] = ld16<TLAUX>(rW, fgo, k * 4096);
        const unsigned miss = (untag(gf[0], t) | untag(gf[1], t)) | (untag(gf[2], t) | untag(gf[3], t));
        if (wave_none(miss != 0u)) break;
        if (FSLEEP) __builtin_amdgcn_s_sleep(FSLEEP);
      }
      float v[4];
#pragma unroll
      for (int k = 0; k < 4; ++k) v[k] = (u2f(gf[0][k]) + u2f(gf[1][k])) + (u2f(gf[2][k]) + u2f(gf[3][k]));
      float r = fold16(fold32(v[0], v[1]), fold32(v[2], v[3]));
      r += dpp<ROR8>(r); r += dpp<ROR4>(r); r += dpp<XOR2>(r);
      __builtin_amdgcn_raw_buffer_store_b16((unsigned short)pkbf(r, r), mk(out, NB * DM * 2), oo2, 0, 0);
      if (id == 0 && lane == 0) __builtin_amdgcn_raw_buffer_store_b32(t, rW, WS_E, 0, AUX_SC1);
    }
  }
  STAMP(10);
  if (id == AMP_ID && wv == AMP_W) {
    const unsigned long long now = __builtin_amdgcn_s_memrealtime();
    const unsigned long long until = now + AMP_A * (stamp[AMP_K] - stamp[AMP_K0]);
    while (__builtin_amdgcn_s_memrealtime() < until) __builtin_amdgcn_s_sleep(1);
  }
}

}

extern "C" void kernel_launch(void* const* d_in, const int* in_sizes, int n_in, void* d_out, int out_size, void* d_ws, size_t ws_size,
                              hipStream_t stream) {
  (void)in_sizes; (void)n_in; (void)out_size; (void)ws_size;
  Touch tt;
  {
    const char* const kc = (const char*)d_in[1]; const char* const vc = (const char*)d_in[2];
    const char* const wq = (const char*)d_in[3]; const char* const wo = (const char*)d_in[4];
    const char* const w1 = (const char*)d_in[6]; const char* const w2 = (const char*)d_in[7];
    constexpr unsigned HEAD = SK * DH * 2, W0 = (SK - WIN) * DH * 2, WB = WIN * DH * 2, WQB = DM * NQ * 2, W1B = DM * DFF * 2;
    for (int n = 0; n < 16; ++n)
      tt.e[n] = TouchEntry{((n >= 4 && n < 12) ? vc : kc) + (size_t)(n & 3) * HKV * HEAD + W0 + ((n >> 2) & 1 ? 0 : WB - 128), HEAD, 0u};
    const char* const rest[16] = {wq, wq + WQB / 2, wq + WQB - 1024, w1, w1 + W1B - 1024, w2, w2 + W1B - 1024, wo, wo + WQB / 8 - 128,
                                  (const char*)d_in[0], (const char*)d_in[8], (const char*)d_out, (const char*)d_ws + 1024,
                                  (const char*)d_ws + WS_O - 1024, (const char*)d_ws + WS_END, vc + W0 + WB / 2};
    const unsigned step[16] = {128, 128, 128, 128, 128, 128, 128, WQB / 8, WQB / 8, 128, 112, 128, 128, 128, 128, HEAD};
    for (int n = 0; n < 16; ++n) tt.e[16 + n] = TouchEntry{rest[n], step[n], 0u};
  }
  gated_mix_decode_kernel<<<256, 512, 0, stream>>>(
      (const unsigned short*)d_in[0], (const unsigned short*)d_in[1], (const unsigned short*)d_in[2], (const unsigned short*)d_in[3],
      (const unsigned short*)d_in[4], (const unsigned short*)d_in[6], (const unsigned short*)d_in[7], (const unsigned short*)d_in[8],
      (unsigned short*)d_out, (unsigned char*)d_ws, tt);
}
```

```cpp
#include <hip/hip_runtime.h>
#include <hip/hip_bf16.h>
#include <cstdint>

namespace {

typedef unsigned v4u __attribute__((ext_vector_type(4)));
typedef unsigned v2u __attribute__((ext_vector_type(2)));
typedef unsigned v16u __attribute__((ext_vector_type(16)));
typedef float f32x4 __attribute__((ext_vector_type(4)));
typedef float f32x2 __attribute__((ext_vector_type(2)));
typedef short s16x4 __attribute__((ext_vector_type(4)));
typedef __bf16 bf16x8 __attribute__((ext_vector_type(8)));
typedef __bf16 bf16x2 __attribute__((ext_vector_type(2)));
typedef __amdgpu_buffer_rsrc_t rsrc_t;
typedef __attribute__((address_space(3))) unsigned lds_u32;

constexpr int NB = 4, DM = 448, HKV = 8, SK = 8192, DH = 256, WIN = 2048, DFF = 1792, NQ = 4096;
constexpr int AUX_SC1 = 16;
constexpr int OOB = 0x40000000;
constexpr unsigned SPIN_CAP = 200000, TAG_MASK = 1023u;

#define WAUX 2
#define KVAUX 2
#define THR 4
#define QG_AT 22
#define VB2 4
#define KPRE 2
#define CODEPF 1
#define CODE_BYTES 12928
#define ILV 1
#define ADAPT 1
#define ADAPT_INIT 2
#define PACE_A 80
#define PACE_T 160
#define PACE_D 25
#define PACE_MAX 3
#define HDRY 0
#define ODELAY 60
#define FDELAY 14
#define FSLEEP 0
#define TWN 8
#define OPOS 2
#define STAUX 16
#define QLAUX 16
#define PWAUX 16
#define TLAUX 16
#define KB0 8
#define KBURST 8
#define JSKIP 3
#define SLOTX 1
#define TLBW 2
#define MIDT 0
#define LAGW 10
#define HFIRST 1
#define MIDW 6
#define PACE_EVERY 4
#define PACE0 11
#define THR0 THR
#define GREF 3
#define GR_LATE 100
#define PACE_B 300
#define TPRE 0
#define PREWARM 1
#define PSLEEP 0
#define ABL 0

constexpr int WS_E = 0;
constexpr int WS_T = 4096;
constexpr int WS_Q = WS_T + 8192;
constexpr int WS_H = WS_Q + 8 * 4 * 8192;
constexpr int WS_O = WS_H + 28 * 4 * 2048 + 4096;
constexpr int WS_F = WS_O + 16 * 16 * 4096;
constexpr int WS_END = WS_F + 2 * 28 * 16384;
constexpr int WS_DIAG = 8 << 20;

constexpr int OM_ROW = 68 * 4, FM_ROW = 544;
constexpr int L_QP = 0;
constexpr int L_Q = 8192;
constexpr int L_P = 9216;
constexpr int L_Z = 10240;
constexpr int L_CNT = 10304;
constexpr int L_TAG = 10320;
constexpr int L_HP = 10368;
constexpr int L_ST = 10432;
constexpr int L_OM = 10496;
constexpr int L_FM = L_OM + 32 * OM_ROW;
constexpr int L_END = L_FM + 32 * FM_ROW;

__device__ __forceinline__ rsrc_t mk(const void* p, int bytes) { return __builtin_amdgcn_make_buffer_rsrc((void*)p, 0, bytes, 0x00020000); }
template <int AUX> __device__ __forceinline__ v4u ld16(rsrc_t r, int vo, int so) { return __builtin_amdgcn_raw_buffer_load_b128(r, vo, so, AUX); }
__device__ __forceinline__ int oob_if(bool c) { return c ? OOB : 0; }
__device__ __forceinline__ float u2f(unsigned u) { return __builtin_bit_cast(float, u); }
__device__ __forceinline__ unsigned f2u(float f) { return __builtin_bit_cast(unsigned, f); }
__device__ __forceinline__ unsigned tagged(float v, unsigned t, unsigned keep) { return (f2u(v) & keep) | t; }
__device__ __forceinline__ void publish(rsrc_t r, int vo, float a, float b, float c, float d, unsigned t, unsigned keep = ~TAG_MASK) {
  __builtin_amdgcn_raw_buffer_store_b128(v4u{tagged(a, t, keep), tagged(b, t, keep), tagged(c, t, keep), tagged(d, t, keep)}, r, vo, 0, STAUX);
}
__device__ __forceinline__ unsigned untag(v4u& g, unsigned t) { g ^= t; return (g.x | g.y | g.z | g.w) & TAG_MASK; }
__device__ __forceinline__ bool wave_none(bool p) { return __builtin_amdgcn_ballot_w64(p) == 0; }
__device__ __forceinline__ float dot2(unsigned a, unsigned b, float c) {
  return __builtin_amdgcn_fdot2_f32_bf16(__builtin_bit_cast(bf16x2, a), __builtin_bit_cast(bf16x2, b), c, false);
}
__device__ __forceinline__ unsigned pkbf(float lo, float hi) {
  const bf16x2 v = {(__bf16)lo, (__bf16)hi};
  return __builtin_bit_cast(unsigned, v);
}
__device__ __forceinline__ unsigned perm_lo(unsigned a, unsigned b) { return __builtin_amdgcn_perm(b, a, 0x05040100u); }
__device__ __forceinline__ unsigned perm_hi(unsigned a, unsigned b) { return __builtin_amdgcn_perm(b, a, 0x07060302u); }
template <int CTRL> __device__ __forceinline__ unsigned dppu(unsigned x) { return (unsigned)__builtin_amdgcn_mov_dpp((int)x, CTRL, 0xf, 0xf, true); }
template <int CTRL> __device__ __forceinline__ float dpp(float x) { return u2f(dppu<CTRL>(f2u(x))); }
constexpr int XOR1 = 0xB1, XOR2 = 0x4E, XOR7 = 0x141, ROR8 = 0x128, ROR4 = 0x124;
__device__ __forceinline__ float fmx(float a, float c) { return __builtin_amdgcn_fmed3f(a, c, __builtin_inff()); }
__device__ __forceinline__ float fold16(float a, float b) {
  auto s = __builtin_amdgcn_permlane16_swap(f2u(a), f2u(b), false, false);
  return u2f(s[0]) + u2f(s[1]);
}
__device__ __forceinline__ float fold32(float a, float b) {
  auto s = __builtin_amdgcn_permlane32_swap(f2u(a), f2u(b), false, false);
  return u2f(s[0]) + u2f(s[1]);
}
__device__ __forceinline__ float x16_sum(float x) { return fold16(x, x); }
__device__ __forceinline__ float x32_sum(float x) { return fold32(x, x); }
__device__ __forceinline__ float x16_max(float x) {
  auto s = __builtin_amdgcn_permlane16_swap(f2u(x), f2u(x), false, false);
  return fmx(u2f(s[0]), u2f(s[1]));
}
__device__ __forceinline__ float x32_max(float x) {
  auto s = __builtin_amdgcn_permlane32_swap(f2u(x), f2u(x), false, false);
  return fmx(u2f(s[0]), u2f(s[1]));
}
__device__ __forceinline__ float row_sum(float x) { x += dpp<XOR1>(x); x += dpp<XOR2>(x); x += dpp<ROR4>(x); x += dpp<ROR8>(x); return x; }
__device__ __forceinline__ float row_max(float x) {
  x = fmx(x, dpp<XOR1>(x)); x = fmx(x, dpp<XOR2>(x)); x = fmx(x, dpp<ROR4>(x)); x = fmx(x, dpp<ROR8>(x)); return x;
}
__device__ __forceinline__ f32x4 mfma32(v4u a, v4u b, f32x4 c) {
  return __builtin_amdgcn_mfma_f32_16x16x32_bf16(__builtin_bit_cast(bf16x8, a), __builtin_bit_cast(bf16x8, b), c, 0, 0, 0);
}
__device__ __forceinline__ f32x4 mfma16(v2u a, v2u b, f32x4 c) {
  return __builtin_amdgcn_mfma_f32_16x16x16bf16_1k(__builtin_bit_cast(s16x4, a), __builtin_bit_cast(s16x4, b), c, 0, 0, 0);
}

#define SB() __builtin_amdgcn_sched_barrier(0)
#define REREAD(off) asm volatile("" : "+v"(off) :: "memory")
#define VMWAIT(n) __builtin_amdgcn_s_waitcnt(((n) & 15) | (7 << 4) | (15 << 8) | ((((n) >> 4) & 3) << 14))
#define THROTTLE() do { SB(); VMWAIT(LAGW - 1); \
    asm volatile("s_cmp_gt_i32 %0, 0\n\ts_cbranch_scc1 1f\n\ts_waitcnt vmcnt(%1)\n1:" :: "s"(lag), "i"(THR) : "scc"); SB(); } while (0)
#define PACE(k) do { lag = (int)((unsigned)__builtin_amdgcn_s_memrealtime() - tstart) - (PACE_A + (PACE_T * (k)) / 10); SB(); } while (0)
#define LDSBAR() do { SB(); __builtin_amdgcn_fence(__ATOMIC_RELEASE, "workgroup", "local"); __builtin_amdgcn_s_barrier(); \
                      __builtin_amdgcn_fence(__ATOMIC_ACQUIRE, "workgroup", "local"); SB(); } while (0)

__device__ __forceinline__ void lds_arrive(unsigned char* lds, int off, int lane) {
  __builtin_amdgcn_fence(__ATOMIC_RELEASE, "workgroup", "local");
  if (lane == 0) __hip_atomic_fetch_add((lds_u32*)(lds + off), 1u, __ATOMIC_RELAXED, __HIP_MEMORY_SCOPE_WORKGROUP);
}
__device__ __forceinline__ void lds_wait(unsigned char* lds, int off, unsigned want) {
  _Pragma("nounroll") for (unsigned spin = 0; spin < 16u * SPIN_CAP; ++spin) {
    if ((unsigned)__builtin_amdgcn_readfirstlane(__hip_atomic_load((lds_u32*)(lds + off), __ATOMIC_RELAXED, __HIP_MEMORY_SCOPE_WORKGROUP)) >= want) break;
    __builtin_amdgcn_s_sleep(1);
  }
  __builtin_amdgcn_fence(__ATOMIC_ACQUIRE, "workgroup", "local");
}

#define STAMP(k) do { } while (0)

struct TouchEntry { const void* p; unsigned stride, zero; };
struct Touch { TouchEntry e[32]; };

__global__ void __launch_bounds__(512, 2)
gated_mix_decode_kernel(const unsigned short*, const unsigned short*, const unsigned short*, const unsigned short*, const unsigned short*,
                        const unsigned short*, const unsigned short*, const unsigned short*, unsigned short*, unsigned char*, Touch) {
  __shared__ __attribute__((aligned(128))) unsigned char lds[L_END];
  STAMP(0);
  unsigned tstart = (unsigned)__builtin_amdgcn_s_memrealtime();
  v16u ka; v4u kb;
  v4u tp;
  asm volatile("s_load_dwordx4 %2, %3, %4 offset:0x50\n\ts_load_dwordx16 %0, %3, 0x0\n\ts_load_dwordx4 %1, %3, 0x40"
               : "=&s"(ka), "=&s"(kb), "=&s"(tp) : "s"(__builtin_amdgcn_kernarg_segment_ptr()), "s"((blockIdx.x >> 3) * 16u) : "memory");
  SB();
  unsigned cpf;
  {
    unsigned long long pc;
    asm volatile("s_getpc_b64 %0" : "=s"(pc));
    constexpr int PER = (CODE_BYTES / 128 + 3) / 4;
    const int line = ((blockIdx.x >> 3) & 3) * PER + threadIdx.x;
    cpf = __builtin_amdgcn_raw_buffer_load_b32(mk((const void*)pc, CODE_BYTES), (line * 128) | oob_if(threadIdx.x >= PER), 0, 0);
    SB();
  }
  const int id = blockIdx.x, kvh = id & 7, b = (id >> 3) & 3, j = id >> 5;
  const int lane = threadIdx.x & 63, wv = __builtin_amdgcn_readfirstlane(threadIdx.x >> 6), w = wv & 3;
  const bool helper = wv < 4;
  const int blk0 = j * 16384 + w * 4096;
  int fo;
  {
    const int base = helper ? (112 * b + 32 * w) * (NQ * 2) + (kvh * 8 + j) * 128 : blk0;
    const int rstr = helper ? 4 * NQ * 2 : 512, rlim = wv == 3 ? 4 : 8;
    fo = (base + (lane >> 3) * rstr + (lane & 7) * 16) | oob_if((lane >> 3) >= rlim);
    asm volatile("" : "+v"(fo));
  }
  const int m16 = lane & 15, g4 = lane >> 4;
  int xo = (((m16 >> 2) * DM + 112 * b + 32 * w + 8 * g4 + 4 * (m16 & 1)) * 2) | oob_if(!helper | ((m16 & 2) != 0) | (32 * w + 8 * g4 >= 112));
  int eo = WS_E | oob_if(!helper);
  asm volatile("" : "+v"(xo), "+v"(eo));
  asm volatile("s_waitcnt lgkmcnt(0)" : "+s"(ka), "+s"(kb), "+s"(tp) :: "memory");
  const unsigned tw = __builtin_amdgcn_raw_buffer_load_b32(mk((const void*)(((unsigned long long)tp.y << 32) | tp.x), OOB), oob_if(threadIdx.x != 0), tp.z * kvh, 2);
  SB();
#define KARG(T, lo, hi) ((T)(((unsigned long long)(hi) << 32) | (lo)))
  const unsigned short* x = KARG(const unsigned short*, ka[0], ka[1]);
  const unsigned short* kc = KARG(const unsigned short*, ka[2], ka[3]);
  const unsigned short* vc = KARG(const unsigned short*, ka[4], ka[5]);
  const unsigned short* wq = KARG(const unsigned short*, ka[6], ka[7]);
  const unsigned short* wo = KARG(const unsigned short*, ka[8], ka[9]);
  const unsigned short* w1 = KARG(const unsigned short*, ka[10], ka[11]);
  const unsigned short* w2 = KARG(const unsigned short*, ka[12], ka[13]);
  const unsigned short* gw = KARG(const unsigned short*, ka[14], ka[15]);
  unsigned short* out = KARG(unsigned short*, kb[0], kb[1]);
  unsigned char* ws = KARG(unsigned char*, kb[2], kb[3]);
  const rsrc_t rW = mk(ws, WS_END), rX = mk(x, NB * DM * 2);
  const v2u xq = __builtin_amdgcn_raw_buffer_load_b64(rX, xo, 0, 0);
  const unsigned ep = __builtin_amdgcn_raw_buffer_load_b32(rW, eo, 0, AUX_SC1);
  SB();
  const size_t head = ((size_t)(id & 31) * SK + (SK - WIN)) * DH;
  const rsrc_t rF = mk(helper ? wq : kc + head, helper ? DM * NQ * 2 : WIN * DH * 2);
  const int fstep = helper ? NQ * 2 : 128;
  v4u first[4];
#pragma unroll
  for (int i = 0; i < 4; ++i) first[i] = ld16<2>(rF, fo, i * fstep);
  SB();
  const int j1 = j == 7 ? JSKIP : j;
  const int bp = b >> 1, r16 = (b & 1) * 8 + j, pu = kvh * 16 + r16, fs = kvh * 14 + r16;
  const f32x4 zero4 = {0.f, 0.f, 0.f, 0.f};

  if (__builtin_expect(helper, HFIRST)) {
    const int tid = threadIdx.x;
    const rsrc_t r1 = mk(w1, DM * DFF * 2), rG = mk(gw, DM * 2);
    const v4u (&wqr)[4] = first;
    v4u w1r[2];
    v2u xh;
    {
      const int rr = 2 * g4 + (m16 & 1);
      xh.x = __builtin_amdgcn_raw_buffer_load_b32(rX, (((m16 >> 2) * DM + 56 * kvh + 16 * w + 2 * rr) * 2) | oob_if(((m16 & 2) != 0) | (16 * w + 2 * rr >= 56)), 0, 0);
      xh.y = 0u;
      const int row1 = 16 * w + 2 * (lane >> 3);
      const int o1 = ((56 * kvh + row1) * (DFF * 2) + (4 * j1 + b) * 128 + (lane & 7) * 16) | oob_if((j == JSKIP) | (row1 >= 56));
#pragma unroll
      for (int i = 0; i < 2; ++i) w1r[i] = ld16<WAUX>(r1, o1, i * (DFF * 2));
    }
    SB();
    STAMP(11);
    if (tid < 24) *(unsigned*)(lds + L_Z + tid * 4) = 0u;
#define TILE_PARTIAL(AF, BLO, BHI, LOFF) do { float pv[8]; \
      _Pragma("unroll") for (int dw = 0; dw < 4; ++dw) { \
        const f32x4 dl = mfma16(AF, BLO, zero4), dh = mfma16(AF, BHI, zero4); \
        const float sl = (lane & 8) ? dl[1] : dl[0], sh = (lane & 8) ? dh[1] : dh[0]; \
        pv[2 * dw] = sl + dpp<ROR8>(sl); pv[2 * dw + 1] = sh + dpp<ROR8>(sh); } \
      *(f32x4*)(lds + (LOFF) + ((w * 4 + g4) * 64 + 8 * (lane & 7) + ((lane & 8) >> 1)) * 4) = \
          (lane & 8) ? f32x4{pv[4], pv[5], pv[6], pv[7]} : f32x4{pv[0], pv[1], pv[2], pv[3]}; } while (0)
#define TILE_PUBLISH(LOFF, DST) do { f32x4 s = zero4; \
      _Pragma("unroll") for (int ww = 0; ww < 4; ++ww) s += *(const f32x4*)(lds + (LOFF) + ((ww * 4 + w) * 64 + 4 * m16) * 4); \
      publish(rW, (DST) | oob_if(lane >= 16), s[0], s[1], s[2], s[3], t); SB(); } while (0)
    TILE_PARTIAL(xq, (v2u{perm_lo(wqr[0][dw], wqr[1][dw]), perm_lo(wqr[2][dw], wqr[3][dw])}),
                 (v2u{perm_hi(wqr[0][dw], wqr[1][dw]), perm_hi(wqr[2][dw], wqr[3][dw])}), L_QP);
    const unsigned t = ((unsigned)__builtin_amdgcn_readfirstlane(ep) + 1u) & TAG_MASK;
    asm volatile("" :: "v"(tw));
    asm volatile("" :: "v"(cpf));
    STAMP(1);
    LDSBAR();
    tstart = (unsigned)__builtin_amdgcn_s_memrealtime();
    TILE_PUBLISH(L_QP, WS_Q + (kvh * 4 + w) * 8192 + j * 1024 + b * 256 + m16 * 16);
    STAMP(2);
    {
      const unsigned tv0 = (tstart << 10) | t;
      __builtin_amdgcn_raw_buffer_store_b128(v4u{tv0, tv0, tv0, tv0}, rW, (WS_T + (tid >> 3) * 1024 + kvh * 128 + (tid & 7) * 16) | oob_if((id >= 8) | (tid >= 64)), 0, AUX_SC1);
      SB();
    }
    TILE_PARTIAL(xh, (v2u{perm_lo(w1r[0][dw], w1r[1][dw]), 0u}), (v2u{perm_hi(w1r[0][dw], w1r[1][dw]), 0u}), L_QP + 4096);
    lds_arrive(lds, L_CNT, lane);
    lds_wait(lds, L_CNT, 4);
    TILE_PUBLISH(L_QP + 4096, (WS_H + ((4 * j1 + b) * 4 + w) * 2048 + kvh * 256 + m16 * 16) | oob_if(j == JSKIP));
    STAMP(3);
    lds_wait(lds, L_CNT + 8, 1);
    STAMP(5);
    {
      int qgo = WS_Q + (kvh * 4 + b) * 8192 + (2 * w + (lane >> 5)) * 1024 + (g4 & 1) * 256 + m16 * 16;
      const unsigned gt = __builtin_amdgcn_raw_buffer_load_b32(rW, (WS_T + kvh * 1024 + lane * 128) | oob_if((w != 2) | (lane >= 8)), 0, AUX_SC1);
      v4u g0, g1;
      _Pragma("nounroll") for (unsigned spin = 0; spin < SPIN_CAP; ++spin) {
        REREAD(qgo);
        g0 = ld16<AUX_SC1>(rW, qgo, 0);
        g1 = ld16<AUX_SC1>(rW, qgo, 512);
        if (wave_none((untag(g0, t) | untag(g1, t)) != 0u)) break;
        __builtin_amdgcn_s_sleep(8);
      }
      float q[4];
#pragma unroll
      for (int k = 0; k < 4; ++k) q[k] = x16_sum(u2f(g0[k]) + u2f(g1[k]));
      {
        const int d = (int)(gt - (tstart << 10)) >> 10;
        const float sum = row_sum(((gt & TAG_MASK) == t && lane < 8) ? (float)d : 0.f);
        if (tid == 128) *(int*)(lds + L_TAG + 12) = (int)(sum * 0.125f) + (GREF == 3 ? (int)tstart : 0);
      }
      if ((g4 & 1) == 0) *(v2u*)(lds + L_Q + ((2 * w + (lane >> 5)) * 64 + 4 * m16) * 2) = v2u{pkbf(q[0], q[1]), pkbf(q[2], q[3])};
    }
    STAMP(4);
    LDSBAR();
    if (PREWARM) {
      const int po = WS_O + ((bp * 8 + kvh) * 16 + r16) * 4096 + (lane & 31) * 128;
      const int pf = WS_F + (bp * 28 + (pu >> 2)) * 16384 + (w * 32 + (lane & 31)) * 128;
      const unsigned pw = __builtin_amdgcn_raw_buffer_load_b32(rW, lane < 32 ? (w == 0 ? po : OOB) : (((PREWARM & 2) && pu < 112) ? pf : OOB), 0, PWAUX);
      asm volatile("" :: "v"(pw));
    }
    if (w == 3) {
      const int go_ = (lane * 16) | oob_if(lane >= 56);
      const v4u xg0 = ld16<0>(rX, go_, 2 * bp * DM * 2), xg1 = ld16<0>(rX, go_, (2 * bp + 1) * DM * 2), gg = ld16<0>(rG, go_, 0);
      const bool hon = r16 < 14;
      int hgo = (WS_H + ((fs >> 2) * 4 + 2 * bp + (lane >> 5)) * 2048 + ((lane >> 2) & 7) * 256 + (fs & 3) * 64 + (lane & 3) * 16) | oob_if(!hon);
      v4u gh;
      _Pragma("nounroll") for (unsigned spin = 0; spin < SPIN_CAP; ++spin) {
        REREAD(hgo);
        gh = ld16<AUX_SC1>(rW, hgo, 0);
        if (wave_none(hon & (untag(gh, t) != 0u))) break;
        __builtin_amdgcn_s_sleep(8);
      }
      float d0 = 0.f, d1 = 0.f;
#pragma unroll
      for (int k = 0; k < 4; ++k) { d0 = dot2(xg0[k], gg[k], d0); d1 = dot2(xg1[k], gg[k], d1); }
      const float gate = 1.f / (1.f + __expf(-x16_sum(row_sum(fold32(d0, d1)))));
      if ((lane & 31) == 0) *(float*)(lds + L_TAG + 4 + 4 * (lane >> 5)) = gate;
      if (lane == 1) *(unsigned*)(lds + L_TAG) = t;
      float h[4];
#pragma unroll
      for (int k = 0; k < 4; ++k) {
        float a = hon ? u2f(gh[k]) : 0.f;
        a += dpp<ROR4>(a);
        a += dpp<ROR8>(a);
        h[k] = fmaxf(x16_sum(a), 0.f) * (1.f - gate);
      }
      if ((lane & 28) == 0) *(v2u*)(lds + L_HP + (lane >> 5) * 32 + (lane & 3) * 8) = v2u{pkbf(h[0], h[1]), pkbf(h[2], h[3])};
    }
    if (MIDT && w == 1) {
      const unsigned char* p = lane == 0 ? (const unsigned char*)(wo + (kvh * 512 + 32 * r16) * DM)
                             : lane == 1 ? (const unsigned char*)(w2 + (r16 < 14 ? 16 * fs : 0) * DM)
                             : lane == 2 ? (const unsigned char*)out + (id & 15) * 128 : ws + WS_END + 4096 + id * 128;
      unsigned mt = 0;
      if (lane < 4) mt = __builtin_nontemporal_load((const __attribute__((address_space(1))) unsigned*)p);
      asm volatile("" :: "v"(mt));
    }
    lds_wait(lds, L_CNT + 4, 4);
    float lse;
    f32x4 fc;
    {
      const int h = tid >> 7;
      const f32x4 e0 = *(const f32x4*)(lds + L_ST + h * 32), e1 = *(const f32x4*)(lds + L_ST + h * 32 + 16);
      const float M = fmx(fmx(e0[0], e0[2]), fmx(e1[0], e1[2]));
      fc = f32x4{__builtin_amdgcn_exp2f(e0[0] - M), __builtin_amdgcn_exp2f(e0[2] - M), __builtin_amdgcn_exp2f(e1[0] - M), __builtin_amdgcn_exp2f(e1[2] - M)};
      const float L = (fc[0] * e0[1] + fc[1] * e0[3]) + (fc[2] * e1[1] + fc[3] * e1[3]);
      fc *= 1.f / L;
      lse = M + __builtin_amdgcn_logf(L);
    }
    float lse_hi = u2f(f2u(lse) & ~TAG_MASK), lse_lo = lse - lse_hi;
    int oa = L_OM + 2 * (tid & 3) * OM_ROW + (tid >> 2) * 4, fa = L_FM + 2 * (tid & 3) * FM_ROW + (tid >> 2) * 8;
    int odst = (ABL & 128) ? OOB : WS_O + ((bp * 8 + kvh) * 32 + (tid >> 3)) * 2048 + (r16 ^ (SLOTX ? ((tid >> 3) & 7) : 0)) * 128 + (tid & 7) * 16;
    int fdst = tid < 224 ? WS_F + (bp * 28 + (tid >> 3)) * 16384 + (pu ^ (SLOTX ? ((tid >> 3) & 7) : 0)) * 128 + (tid & 7) * 16 : OOB;
    asm volatile("" : "+v"(lse_hi), "+v"(lse_lo), "+v"(fc), "+v"(oa), "+v"(fa), "+v"(odst), "+v"(fdst));
    STAMP(6);
    int pass = HDRY ? 0 : 1;
    unsigned tv = t, keep = ~TAG_MASK;
    asm volatile("" : "+s"(pass), "+v"(tv), "+s"(keep));
    _Pragma("nounroll") for (; pass < 2; ++pass) {
      const int live = pass ? 0 : OOB;
      if (pass) LDSBAR();
      {
        float a = 0.f, c = 0.f;
#pragma unroll
        for (int ww = 0; ww < 4; ++ww) {
          a = fmaf(fc[ww], *(const float*)(lds + oa + ww * 8 * OM_ROW), a);
          c = fmaf(fc[ww], *(const float*)(lds + oa + ww * 8 * OM_ROW + OM_ROW), c);
        }
        publish(rW, odst | live, a, c, lse_hi, lse_lo, tv, keep);
      }
      STAMP(7);
      if (pass) LDSBAR();
      {
        f32x2 s0 = {0.f, 0.f}, s1 = {0.f, 0.f};
#pragma unroll
        for (int ww = 0; ww < 4; ++ww) {
          s0 += *(const f32x2*)(lds + fa + ww * 8 * FM_ROW);
          s1 += *(const f32x2*)(lds + fa + ww * 8 * FM_ROW + FM_ROW);
        }
        publish(rW, fdst | live, s0.x, s0.y, s1.x, s1.y, tv, keep);
        SB();
        if (pass) lds_arrive(lds, L_CNT + 12, lane);
      }
    }
    STAMP(9);
  } else {
    const rsrc_t rV = mk(vc + head, WIN * DH * 2);
    constexpr int BSTR = 131072;
    v4u kr[32], vr[32];
    int lag = 0;
#define KLOAD(i) kr[i] = ld16<KVAUX>(rF, fo, ((i) >> 2) * BSTR + ((i) & 3) * 128)
#pragma unroll
    for (int i = 0; i < 4; ++i) kr[i] = first[i];
    STAMP(1);
#pragma unroll
    for (int i = 4; i < 32; ++i) {
      if (i < 8 && i >= KBURST) { SB(); VMWAIT(KBURST - 1); SB(); } else if (i > PACE0) THROTTLE(); else if (i >= 8) { SB(); VMWAIT(THR0); SB(); }
      KLOAD(i);
      if (i == PACE0 || (i > PACE0 && (i % PACE_EVERY) == PACE_EVERY - 1)) PACE(i);
      if (i == KB0) { LDSBAR(); STAMP(2); }
      if (i == QG_AT && w == 0) lds_arrive(lds, L_CNT + 8, lane);
    }
    const int vo = blk0 + lane * 16;
#define VLOAD(i) vr[i] = ld16<KVAUX>(rV, vo, ((i) >> 2) * BSTR + ((i) & 3) * 1024)
#define VSTEP(i) do { THROTTLE(); VLOAD(i); SB(); if (((i) % PACE_EVERY) == PACE_EVERY - 1) PACE(32 + (i)); } while (0)
    asm volatile("" :: "v"(cpf));
    asm volatile("" :: "v"(tw));
    asm volatile("" :: "v"(xq), "v"(ep));
    STAMP(3);
#pragma unroll
    for (int i = 0; i < VB2; ++i) { if (PACE0 < 32) VSTEP(i); else { SB(); VMWAIT(THR0); SB(); VLOAD(i); SB(); } }
    LDSBAR();
    STAMP(4);
    tstart = __builtin_amdgcn_readfirstlane(*(const int*)(lds + L_TAG + 12)) + (PACE_B - PACE_A - (PACE_T * 31) / 10);
    PACE(31 + VB2);
    v4u pa[4];
    {
      v4u qf[2][4];
#pragma unroll
      for (int h = 0; h < 2; ++h)
#pragma unroll
        for (int t = 0; t < 4; ++t) qf[h][t] = *(const v4u*)(lds + L_Q + (h * 256 + 64 * t + 8 * (lane & 7)) * 2);
      VSTEP(VB2);
      float s[8][2];
#pragma unroll
      for (int i = 0; i < 8; i += 2) {
        float a0 = 0.f, a1 = 0.f, c0 = 0.f, c1 = 0.f;
#pragma unroll
        for (int t = 0; t < 4; ++t) {
#pragma unroll
          for (int dw = 0; dw < 4; ++dw) {
            a0 = dot2(kr[4 * i + t][dw], qf[0][t][dw], a0); c0 = dot2(kr[4 * i + 4 + t][dw], qf[0][t][dw], c0);
            a1 = dot2(kr[4 * i + t][dw], qf[1][t][dw], a1); c1 = dot2(kr[4 * i + 4 + t][dw], qf[1][t][dw], c1);
          }
          if (t == 1) VSTEP(VB2 + 1 + i);
        }
        s[i][0] = a0; s[i][1] = a1; s[i + 1][0] = c0; s[i + 1][1] = c1;
        VSTEP(VB2 + 2 + i);
      }
#pragma unroll
      for (int i = 0; i < 4; ++i)
#pragma unroll
        for (int h = 0; h < 2; ++h) {
          const float lo = s[i][h] + dpp<XOR7>(s[i][h]), hi = s[i + 4][h] + dpp<XOR7>(s[i + 4][h]);
          s[i][h] = (lane & 4) ? hi : lo;
        }
#pragma unroll
      for (int i = 0; i < 2; ++i)
#pragma unroll
        for (int h = 0; h < 2; ++h) {
          const float lo = s[i][h] + dpp<XOR2>(s[i][h]), hi = s[i + 2][h] + dpp<XOR2>(s[i + 2][h]);
          s[i][h] = (lane & 2) ? hi : lo;
        }
#pragma unroll
      for (int h = 0; h < 2; ++h) {
        const float lo = s[0][h] + dpp<XOR1>(s[0][h]), hi = s[1][h] + dpp<XOR1>(s[1][h]);
        s[0][h] = (lane & 1) ? hi : lo;
      }
      VSTEP(VB2 + 9);
      constexpr float C2 = 0.0625f * 1.4426950408889634f;
      const float z0 = s[0][0] * C2, z1 = s[0][1] * C2;
      const float m0 = x32_max(x16_max(row_max(z0))), m1 = x32_max(x16_max(row_max(z1)));
      const float p0 = __builtin_amdgcn_exp2f(z0 - m0), p1 = __builtin_amdgcn_exp2f(z1 - m1);
      const unsigned pp = pkbf(p0, p1);
      unsigned short* pl = (unsigned short*)(lds + L_P + w * 256 + ((lane >> 3) & 1) * 64 + (lane & 7) * 8 + (lane >> 4) * 2);
      pl[0] = (unsigned short)pp;
      pl[64] = (unsigned short)(pp >> 16);
      VSTEP(VB2 + 10);
      const float l01 = x16_sum(row_sum(fold32(p0, p1)));
      if ((lane & 31) == 0) *(f32x2*)(lds + L_ST + ((lane >> 5) * 4 + w) * 8) = f32x2{lane ? m1 : m0, l01};
      lds_arrive(lds, L_CNT + 4, lane);
      const bool valid = (m16 & 3) == 0 && ((m16 >> 2) & 1) == (g4 & 1);
      const int a = valid ? L_P + w * 256 + (m16 >> 3) * 128 + (g4 >> 1) * 64 : L_Z;
#pragma unroll
      for (int G = 0; G < 4; ++G) pa[G] = *(const v4u*)(lds + a + G * 16);
    }
    STAMP(5);
    f32x4 acc[8];
#pragma unroll
    for (int e = 0; e < 8; ++e) acc[e] = zero4;
    v4u tr[12], bf[8];
    const rsrc_t rO = mk(wo, NQ * DM * 2), r2 = mk(w2, DFF * DM * 2);
    const int o2 = (r16 < 14 && lane < 56) ? (16 * fs + 4 * w) * (DM * 2) + lane * 16 : OOB;
    const int oo = lane < 56 ? (kvh * 512 + 32 * r16 + 8 * w) * (DM * 2) + lane * 16 : OOB;
#define TLOAD(i) tr[i] = (i) < 4 ? ld16<WAUX>(r2, o2, (i) * (DM * 2)) : ld16<WAUX>(rO, oo, ((i) - 4) * (DM * 2))
#define TSTEP(i) do { THROTTLE(); TLOAD(i); SB(); } while (0)
#define PAIRUP(src, pr) do { _Pragma("unroll") for (int dw = 0; dw < 4; ++dw) { \
      bf[2 * dw][pr] = perm_lo((src)[2 * (pr)][dw], (src)[2 * (pr) + 1][dw]); \
      bf[2 * dw + 1][pr] = perm_hi((src)[2 * (pr)][dw], (src)[2 * (pr) + 1][dw]); } } while (0)
#define PIN_PAIR(pr) do { _Pragma("unroll") for (int e = 0; e < 8; ++e) { unsigned tmp_ = bf[e][pr]; asm volatile("" : "+v"(tmp_)); bf[e][pr] = tmp_; } SB(); } while (0)
#define PVMMA(G) do { _Pragma("unroll") for (int e = 0; e < 8; ++e) acc[e] = mfma32(pa[G], bf[e], acc[e]); } while (0)
#define PVGROUP(G) do { PAIRUP(vr + 8 * (G), 0); PAIRUP(vr + 8 * (G), 1); PAIRUP(vr + 8 * (G), 2); PAIRUP(vr + 8 * (G), 3); PVMMA(G); SB(); } while (0)
    constexpr int TIN = TPRE, TWIN = (TIN + TWN < 12) ? TIN + TWN : 12, V0 = VB2 + 11;
#pragma unroll
    for (int i = V0; i < 32; ++i) {
      VSTEP(i);
#pragma unroll
      for (int k = 0; k < TIN; ++k)
        if (V0 + (k * (31 - V0)) / (TIN ? TIN : 1) == i) TSTEP(k);
      if (i == V0) PVGROUP(0);
      if (i == 24) PVGROUP(1);
      if (i == 31) PVGROUP(2);
    }
#pragma unroll
    for (int k = 0; k < 8; ++k) {
      if (k + TWN >= 8 && TIN + k + TWN - 8 < TWIN) { SB(); VMWAIT(TWN - 1); SB(); TLOAD(TIN + k + TWN - 8); SB(); }
      if (k == 1) { PAIRUP(vr + 24, 0); PIN_PAIR(0); }
      if (k == 3) { PAIRUP(vr + 24, 1); PIN_PAIR(1); }
      if (k == 5) { PAIRUP(vr + 24, 2); PIN_PAIR(2); }
    }
    int oma = L_OM + w * 8 * OM_ROW + lane * 4;
    asm volatile("" : "+v"(oma));
    SB();
    PAIRUP(vr + 24, 3);
    PVMMA(3);
    STAMP(6);
#pragma unroll
    for (int e = 0; e < 8; ++e) *(float*)(lds + oma + e * OM_ROW) = acc[e][0];
    LDSBAR();
    const unsigned tb3 = ODELAY > 0 ? (unsigned)__builtin_amdgcn_s_memrealtime() : 0u;
    STAMP(7);
#pragma unroll
    for (int i = TWIN; i < 12; ++i) TLOAD(i);
    SB();
    const bool diag = (m16 >> 2) == g4;
    const v4u tg = *(const v4u*)(lds + L_TAG);
    const v2u ah = *(const v2u*)(lds + ((diag && (m16 & 3) < 2) ? L_HP + (m16 & 3) * 32 + w * 8 : L_Z));
    const unsigned t = tg.x;
#pragma unroll
    for (int dw = 0; dw < 4; ++dw) {
      acc[2 * dw] = mfma16(ah, v2u{perm_lo(tr[0][dw], tr[1][dw]), perm_lo(tr[2][dw], tr[3][dw])}, zero4);
      acc[2 * dw + 1] = mfma16(ah, v2u{perm_hi(tr[0][dw], tr[1][dw]), perm_hi(tr[2][dw], tr[3][dw])}, zero4);
    }
    SB();
    int ogo = WS_O + (((bp * 8 + kvh) * 16 + r16) * 2 + (w >> 1)) * 2048 + (lane >> 2) * 128 + (w & 1) * 64 + (lane & 3) * 16;
    float gate = u2f(lane < 32 ? tg.y : tg.z);
    unsigned m0 = (diag && (lane & 1) == 0) ? 0xffffffffu : 0u, m1 = (diag && (lane & 1) != 0) ? 0xffffffffu : 0u;
    int fma_ = L_FM + w * 8 * FM_ROW + lane * 8;
    asm volatile("" : "+v"(gate), "+v"(m0), "+v"(m1), "+v"(fma_), "+v"(ogo));
    SB();
    if (OPOS >= 1) { PAIRUP(tr + 4, 0); PIN_PAIR(0); }
    if (OPOS >= 2) { PAIRUP(tr + 4, 1); PIN_PAIR(1); }
    if (OPOS >= 3) { PAIRUP(tr + 4, 2); PIN_PAIR(2); }
    if (OPOS >= 4) { PAIRUP(tr + 4, 3); PIN_PAIR(3); }
    if (ODELAY > 0) while ((int)((unsigned)__builtin_amdgcn_s_memrealtime() - tb3) < ODELAY) {}
    SB();
    v4u go = ld16<TLAUX>(rW, ogo, 0);
    SB();
    if (OPOS < 1) { PAIRUP(tr + 4, 0); PIN_PAIR(0); }
    if (OPOS < 2) { PAIRUP(tr + 4, 1); PIN_PAIR(1); }
    if (OPOS < 3) { PAIRUP(tr + 4, 2); PIN_PAIR(2); }
    if (OPOS < 4) { PAIRUP(tr + 4, 3); PIN_PAIR(3); }
    STAMP(11);
    if (!wave_none(untag(go, t) != 0u)) {
      _Pragma("nounroll") for (unsigned spin = 0; spin < SPIN_CAP; ++spin) {
        if (PSLEEP) __builtin_amdgcn_s_sleep(PSLEEP);
        REREAD(ogo);
        go = ld16<TLAUX>(rW, ogo, 0);
        if (wave_none(untag(go, t) != 0u)) break;
      }
    }
    STAMP(8);
    {
      const float lm = u2f(go.z) + u2f(go.w);
      float M = fmx(lm, dpp<ROR4>(lm));
      M = fmx(M, dpp<ROR8>(M));
      M = x16_max(M);
      float d = __builtin_amdgcn_exp2f(lm - M);
      float a = d * u2f(go.x), c = d * u2f(go.y);
      a += dpp<ROR4>(a); c += dpp<ROR4>(c); d += dpp<ROR4>(d);
      a += dpp<ROR8>(a); c += dpp<ROR8>(c); d += dpp<ROR8>(d);
      a = x16_sum(a); c = x16_sum(c);
      d = gate * __builtin_amdgcn_rcpf(x16_sum(d));
      const unsigned op = pkbf(a * d, c * d);
      const auto sw = __builtin_amdgcn_permlane32_swap(op, op, false, false);
      const v4u b0 = {dppu<0x00>(sw[0]), dppu<0x55>(sw[0]), dppu<0xAA>(sw[0]), dppu<0xFF>(sw[0])};
      const v4u b1 = {dppu<0x00>(sw[1]), dppu<0x55>(sw[1]), dppu<0xAA>(sw[1]), dppu<0xFF>(sw[1])};
      const v4u ao = (b0 & m0) | (b1 & m1);
#pragma unroll
      for (int e = 0; e < 8; ++e) acc[e] = mfma32(ao, bf[e], acc[e]);
#pragma unroll
      for (int e = 0; e < 8; ++e) *(f32x2*)(lds + fma_ + e * FM_ROW) = f32x2{acc[e][0], acc[e][1]};
    }
    LDSBAR();
    STAMP(9);
    if (w == 0 && pu < 112) {
      int fgo = WS_F + (bp * 28 + (pu >> 2)) * 16384 + (lane >> 1) * 128 + ((pu & 3) * 2 + (lane & 1)) * 16;
      const int oo2 = (lane & 14) == 0 ? ((2 * bp + (lane >> 5)) * DM + 4 * pu + 2 * (lane & 1) + (g4 & 1)) * 2 : OOB;
      v4u gf[4];
      lds_wait(lds, L_CNT + 12, 4);
      if (FDELAY) __builtin_amdgcn_s_sleep(FDELAY);
      _Pragma("nounroll") for (unsigned spin = 0; spin < SPIN_CAP; ++spin) {
        REREAD(fgo);
#pragma unroll
        for (int k = 0; k < 4; ++k) gf[k] = ld16<TLAUX>(rW, fgo, k * 4096);
        const unsigned miss = (untag(gf[0], t) | untag(gf[1], t)) | (untag(gf[2], t) | untag(gf[3], t));
        if (wave_none(miss != 0u)) break;
        if (FSLEEP) __builtin_amdgcn_s_sleep(FSLEEP);
      }
      float v[4];
#pragma unroll
      for (int k = 0; k < 4; ++k) v[k] = (u2f(gf[0][k]) + u2f(gf[1][k])) + (u2f(gf[2][k]) + u2f(gf[3][k]));
      float r = fold16(fold32(v[0], v[1]), fold32(v[2], v[3]));
      r += dpp<ROR8>(r); r += dpp<ROR4>(r); r += dpp<XOR2>(r);
      __builtin_amdgcn_raw_buffer_store_b16((unsigned short)pkbf(r, r), mk(out, NB * DM * 2), oo2, 0, 0);
      if (id == 0 && lane == 0) __builtin_amdgcn_raw_buffer_store_b32(t, rW, WS_E, 0, AUX_SC1);
    }
  }
}

}

extern "C" void kernel_launch(void* const* d_in, const int* in_sizes, int n_in, void* d_out, int out_size, void* d_ws, size_t ws_size,
                              hipStream_t stream) {
  (void)in_sizes; (void)n_in; (void)out_size; (void)ws_size;
  Touch tt;
  {
    const char* const kc = (const char*)d_in[1]; const char* const vc = (const char*)d_in[2];
    const char* const wq = (const char*)d_in[3]; const char* const wo = (const char*)d_in[4];
    const char* const w1 = (const char*)d_in[6]; const char* const w2 = (const char*)d_in[7];
    constexpr unsigned HEAD = SK * DH * 2, W0 = (SK - WIN) * DH * 2, WB = WIN * DH * 2, WQB = DM * NQ * 2, W1B = DM * DFF * 2;
    for (int n = 0; n < 16; ++n)
      tt.e[n] = TouchEntry{((n >= 4 && n < 12) ? vc : kc) + (size_t)(n & 3) * HKV * HEAD + W0 + ((n >> 2) & 1 ? 0 : WB - 128), HEAD, 0u};
    const char* const rest[16] = {wq, wq + WQB / 2, wq + WQB - 1024, w1, w1 + W1B - 1024, w2, w2 + W1B - 1024, wo, wo + WQB / 8 - 128,
                                  (const char*)d_in[0], (const char*)d_in[8], (const char*)d_out, (const char*)d_ws + 1024,
                                  (const char*)d_ws + WS_O - 1024, (const char*)d_ws + WS_END, vc + W0 + WB / 2};
    const unsigned step[16] = {128, 128, 128, 128, 128, 128, 128, WQB / 8, WQB / 8, 128, 112, 128, 128, 128, 128, HEAD};
    for (int n = 0; n < 16; ++n) tt.e[16 + n] = TouchEntry{rest[n], step[n], 0u};
  }
  gated_mix_decode_kernel<<<256, 512, 0, stream>>>(
      (const unsigned short*)d_in[0], (const unsigned short*)d_in[1], (const unsigned short*)d_in[2], (const unsigned short*)d_in[3],
      (const unsigned short*)d_in[4], (const unsigned short*)d_in[6], (const unsigned short*)d_in[7], (const unsigned short*)d_in[8],
      (unsigned short*)d_out, (unsigned char*)d_ws, tt);
}
```

```cpp
#define PROBE 1
#include <hip/hip_runtime.h>
#include <hip/hip_bf16.h>
#include <cstdint>

namespace {

typedef unsigned v4u __attribute__((ext_vector_type(4)));
typedef unsigned v2u __attribute__((ext_vector_type(2)));
typedef unsigned v16u __attribute__((ext_vector_type(16)));
typedef float f32x4 __attribute__((ext_vector_type(4)));
typedef float f32x2 __attribute__((ext_vector_type(2)));
typedef short s16x4 __attribute__((ext_vector_type(4)));
typedef __bf16 bf16x8 __attribute__((ext_vector_type(8)));
typedef __bf16 bf16x2 __attribute__((ext_vector_type(2)));
typedef __amdgpu_buffer_rsrc_t rsrc_t;
typedef __attribute__((address_space(3))) unsigned lds_u32;

constexpr int NB = 4, DM = 448, HKV = 8, SK = 8192, DH = 256, WIN = 2048, DFF = 1792, NQ = 4096;
constexpr int AUX_NT = 2, AUX_SC1 = 16;
constexpr int OOB = 0x40000000;
constexpr unsigned SPIN_CAP = 200000, TAG_MASK = 1023u;

#define WIN_BASE 5
#define WIN_LAG 10
#define PACE_A 80
#define PACE_B 300
#define PACE_T 160
#define KB0 8
#define QG_AT 22
#define VB2 4
#define O_DELAY 60
#define F_DELAY 66
#define CODE_BYTES 12544
constexpr int BSTR = 131072;
constexpr int JSKIP = 3;

constexpr int WS_E = 0;
constexpr int WS_T = 4096;
constexpr int WS_Q = WS_T + 8192;
constexpr int WS_H = WS_Q + 8 * 4 * 8192;
constexpr int WS_O = WS_H + 28 * 4 * 2048 + 4096;
constexpr int WS_F = WS_O + 16 * 16 * 4096;
constexpr int WS_END = WS_F + 2 * 28 * 16384;
constexpr int WS_DIAG = 8 << 20;

constexpr int OM_ROW = 68 * 4, FM_ROW = 544;
constexpr int L_QP = 0;
constexpr int L_Q = 8192;
constexpr int L_P = 9216;
constexpr int L_Z = 10240;
constexpr int L_CNT = 10304;
constexpr int L_TAG = 10320;
constexpr int L_HP = 10368;
constexpr int L_ST = 10432;
constexpr int L_OM = 10496;
constexpr int L_FM = L_OM + 32 * OM_ROW;
constexpr int L_END = L_FM + 32 * FM_ROW;

__device__ __forceinline__ rsrc_t mk(const void* p, int bytes) { return __builtin_amdgcn_make_buffer_rsrc((void*)p, 0, bytes, 0x00020000); }
template <int AUX> __device__ __forceinline__ v4u ld16(rsrc_t r, int vo, int so) { return __builtin_amdgcn_raw_buffer_load_b128(r, vo, so, AUX); }
__device__ __forceinline__ int oob_if(bool c) { return c ? OOB : 0; }
__device__ __forceinline__ float u2f(unsigned u) { return __builtin_bit_cast(float, u); }
__device__ __forceinline__ unsigned f2u(float f) { return __builtin_bit_cast(unsigned, f); }
__device__ __forceinline__ unsigned now() { return (unsigned)__builtin_amdgcn_s_memrealtime(); }
__device__ __forceinline__ void publish(rsrc_t r, int vo, float a, float b, float c, float d, unsigned t, unsigned keep = ~TAG_MASK) {
  __builtin_amdgcn_raw_buffer_store_b128(v4u{(f2u(a) & keep) | t, (f2u(b) & keep) | t, (f2u(c) & keep) | t, (f2u(d) & keep) | t}, r, vo, 0, AUX_SC1);
}
__device__ __forceinline__ unsigned untag(v4u& g, unsigned t) { g ^= t; return (g.x | g.y | g.z | g.w) & TAG_MASK; }
__device__ __forceinline__ bool wave_none(bool p) { return __builtin_amdgcn_ballot_w64(p) == 0; }
__device__ __forceinline__ float dot2(unsigned a, unsigned b, float c) {
  return __builtin_amdgcn_fdot2_f32_bf16(__builtin_bit_cast(bf16x2, a), __builtin_bit_cast(bf16x2, b), c, false);
}
__device__ __forceinline__ unsigned pkbf(float lo, float hi) {
  const bf16x2 v = {(__bf16)lo, (__bf16)hi};
  return __builtin_bit_cast(unsigned, v);
}
__device__ __forceinline__ unsigned perm_lo(unsigned a, unsigned b) { return __builtin_amdgcn_perm(b, a, 0x05040100u); }
__device__ __forceinline__ unsigned perm_hi(unsigned a, unsigned b) { return __builtin_amdgcn_perm(b, a, 0x07060302u); }
template <int CTRL> __device__ __forceinline__ unsigned dppu(unsigned x) { return (unsigned)__builtin_amdgcn_mov_dpp((int)x, CTRL, 0xf, 0xf, true); }
template <int CTRL> __device__ __forceinline__ float dpp(float x) { return u2f(dppu<CTRL>(f2u(x))); }
constexpr int XOR1 = 0xB1, XOR2 = 0x4E, XOR7 = 0x141, ROR8 = 0x128, ROR4 = 0x124;
__device__ __forceinline__ float fmx(float a, float c, float inf) { return __builtin_amdgcn_fmed3f(a, c, inf); }
__device__ __forceinline__ float fold16(float a, float b) {
  auto s = __builtin_amdgcn_permlane16_swap(f2u(a), f2u(b), false, false);
  return u2f(s[0]) + u2f(s[1]);
}
__device__ __forceinline__ float fold32(float a, float b) {
  auto s = __builtin_amdgcn_permlane32_swap(f2u(a), f2u(b), false, false);
  return u2f(s[0]) + u2f(s[1]);
}
__device__ __forceinline__ float x16_sum(float x) { return fold16(x, x); }
__device__ __forceinline__ float x16_max(float x, float inf) {
  auto s = __builtin_amdgcn_permlane16_swap(f2u(x), f2u(x), false, false);
  return fmx(u2f(s[0]), u2f(s[1]), inf);
}
__device__ __forceinline__ float row_sum(float x) { x += dpp<XOR1>(x); x += dpp<XOR2>(x); x += dpp<ROR4>(x); x += dpp<ROR8>(x); return x; }
__device__ __forceinline__ float wave_max(float x, float inf) {
  x = fmx(x, dpp<XOR1>(x), inf); x = fmx(x, dpp<XOR2>(x), inf); x = fmx(x, dpp<ROR4>(x), inf); x = fmx(x, dpp<ROR8>(x), inf);
  x = x16_max(x, inf);
  auto s = __builtin_amdgcn_permlane32_swap(f2u(x), f2u(x), false, false);
  return fmx(u2f(s[0]), u2f(s[1]), inf);
}
__device__ __forceinline__ f32x4 mfma32(v4u a, v4u b, f32x4 c) {
  return __builtin_amdgcn_mfma_f32_16x16x32_bf16(__builtin_bit_cast(bf16x8, a), __builtin_bit_cast(bf16x8, b), c, 0, 0, 0);
}
__device__ __forceinline__ f32x4 mfma16(v2u a, v2u b, f32x4 c) {
  return __builtin_amdgcn_mfma_f32_16x16x16bf16_1k(__builtin_bit_cast(s16x4, a), __builtin_bit_cast(s16x4, b), c, 0, 0, 0);
}

#define SB() __builtin_amdgcn_sched_barrier(0)
#define REREAD(off) asm volatile("" : "+v"(off) :: "memory")
#define VMWAIT(n) do { SB(); __builtin_amdgcn_s_waitcnt(((n) & 15) | (7 << 4) | (15 << 8) | ((((n) >> 4) & 3) << 14)); SB(); } while (0)
#define THROTTLE() do { VMWAIT(WIN_LAG - 1); \
    asm volatile("s_cmp_gt_i32 %0, 0\n\ts_cbranch_scc1 1f\n\ts_waitcnt vmcnt(%1)\n1:" :: "s"(lag), "i"(WIN_BASE - 1) : "scc"); SB(); } while (0)
#define PACE(k) do { lag = (int)(now() - tref) - (PACE_T * (k)) / 10; SB(); } while (0)
#define LDSBAR() do { SB(); __builtin_amdgcn_fence(__ATOMIC_RELEASE, "workgroup", "local"); __builtin_amdgcn_s_barrier(); \
                      __builtin_amdgcn_fence(__ATOMIC_ACQUIRE, "workgroup", "local"); SB(); } while (0)

__device__ __forceinline__ void lds_arrive(unsigned char* lds, int off, int lane) {
  __builtin_amdgcn_fence(__ATOMIC_RELEASE, "workgroup", "local");
  if (lane == 0) __hip_atomic_fetch_add((lds_u32*)(lds + off), 1u, __ATOMIC_RELAXED, __HIP_MEMORY_SCOPE_WORKGROUP);
}
__device__ __forceinline__ void lds_wait(unsigned char* lds, int off, unsigned want) {
  _Pragma("nounroll") for (unsigned spin = 0; spin < 16u * SPIN_CAP; ++spin) {
    if ((unsigned)__builtin_amdgcn_readfirstlane(__hip_atomic_load((lds_u32*)(lds + off), __ATOMIC_RELAXED, __HIP_MEMORY_SCOPE_WORKGROUP)) >= want) break;
    __builtin_amdgcn_s_sleep(1);
  }
  __builtin_amdgcn_fence(__ATOMIC_ACQUIRE, "workgroup", "local");
}

#define STAMP(k) do { } while (0)

struct TouchEntry { const void* p; unsigned stride, zero; };
struct Touch { TouchEntry e[32]; };

__global__ void __launch_bounds__(512, 2)
gated_mix_decode_kernel(const unsigned short*, const unsigned short*, const unsigned short*, const unsigned short*, const unsigned short*,
                        const unsigned short*, const unsigned short*, const unsigned short*, unsigned short*, unsigned char*, Touch) {
  __shared__ __attribute__((aligned(128))) unsigned char lds[L_END];
  STAMP(0);
  v16u ka; v4u kb, tp; v2u t0;
  asm volatile("s_load_dwordx4 %2, %4, %5 offset:0x50\n\ts_load_dwordx16 %0, %4, 0x0\n\ts_load_dwordx4 %1, %4, 0x40\n\ts_memrealtime %3"
               : "=&s"(ka), "=&s"(kb), "=&s"(tp), "=&s"(t0) : "s"(__builtin_amdgcn_kernarg_segment_ptr()), "s"((blockIdx.x >> 3) * 16u) : "memory");
  SB();
  unsigned cpf;
  {
    unsigned long long pc;
    asm volatile("s_getpc_b64 %0" : "=s"(pc));
    constexpr int PER = (CODE_BYTES / 128 + 3) / 4;
    const int line = ((blockIdx.x >> 3) & 3) * PER + threadIdx.x;
    cpf = __builtin_amdgcn_raw_buffer_load_b32(mk((const void*)pc, CODE_BYTES), (line * 128) | oob_if(threadIdx.x >= PER), 0, 0);
    SB();
  }
  const int id = blockIdx.x, kvh = id & 7, b = (id >> 3) & 3, j = id >> 5;
  const int lane = threadIdx.x & 63, wv = __builtin_amdgcn_readfirstlane(threadIdx.x >> 6), w = wv & 3;
  const int m16 = lane & 15, g4 = lane >> 4;
  const bool helper = wv < 4;
  const int blk0 = j * 16384 + w * 4096;
  int fo = ((helper ? (112 * b + 32 * w) * (NQ * 2) + (kvh * 8 + j) * 128 : blk0) + (lane >> 3) * (helper ? 4 * NQ * 2 : 512) + (lane & 7) * 16) |
           oob_if((lane >> 3) >= (wv == 3 ? 4 : 8));
  int xo = (((m16 >> 2) * DM + 112 * b + 32 * w + 8 * g4 + 4 * (m16 & 1)) * 2) | oob_if(!helper | ((m16 & 2) != 0) | (32 * w + 8 * g4 >= 112));
  int eo = WS_E | oob_if(!helper), to = oob_if(threadIdx.x != 0);
  unsigned head = ((id & 31) * SK + (SK - WIN)) * DH * 2;
  int fs1 = helper ? NQ * 2 : 128, fs2 = 2 * fs1, fs3 = 3 * fs1;
  asm volatile("" : "+v"(fo), "+v"(xo), "+v"(eo), "+v"(to), "+s"(head), "+s"(fs1), "+s"(fs2), "+s"(fs3));
  asm volatile("s_waitcnt lgkmcnt(0)" : "+s"(ka), "+s"(kb), "+s"(tp), "+s"(t0) :: "memory");
#define KARG(T, lo, hi) ((T)(((unsigned long long)(hi) << 32) | (lo)))
  const unsigned tw = __builtin_amdgcn_raw_buffer_load_b32(mk(KARG(const void*, tp.x, tp.y), OOB), to, tp.z * kvh, AUX_NT);
  SB();
  unsigned char* ws = KARG(unsigned char*, kb[2], kb[3]);
  const rsrc_t rW = mk(ws, WS_END), rX = mk(KARG(const void*, ka[0], ka[1]), NB * DM * 2);
  const v2u xq = __builtin_amdgcn_raw_buffer_load_b64(rX, xo, 0, 0);
  const unsigned ep = __builtin_amdgcn_raw_buffer_load_b32(rW, eo, 0, AUX_SC1);
  SB();
  const rsrc_t rF = mk(helper ? KARG(const char*, ka[6], ka[7]) : KARG(const char*, ka[2], ka[3]) + head, helper ? DM * NQ * 2 : WIN * DH * 2);
  const v4u first[4] = {ld16<AUX_NT>(rF, fo, 0), ld16<AUX_NT>(rF, fo, fs1), ld16<AUX_NT>(rF, fo, fs2), ld16<AUX_NT>(rF, fo, fs3)};
  SB();
  const int bp = b >> 1, r16 = (b & 1) * 8 + j, pu = kvh * 16 + r16, fs = kvh * 14 + r16;
  const f32x4 zero4 = {0.f, 0.f, 0.f, 0.f};
  float inf = __builtin_inff();
  asm volatile("" : "+s"(inf));

  if (__builtin_expect(helper, 1)) {
    const int tid = threadIdx.x;
    const int j1 = j == 7 ? JSKIP : j;
    v4u w1r[2];
    v2u xh;
    {
      const int rr = 2 * g4 + (m16 & 1);
      xh.x = __builtin_amdgcn_raw_buffer_load_b32(rX, (((m16 >> 2) * DM + 56 * kvh + 16 * w + 2 * rr) * 2) | oob_if(((m16 & 2) != 0) | (16 * w + 2 * rr >= 56)), 0, 0);
      xh.y = 0u;
      const rsrc_t r1 = mk(KARG(const void*, ka[10], ka[11]), DM * DFF * 2);
      const int row1 = 16 * w + 2 * (lane >> 3);
      const int o1 = ((56 * kvh + row1) * (DFF * 2) + (4 * j1 + b) * 128 + (lane & 7) * 16) | oob_if((j == JSKIP) | (row1 >= 56));
#pragma unroll
      for (int i = 0; i < 2; ++i) w1r[i] = ld16<AUX_NT>(r1, o1, i * (DFF * 2));
    }
    SB();
    STAMP(11);
    if (tid < 24) *(unsigned*)(lds + L_Z + tid * 4) = 0u;
#define TILE_PARTIAL(AF, BLO, BHI, LOFF) do { float pv[8]; \
      _Pragma("unroll") for (int dw = 0; dw < 4; ++dw) { \
        const f32x4 dl = mfma16(AF, BLO, zero4), dh = mfma16(AF, BHI, zero4); \
        const float sl = (lane & 8) ? dl[1] : dl[0], sh = (lane & 8) ? dh[1] : dh[0]; \
        pv[2 * dw] = sl + dpp<ROR8>(sl); pv[2 * dw + 1] = sh + dpp<ROR8>(sh); } \
      *(f32x4*)(lds + (LOFF) + ((w * 4 + g4) * 64 + 8 * (lane & 7) + ((lane & 8) >> 1)) * 4) = \
          (lane & 8) ? f32x4{pv[4], pv[5], pv[6], pv[7]} : f32x4{pv[0], pv[1], pv[2], pv[3]}; } while (0)
#define TILE_PUBLISH(LOFF, DST) do { f32x4 s = zero4; \
      _Pragma("unroll") for (int ww = 0; ww < 4; ++ww) s += *(const f32x4*)(lds + (LOFF) + ((ww * 4 + w) * 64 + 4 * m16) * 4); \
      publish(rW, (DST) | oob_if(lane >= 16), s[0], s[1], s[2], s[3], t); SB(); } while (0)
    TILE_PARTIAL(xq, (v2u{perm_lo(first[0][dw], first[1][dw]), perm_lo(first[2][dw], first[3][dw])}),
                 (v2u{perm_hi(first[0][dw], first[1][dw]), perm_hi(first[2][dw], first[3][dw])}), L_QP);
    const unsigned t = ((unsigned)__builtin_amdgcn_readfirstlane(ep) + 1u) & TAG_MASK;
    asm volatile("" :: "v"(tw), "v"(cpf));
    STAMP(1);
    LDSBAR();
    const unsigned tb0 = now();
    TILE_PUBLISH(L_QP, WS_Q + (kvh * 4 + w) * 8192 + j * 1024 + b * 256 + m16 * 16);
    STAMP(2);
    {
      const unsigned tv0 = (tb0 << 10) | t;
      __builtin_amdgcn_raw_buffer_store_b128(v4u{tv0, tv0, tv0, tv0}, rW,
                                             (WS_T + (tid >> 3) * 1024 + kvh * 128 + (tid & 7) * 16) | oob_if((id >= 8) | (tid >= 64)), 0, AUX_SC1);
      SB();
    }
    TILE_PARTIAL(xh, (v2u{perm_lo(w1r[0][dw], w1r[1][dw]), 0u}), (v2u{perm_hi(w1r[0][dw], w1r[1][dw]), 0u}), L_QP + 4096);
    lds_arrive(lds, L_CNT, lane);
    lds_wait(lds, L_CNT, 4);
    TILE_PUBLISH(L_QP + 4096, (WS_H + ((4 * j1 + b) * 4 + w) * 2048 + kvh * 256 + m16 * 16) | oob_if(j == JSKIP));
    STAMP(3);
    lds_wait(lds, L_CNT + 8, 1);
    STAMP(5);
    {
      int qgo = WS_Q + (kvh * 4 + b) * 8192 + (2 * w + (lane >> 5)) * 1024 + (g4 & 1) * 256 + m16 * 16;
      v4u gt = ld16<AUX_SC1>(rW, (WS_T + kvh * 1024 + lane * 128) | oob_if((w != 2) | (lane >= 8)), 0);
      v4u g0, g1;
      _Pragma("nounroll") for (unsigned spin = 0; spin < SPIN_CAP; ++spin) {
        REREAD(qgo);
        g0 = ld16<AUX_SC1>(rW, qgo, 0);
        g1 = ld16<AUX_SC1>(rW, qgo, 512);
        if (wave_none((untag(g0, t) | untag(g1, t)) != 0u)) break;
        __builtin_amdgcn_s_sleep(8);
      }
      float q[4];
#pragma unroll
      for (int k = 0; k < 4; ++k) q[k] = x16_sum(u2f(g0[k]) + u2f(g1[k]));
      if ((g4 & 1) == 0) *(v2u*)(lds + L_Q + ((2 * w + (lane >> 5)) * 64 + 4 * m16) * 2) = v2u{pkbf(q[0], q[1]), pkbf(q[2], q[3])};
      const bool seen = (untag(gt, t) == 0u) & (lane < 8);
      const float sum = row_sum(seen ? (float)((int)(gt.x - (tb0 << 10)) >> 10) : 0.f);
      if (tid == 128) *(unsigned*)(lds + L_TAG + 12) = tb0 + (int)(sum * 0.125f);
    }
    STAMP(4);
    LDSBAR();
    {
      const unsigned pw = __builtin_amdgcn_raw_buffer_load_b32(rW, (WS_O + ((bp * 8 + kvh) * 16 + r16) * 4096 + tid * 128) | oob_if(tid >= 32), 0, AUX_SC1);
      asm volatile("" :: "v"(pw));
    }
    if (w == 3) {
      const int go_ = (lane * 16) | oob_if(lane >= 56);
      const v4u xg0 = ld16<0>(rX, go_, 2 * bp * DM * 2), xg1 = ld16<0>(rX, go_, (2 * bp + 1) * DM * 2);
      const v4u gg = ld16<0>(mk(KARG(const void*, ka[14], ka[15]), DM * 2), go_, 0);
      const bool hon = r16 < 14;
      int hgo = (WS_H + ((fs >> 2) * 4 + 2 * bp + (lane >> 5)) * 2048 + ((lane >> 2) & 7) * 256 + (fs & 3) * 64 + (lane & 3) * 16) | oob_if(!hon);
      v4u gh;
      _Pragma("nounroll") for (unsigned spin = 0; spin < SPIN_CAP; ++spin) {
        REREAD(hgo);
        gh = ld16<AUX_SC1>(rW, hgo, 0);
        if (wave_none(hon & (untag(gh, t) != 0u))) break;
        __builtin_amdgcn_s_sleep(8);
      }
      float d0 = 0.f, d1 = 0.f;
#pragma unroll
      for (int k = 0; k < 4; ++k) { d0 = dot2(xg0[k], gg[k], d0); d1 = dot2(xg1[k], gg[k], d1); }
      const float gate = 1.f / (1.f + __expf(-x16_sum(row_sum(fold32(d0, d1)))));
      if ((lane & 31) == 0) *(float*)(lds + L_TAG + 4 + 4 * (lane >> 5)) = gate;
      if (lane == 1) *(unsigned*)(lds + L_TAG) = t;
      float h[4];
#pragma unroll
      for (int k = 0; k < 4; ++k) {
        float a = hon ? u2f(gh[k]) : 0.f;
        a += dpp<ROR4>(a);
        a += dpp<ROR8>(a);
        h[k] = fmaxf(x16_sum(a), 0.f) * (1.f - gate);
      }
      if ((lane & 28) == 0) *(v2u*)(lds + L_HP + (lane >> 5) * 32 + (lane & 3) * 8) = v2u{pkbf(h[0], h[1]), pkbf(h[2], h[3])};
    }
    lds_wait(lds, L_CNT + 4, 4);
    const f32x4 e0 = *(const f32x4*)(lds + L_ST + (tid >> 7) * 32), e1 = *(const f32x4*)(lds + L_ST + (tid >> 7) * 32 + 16);
    const float M = fmx(fmx(e0[0], e0[2], inf), fmx(e1[0], e1[2], inf), inf);
    f32x4 fc = {__builtin_amdgcn_exp2f(e0[0] - M), __builtin_amdgcn_exp2f(e0[2] - M), __builtin_amdgcn_exp2f(e1[0] - M), __builtin_amdgcn_exp2f(e1[2] - M)};
    const float L = (fc[0] * e0[1] + fc[1] * e0[3]) + (fc[2] * e1[1] + fc[3] * e1[3]);
    fc *= 1.f / L;
    const float lse = M + __builtin_amdgcn_logf(L);
    float lse_hi = u2f(f2u(lse) & ~TAG_MASK), lse_lo = lse - lse_hi;
    int oa = L_OM + 2 * (tid & 3) * OM_ROW + (tid >> 2) * 4, fa = L_FM + 2 * (tid & 3) * FM_ROW + (tid >> 2) * 8;
    int odst = WS_O + ((bp * 8 + kvh) * 32 + (tid >> 3)) * 2048 + (r16 ^ ((tid >> 3) & 7)) * 128 + (tid & 7) * 16;
    int fdst = (WS_F + (bp * 28 + (tid >> 3)) * 16384 + (pu ^ ((tid >> 3) & 7)) * 128 + (tid & 7) * 16) | oob_if(tid >= 224);
    unsigned tv = t, keep = ~TAG_MASK;
    asm volatile("" : "+v"(lse_hi), "+v"(lse_lo), "+v"(fc), "+v"(oa), "+v"(fa), "+v"(odst), "+v"(fdst), "+v"(tv), "+s"(keep));
    STAMP(6);
    LDSBAR();
    {
      float a = 0.f, c = 0.f;
#pragma unroll
      for (int ww = 0; ww < 4; ++ww) {
        a = fmaf(fc[ww], *(const float*)(lds + oa + ww * 8 * OM_ROW), a);
        c = fmaf(fc[ww], *(const float*)(lds + oa + ww * 8 * OM_ROW + OM_ROW), c);
      }
      publish(rW, odst, a, c, lse_hi, lse_lo, tv, keep);
    }
    STAMP(7);
    LDSBAR();
    {
      f32x2 p0[4], p1[4];
#pragma unroll
      for (int ww = 0; ww < 4; ++ww) {
        p0[ww] = *(const f32x2*)(lds + fa + ww * 8 * FM_ROW);
        p1[ww] = *(const f32x2*)(lds + fa + ww * 8 * FM_ROW + FM_ROW);
      }
      const f32x2 s0 = (p0[0] + p0[1]) + (p0[2] + p0[3]), s1 = (p1[0] + p1[1]) + (p1[2] + p1[3]);
      publish(rW, fdst, s0.x, s0.y, s1.x, s1.y, tv, keep);
    }
    STAMP(9);
  } else {
    const rsrc_t rV = mk(KARG(const char*, ka[4], ka[5]) + head, WIN * DH * 2);
    v4u kr[32], vr[32];
    int lag = 0;
    unsigned tref = t0.x + PACE_A;
#pragma unroll
    for (int i = 0; i < 4; ++i) kr[i] = first[i];
    STAMP(1);
#pragma unroll
    for (int i = 4; i < 32; ++i) {
      if (i >= 12) THROTTLE(); else if (i >= 8) VMWAIT(WIN_BASE - 1);
      kr[i] = ld16<AUX_NT>(rF, fo, (i >> 2) * BSTR + (i & 3) * 128);
      if (i >= 11 && (i & 3) == 3) PACE(i);
      if (i == KB0) { LDSBAR(); STAMP(2); }
      if (i == QG_AT && w == 0) lds_arrive(lds, L_CNT + 8, lane);
    }
    const int vo = blk0 + lane * 16;
#define VSTEP(i) do { THROTTLE(); vr[i] = ld16<AUX_NT>(rV, vo, ((i) >> 2) * BSTR + ((i) & 3) * 1024); SB(); \
                      if (((i) & 3) == 3) PACE(32 + (i)); } while (0)
    asm volatile("" :: "v"(cpf), "v"(tw), "v"(xq), "v"(ep));
    STAMP(3);
#pragma unroll
    for (int i = 0; i < VB2; ++i) VSTEP(i);
    LDSBAR();
    STAMP(4);
    tref = __builtin_amdgcn_readfirstlane(*(const unsigned*)(lds + L_TAG + 12)) + (PACE_B - (PACE_T * 31) / 10);
    PACE(31 + VB2);
    v4u pa[4];
    {
      v4u qf[2][4];
#pragma unroll
      for (int h = 0; h < 2; ++h)
#pragma unroll
        for (int t = 0; t < 4; ++t) qf[h][t] = *(const v4u*)(lds + L_Q + (h * 256 + 64 * t + 8 * (lane & 7)) * 2);
      VSTEP(VB2);
      float s[8][2];
#pragma unroll
      for (int i = 0; i < 8; i += 2) {
        float a0 = 0.f, a1 = 0.f, c0 = 0.f, c1 = 0.f;
#pragma unroll
        for (int t = 0; t < 4; ++t) {
#pragma unroll
          for (int dw = 0; dw < 4; ++dw) {
            a0 = dot2(kr[4 * i + t][dw], qf[0][t][dw], a0); c0 = dot2(kr[4 * i + 4 + t][dw], qf[0][t][dw], c0);
            a1 = dot2(kr[4 * i + t][dw], qf[1][t][dw], a1); c1 = dot2(kr[4 * i + 4 + t][dw], qf[1][t][dw], c1);
          }
          if (t == 1) VSTEP(VB2 + 1 + i);
        }
        s[i][0] = a0; s[i][1] = a1; s[i + 1][0] = c0; s[i + 1][1] = c1;
        VSTEP(VB2 + 2 + i);
      }
#pragma unroll
      for (int i = 0; i < 4; ++i)
#pragma unroll
        for (int h = 0; h < 2; ++h) {
          const float lo = s[i][h] + dpp<XOR7>(s[i][h]), hi = s[i + 4][h] + dpp<XOR7>(s[i + 4][h]);
          s[i][h] = (lane & 4) ? hi : lo;
        }
#pragma unroll
      for (int i = 0; i < 2; ++i)
#pragma unroll
        for (int h = 0; h < 2; ++h) {
          const float lo = s[i][h] + dpp<XOR2>(s[i][h]), hi = s[i + 2][h] + dpp<XOR2>(s[i + 2][h]);
          s[i][h] = (lane & 2) ? hi : lo;
        }
#pragma unroll
      for (int h = 0; h < 2; ++h) {
        const float lo = s[0][h] + dpp<XOR1>(s[0][h]), hi = s[1][h] + dpp<XOR1>(s[1][h]);
        s[0][h] = (lane & 1) ? hi : lo;
      }
      VSTEP(VB2 + 9);
      constexpr float C2 = 0.0625f * 1.4426950408889634f;
      const float z0 = s[0][0] * C2, z1 = s[0][1] * C2;
      const float m0 = wave_max(z0, inf), m1 = wave_max(z1, inf);
      const float p0 = __builtin_amdgcn_exp2f(z0 - m0), p1 = __builtin_amdgcn_exp2f(z1 - m1);
      const unsigned pp = pkbf(p0, p1);
      unsigned short* pl = (unsigned short*)(lds + L_P + w * 256 + ((lane >> 3) & 1) * 64 + (lane & 7) * 8 + (lane >> 4) * 2);
      pl[0] = (unsigned short)pp;
      pl[64] = (unsigned short)(pp >> 16);
      VSTEP(VB2 + 10);
      const float l01 = x16_sum(row_sum(fold32(p0, p1)));
      if ((lane & 31) == 0) *(f32x2*)(lds + L_ST + ((lane >> 5) * 4 + w) * 8) = f32x2{lane ? m1 : m0, l01};
      lds_arrive(lds, L_CNT + 4, lane);
      const bool valid = (m16 & 3) == 0 && ((m16 >> 2) & 1) == (g4 & 1);
      const int a = valid ? L_P + w * 256 + (m16 >> 3) * 128 + (g4 >> 1) * 64 : L_Z;
#pragma unroll
      for (int G = 0; G < 4; ++G) pa[G] = *(const v4u*)(lds + a + G * 16);
    }
    STAMP(5);
    f32x4 acc[8];
#pragma unroll
    for (int e = 0; e < 8; ++e) acc[e] = zero4;
    v4u tr[12], bf[8];
    const rsrc_t rO = mk(KARG(const void*, ka[8], ka[9]), NQ * DM * 2), r2 = mk(KARG(const void*, ka[12], ka[13]), DFF * DM * 2);
    const int o2 = ((16 * fs + 4 * w) * (DM * 2) + lane * 16) | oob_if((r16 >= 14) | (lane >= 56));
    const int oo = ((kvh * 512 + 32 * r16 + 8 * w) * (DM * 2) + lane * 16) | oob_if(lane >= 56);
#define TLOAD(i) tr[i] = (i) < 4 ? ld16<AUX_NT>(r2, o2, (i) * (DM * 2)) : ld16<AUX_NT>(rO, oo, ((i) - 4) * (DM * 2))
#define PAIRUP(src, pr) do { _Pragma("unroll") for (int dw = 0; dw < 4; ++dw) { \
      unsigned lo_ = perm_lo((src)[2 * (pr)][dw], (src)[2 * (pr) + 1][dw]), hi_ = perm_hi((src)[2 * (pr)][dw], (src)[2 * (pr) + 1][dw]); \
      asm volatile("" : "+v"(lo_), "+v"(hi_)); bf[2 * dw][pr] = lo_; bf[2 * dw + 1][pr] = hi_; } SB(); } while (0)
#define PVMMA(G) do { _Pragma("unroll") for (int e = 0; e < 8; ++e) acc[e] = mfma32(pa[G], bf[e], acc[e]); } while (0)
#define PVGROUP(G) do { PAIRUP(vr + 8 * (G), 0); PAIRUP(vr + 8 * (G), 1); PAIRUP(vr + 8 * (G), 2); PAIRUP(vr + 8 * (G), 3); PVMMA(G); SB(); } while (0)
#pragma unroll
    for (int i = VB2 + 11; i < 32; ++i) {
      VSTEP(i);
      if (i == VB2 + 11) PVGROUP(0);
      if (i == 24) PVGROUP(1);
      if (i == 31) PVGROUP(2);
    }
#pragma unroll
    for (int k = 0; k < 8; ++k) {
      VMWAIT(7);
      TLOAD(k);
      SB();
      if (k == 1) PAIRUP(vr + 24, 0);
      if (k == 3) PAIRUP(vr + 24, 1);
      if (k == 5) PAIRUP(vr + 24, 2);
    }
    int oma = L_OM + w * 8 * OM_ROW + lane * 4;
    asm volatile("" : "+v"(oma));
    SB();
#pragma unroll
    for (int dw = 0; dw < 4; ++dw) { bf[2 * dw][3] = perm_lo(vr[30][dw], vr[31][dw]); bf[2 * dw + 1][3] = perm_hi(vr[30][dw], vr[31][dw]); }
    PVMMA(3);
    const unsigned tvp = now();
#pragma unroll
    for (int e = 0; e < 8; ++e) *(float*)(lds + oma + e * OM_ROW) = acc[e][0];
    LDSBAR();
    const unsigned tb3 = now();
    STAMP(7);
#pragma unroll
    for (int i = 8; i < 12; ++i) TLOAD(i);
    SB();
    const bool diag = (m16 >> 2) == g4;
    const v4u tg = *(const v4u*)(lds + L_TAG);
    const v2u ah = *(const v2u*)(lds + ((diag && (m16 & 3) < 2) ? L_HP + (m16 & 3) * 32 + w * 8 : L_Z));
    const unsigned t = tg.x;
#pragma unroll
    for (int dw = 0; dw < 4; ++dw) {
      acc[2 * dw] = mfma16(ah, v2u{perm_lo(tr[0][dw], tr[1][dw]), perm_lo(tr[2][dw], tr[3][dw])}, zero4);
      acc[2 * dw + 1] = mfma16(ah, v2u{perm_hi(tr[0][dw], tr[1][dw]), perm_hi(tr[2][dw], tr[3][dw])}, zero4);
    }
    SB();
    int ogo = WS_O + (((bp * 8 + kvh) * 16 + r16) * 2 + (w >> 1)) * 2048 + (lane >> 2) * 128 + (w & 1) * 64 + (lane & 3) * 16;
    float gate = u2f(lane < 32 ? tg.y : tg.z);
    unsigned m0 = (diag && (lane & 1) == 0) ? 0xffffffffu : 0u, m1 = (diag && (lane & 1) != 0) ? 0xffffffffu : 0u;
    int fma_ = L_FM + w * 8 * FM_ROW + lane * 8;
    asm volatile("" : "+v"(gate), "+v"(m0), "+v"(m1), "+v"(fma_), "+v"(ogo));
    SB();
    PAIRUP(tr + 4, 0);
    PAIRUP(tr + 4, 1);
    while ((int)(now() - tb3) < O_DELAY) {}
    SB();
    v4u go = ld16<AUX_SC1>(rW, ogo, 0);
    SB();
    PAIRUP(tr + 4, 2);
    PAIRUP(tr + 4, 3);
    STAMP(11);
    if (!wave_none(untag(go, t) != 0u)) {
      _Pragma("nounroll") for (unsigned spin = 0; spin < SPIN_CAP; ++spin) {
        REREAD(ogo);
        go = ld16<AUX_SC1>(rW, ogo, 0);
        if (wave_none(untag(go, t) != 0u)) break;
      }
    }
    STAMP(8);
    {
      const float lm = u2f(go.z) + u2f(go.w);
      float M = fmx(lm, dpp<ROR4>(lm), inf);
      M = fmx(M, dpp<ROR8>(M), inf);
      M = x16_max(M, inf);
      float d = __builtin_amdgcn_exp2f(lm - M);
      float a = d * u2f(go.x), c = d * u2f(go.y);
      a += dpp<ROR4>(a); c += dpp<ROR4>(c); d += dpp<ROR4>(d);
      a += dpp<ROR8>(a); c += dpp<ROR8>(c); d += dpp<ROR8>(d);
      a = x16_sum(a); c = x16_sum(c);
      d = gate * __builtin_amdgcn_rcpf(x16_sum(d));
      const unsigned op = pkbf(a * d, c * d);
      const auto sw = __builtin_amdgcn_permlane32_swap(op, op, false, false);
      const v4u b0 = {dppu<0x00>(sw[0]), dppu<0x55>(sw[0]), dppu<0xAA>(sw[0]), dppu<0xFF>(sw[0])};
      const v4u b1 = {dppu<0x00>(sw[1]), dppu<0x55>(sw[1]), dppu<0xAA>(sw[1]), dppu<0xFF>(sw[1])};
      const v4u ao = (b0 & m0) | (b1 & m1);
#pragma unroll
      for (int e = 0; e < 8; ++e) acc[e] = mfma32(ao, bf[e], acc[e]);
#pragma unroll
      for (int e = 0; e < 8; ++e) *(f32x2*)(lds + fma_ + e * FM_ROW) = f32x2{acc[e][0], acc[e][1]};
    }
    if (w == 1) __builtin_amdgcn_raw_buffer_store_b64(v2u{t0.x, tvp}, mk(ws + WS_DIAG, 65536), (id * 128) | oob_if(lane != 0), 0, AUX_SC1);
    LDSBAR();
    if (w == 0 && pu < 112) {
      const unsigned tb4 = now();
      int fgo = WS_F + (bp * 28 + (pu >> 2)) * 16384 + (lane >> 1) * 128 + ((pu & 3) * 2 + (lane & 1)) * 16;
      int oo2 = (((2 * bp + (lane >> 5)) * DM + 4 * pu + 2 * (lane & 1) + (g4 & 1)) * 2) | oob_if((lane & 14) != 0);
      asm volatile("" : "+v"(oo2));
      v4u gf[4];
      while ((int)(now() - tb4) < F_DELAY) {}
      SB();
      _Pragma("nounroll") for (unsigned spin = 0; spin < SPIN_CAP; ++spin) {
        REREAD(fgo);
#pragma unroll
        for (int k = 0; k < 4; ++k) gf[k] = ld16<AUX_SC1>(rW, fgo, k * 4096);
        const unsigned miss = (untag(gf[0], t) | untag(gf[1], t)) | (untag(gf[2], t) | untag(gf[3], t));
        if (wave_none(miss != 0u)) break;
      }
      float v[4];
#pragma unroll
      for (int k = 0; k < 4; ++k) v[k] = (u2f(gf[0][k]) + u2f(gf[1][k])) + (u2f(gf[2][k]) + u2f(gf[3][k]));
      float r = fold16(fold32(v[0], v[1]), fold32(v[2], v[3]));
      r += dpp<ROR8>(r); r += dpp<ROR4>(r); r += dpp<XOR2>(r);
      __builtin_amdgcn_raw_buffer_store_b16((unsigned short)pkbf(r, r), mk(KARG(const void*, kb[0], kb[1]), NB * DM * 2), oo2, 0, 0);
      if (id == 0 && lane == 0) __builtin_amdgcn_raw_buffer_store_b32(t, rW, WS_E, 0, AUX_SC1);
      if (id == 0) {
        const unsigned tend = now();
        while ((int)(now() - tend) < 300) {}
        float e0 = 1e9f, e1 = -1e9f, v1 = -1e9f, v0 = 1e9f;
        for (int k = 0; k < 4; ++k) {
          const v2u z = __builtin_amdgcn_raw_buffer_load_b64(mk(ws + WS_DIAG, 65536), (k * 64 + lane) * 128, 0, AUX_SC1);
          const float a = (float)(int)(z.x - t0.x), c = (float)(int)(z.y - t0.x);
          e0 = fminf(e0, a); e1 = fmaxf(e1, a); v1 = fmaxf(v1, c); v0 = fminf(v0, c);
        }
        e1 = wave_max(e1, inf); v1 = wave_max(v1, inf); e0 = -wave_max(-e0, inf); v0 = -wave_max(-v0, inf);
        float x = PROBE == 1 ? e1 - e0 : PROBE == 2 ? v1 - e1 - 900.f : PROBE == 3 ? (float)(int)(tend - t0.x) - v1 : v1 - v0;
        x = fminf(fmaxf(x, 0.f), 800.f);
        const int xi = __builtin_amdgcn_readfirstlane((int)x) * 10;
        const unsigned tb = now();
        while ((int)(now() - tb) < xi) {}
      }
    }
  }
}

}

extern "C" void kernel_launch(void* const* d_in, const int* in_sizes, int n_in, void* d_out, int out_size, void* d_ws, size_t ws_size,
                              hipStream_t stream) {
  (void)in_sizes; (void)n_in; (void)out_size; (void)ws_size;
  const char* const kc = (const char*)d_in[1]; const char* const vc = (const char*)d_in[2];
  const char* const wq = (const char*)d_in[3]; const char* const wo = (const char*)d_in[4];
  const char* const w1 = (const char*)d_in[6]; const char* const w2 = (const char*)d_in[7];
  const char* const ws = (const char*)d_ws;
  constexpr unsigned HEAD = SK * DH * 2, W0 = (SK - WIN) * DH * 2, WB = WIN * DH * 2, WQB = DM * NQ * 2, W1B = DM * DFF * 2;
  Touch tt;
  for (int n = 0; n < 16; ++n)
    tt.e[n] = TouchEntry{((n >= 4 && n < 12) ? vc : kc) + (size_t)(n & 3) * HKV * HEAD + W0 + (((n >> 2) & 1) ? 0 : WB - 128), HEAD, 0u};
  const char* const rest[16] = {wq, wq + WQB / 2, wq + WQB - 1024, w1, w1 + W1B - 1024, w2, w2 + W1B - 1024, wo, wo + WQB / 8 - 128,
                                (const char*)d_in[0], (const char*)d_in[8], (const char*)d_out, ws + 1024, ws + WS_O - 1024, ws + WS_END,
                                vc + W0 + WB / 2};
  const unsigned step[16] = {128, 128, 128, 128, 128, 128, 128, WQB / 8, WQB / 8, 128, 112, 128, 128, 128, 128, HEAD};
  for (int n = 0; n < 16; ++n) tt.e[16 + n] = TouchEntry{rest[n], step[n], 0u};
  gated_mix_decode_kernel<<<256, 512, 0, stream>>>(
      (const unsigned short*)d_in[0], (const unsigned short*)kc, (const unsigned short*)vc, (const unsigned short*)wq, (const unsigned short*)wo,
      (const unsigned short*)w1, (const unsigned short*)w2, (const unsigned short*)d_in[8], (unsigned short*)d_out, (unsigned char*)d_ws, tt);
}
```

```cpp
#define PROBE 8
#include <hip/hip_runtime.h>
#include <hip/hip_bf16.h>
#include <cstdint>

namespace {

typedef unsigned v4u __attribute__((ext_vector_type(4)));
typedef unsigned v2u __attribute__((ext_vector_type(2)));
typedef unsigned v16u __attribute__((ext_vector_type(16)));
typedef float f32x4 __attribute__((ext_vector_type(4)));
typedef float f32x2 __attribute__((ext_vector_type(2)));
typedef short s16x4 __attribute__((ext_vector_type(4)));
typedef __bf16 bf16x8 __attribute__((ext_vector_type(8)));
typedef __bf16 bf16x2 __attribute__((ext_vector_type(2)));
typedef __amdgpu_buffer_rsrc_t rsrc_t;
typedef __attribute__((address_space(3))) unsigned lds_u32;

constexpr int NB = 4, DM = 448, HKV = 8, SK = 8192, DH = 256, WIN = 2048, DFF = 1792, NQ = 4096;
constexpr int AUX_NT = 2, AUX_SC1 = 16;
constexpr int OOB = 0x40000000;
constexpr unsigned SPIN_CAP = 200000, TAG_MASK = 1023u;

#define WIN_BASE 5
#define WIN_LAG 10
#define PACE_A 80
#define PACE_B 315
#define PACE_T 160
#define KB0 8
#define QG_AT 22
#define VB2 4
#define O_DELAY 60
#define F_DELAY 66
#define CODE_BYTES 12544
constexpr int BSTR = 131072;
constexpr int JSKIP = 3;

constexpr int WS_E = 0;
constexpr int WS_T = 4096;
constexpr int WS_Q = WS_T + 8192;
constexpr int WS_H = WS_Q + 8 * 4 * 8192;
constexpr int WS_O = WS_H + 28 * 4 * 2048 + 4096;
constexpr int WS_F = WS_O + 16 * 16 * 4096;
constexpr int WS_END = WS_F + 2 * 28 * 16384;
constexpr int WS_DIAG = 8 << 20;

constexpr int OM_ROW = 68 * 4, FM_ROW = 544;
constexpr int L_QP = 0;
constexpr int L_Q = 8192;
constexpr int L_P = 9216;
constexpr int L_Z = 10240;
constexpr int L_CNT = 10304;
constexpr int L_TAG = 10320;
constexpr int L_HP = 10368;
constexpr int L_ST = 10432;
constexpr int L_OM = 10496;
constexpr int L_FM = L_OM + 32 * OM_ROW;
constexpr int L_END = L_FM + 32 * FM_ROW;

__device__ __forceinline__ rsrc_t mk(const void* p, int bytes) { return __builtin_amdgcn_make_buffer_rsrc((void*)p, 0, bytes, 0x00020000); }
template <int AUX> __device__ __forceinline__ v4u ld16(rsrc_t r, int vo, int so) { return __builtin_amdgcn_raw_buffer_load_b128(r, vo, so, AUX); }
__device__ __forceinline__ int oob_if(bool c) { return c ? OOB : 0; }
__device__ __forceinline__ float u2f(unsigned u) { return __builtin_bit_cast(float, u); }
__device__ __forceinline__ unsigned f2u(float f) { return __builtin_bit_cast(unsigned, f); }
__device__ __forceinline__ unsigned now() { return (unsigned)__builtin_amdgcn_s_memrealtime(); }
__device__ __forceinline__ void publish(rsrc_t r, int vo, float a, float b, float c, float d, unsigned t, unsigned keep = ~TAG_MASK) {
  __builtin_amdgcn_raw_buffer_store_b128(v4u{(f2u(a) & keep) | t, (f2u(b) & keep) | t, (f2u(c) & keep) | t, (f2u(d) & keep) | t}, r, vo, 0, AUX_SC1);
}
__device__ __forceinline__ unsigned untag(v4u& g, unsigned t) { g ^= t; return (g.x | g.y | g.z | g.w) & TAG_MASK; }
__device__ __forceinline__ bool wave_none(bool p) { return __builtin_amdgcn_ballot_w64(p) == 0; }
__device__ __forceinline__ float dot2(unsigned a, unsigned b, float c) {
  return __builtin_amdgcn_fdot2_f32_bf16(__builtin_bit_cast(bf16x2, a), __builtin_bit_cast(bf16x2, b), c, false);
}
__device__ __forceinline__ unsigned pkbf(float lo, float hi) {
  const bf16x2 v = {(__bf16)lo, (__bf16)hi};
  return __builtin_bit_cast(unsigned, v);
}
__device__ __forceinline__ unsigned perm_lo(unsigned a, unsigned b) { return __builtin_amdgcn_perm(b, a, 0x05040100u); }
__device__ __forceinline__ unsigned perm_hi(unsigned a, unsigned b) { return __builtin_amdgcn_perm(b, a, 0x07060302u); }
template <int CTRL> __device__ __forceinline__ unsigned dppu(unsigned x) { return (unsigned)__builtin_amdgcn_mov_dpp((int)x, CTRL, 0xf, 0xf, true); }
template <int CTRL> __device__ __forceinline__ float dpp(float x) { return u2f(dppu<CTRL>(f2u(x))); }
constexpr int XOR1 = 0xB1, XOR2 = 0x4E, XOR7 = 0x141, ROR8 = 0x128, ROR4 = 0x124;
__device__ __forceinline__ float fmx(float a, float c, float inf) { return __builtin_amdgcn_fmed3f(a, c, inf); }
__device__ __forceinline__ float fold16(float a, float b) {
  auto s = __builtin_amdgcn_permlane16_swap(f2u(a), f2u(b), false, false);
  return u2f(s[0]) + u2f(s[1]);
}
__device__ __forceinline__ float fold32(float a, float b) {
  auto s = __builtin_amdgcn_permlane32_swap(f2u(a), f2u(b), false, false);
  return u2f(s[0]) + u2f(s[1]);
}
__device__ __forceinline__ float x16_sum(float x) { return fold16(x, x); }
__device__ __forceinline__ float x16_max(float x, float inf) {
  auto s = __builtin_amdgcn_permlane16_swap(f2u(x), f2u(x), false, false);
  return fmx(u2f(s[0]), u2f(s[1]), inf);
}
__device__ __forceinline__ float row_sum(float x) { x += dpp<XOR1>(x); x += dpp<XOR2>(x); x += dpp<ROR4>(x); x += dpp<ROR8>(x); return x; }
__device__ __forceinline__ float wave_max(float x, float inf) {
  x = fmx(x, dpp<XOR1>(x), inf); x = fmx(x, dpp<XOR2>(x), inf); x = fmx(x, dpp<ROR4>(x), inf); x = fmx(x, dpp<ROR8>(x), inf);
  x = x16_max(x, inf);
  auto s = __builtin_amdgcn_permlane32_swap(f2u(x), f2u(x), false, false);
  return fmx(u2f(s[0]), u2f(s[1]), inf);
}
__device__ __forceinline__ f32x4 mfma32(v4u a, v4u b, f32x4 c) {
  return __builtin_amdgcn_mfma_f32_16x16x32_bf16(__builtin_bit_cast(bf16x8, a), __builtin_bit_cast(bf16x8, b), c, 0, 0, 0);
}
__device__ __forceinline__ f32x4 mfma16(v2u a, v2u b, f32x4 c) {
  return __builtin_amdgcn_mfma_f32_16x16x16bf16_1k(__builtin_bit_cast(s16x4, a), __builtin_bit_cast(s16x4, b), c, 0, 0, 0);
}

#define SB() __builtin_amdgcn_sched_barrier(0)
#define REREAD(off) asm volatile("" : "+v"(off) :: "memory")
#define VMWAIT(n) do { SB(); __builtin_amdgcn_s_waitcnt(((n) & 15) | (7 << 4) | (15 << 8) | ((((n) >> 4) & 3) << 14)); SB(); } while (0)
#define THROTTLE() do { VMWAIT(WIN_LAG - 1); \
    asm volatile("s_cmp_gt_i32 %0, 0\n\ts_cbranch_scc1 1f\n\ts_waitcnt vmcnt(%1)\n1:" :: "s"(lag), "i"(WIN_BASE - 1) : "scc"); SB(); } while (0)
#define PACE(k) do { lag = (int)(now() - tref) - (PACE_T * (k)) / 10; SB(); } while (0)
#define LDSBAR() do { SB(); __builtin_amdgcn_fence(__ATOMIC_RELEASE, "workgroup", "local"); __builtin_amdgcn_s_barrier(); \
                      __builtin_amdgcn_fence(__ATOMIC_ACQUIRE, "workgroup", "local"); SB(); } while (0)

__device__ __forceinline__ void lds_arrive(unsigned char* lds, int off, int lane) {
  __builtin_amdgcn_fence(__ATOMIC_RELEASE, "workgroup", "local");
  if (lane == 0) __hip_atomic_fetch_add((lds_u32*)(lds + off), 1u, __ATOMIC_RELAXED, __HIP_MEMORY_SCOPE_WORKGROUP);
}
__device__ __forceinline__ void lds_wait(unsigned char* lds, int off, unsigned want) {
  _Pragma("nounroll") for (unsigned spin = 0; spin < 16u * SPIN_CAP; ++spin) {
    if ((unsigned)__builtin_amdgcn_readfirstlane(__hip_atomic_load((lds_u32*)(lds + off), __ATOMIC_RELAXED, __HIP_MEMORY_SCOPE_WORKGROUP)) >= want) break;
    __builtin_amdgcn_s_sleep(1);
  }
  __builtin_amdgcn_fence(__ATOMIC_ACQUIRE, "workgroup", "local");
}

#define STAMP(k) do { } while (0)

struct TouchEntry { const void* p; unsigned stride, zero; };
struct Touch { TouchEntry e[32]; };

__global__ void __launch_bounds__(512, 2)
gated_mix_decode_kernel(const unsigned short*, const unsigned short*, const unsigned short*, const unsigned short*, const unsigned short*,
                        const unsigned short*, const unsigned short*, const unsigned short*, unsigned short*, unsigned char*, Touch) {
  __shared__ __attribute__((aligned(128))) unsigned char lds[L_END];
  STAMP(0);
  v16u ka; v4u kb, tp; v2u t0;
  asm volatile("s_load_dwordx4 %2, %4, %5 offset:0x50\n\ts_load_dwordx16 %0, %4, 0x0\n\ts_load_dwordx4 %1, %4, 0x40\n\ts_memrealtime %3"
               : "=&s"(ka), "=&s"(kb), "=&s"(tp), "=&s"(t0) : "s"(__builtin_amdgcn_kernarg_segment_ptr()), "s"((blockIdx.x >> 3) * 16u) : "memory");
  SB();
  unsigned cpf;
  {
    unsigned long long pc;
    asm volatile("s_getpc_b64 %0" : "=s"(pc));
    constexpr int PER = (CODE_BYTES / 128 + 3) / 4;
    const int line = ((blockIdx.x >> 3) & 3) * PER + threadIdx.x;
    cpf = __builtin_amdgcn_raw_buffer_load_b32(mk((const void*)pc, CODE_BYTES), (line * 128) | oob_if(threadIdx.x >= PER), 0, 0);
    SB();
  }
  const int id = blockIdx.x, kvh = id & 7, b = (id >> 3) & 3, j = id >> 5;
  const int lane = threadIdx.x & 63, wv = __builtin_amdgcn_readfirstlane(threadIdx.x >> 6), w = wv & 3;
  const int m16 = lane & 15, g4 = lane >> 4;
  const bool helper = wv < 4;
  const int blk0 = j * 16384 + w * 4096;
  int fo = ((helper ? (112 * b + 32 * w) * (NQ * 2) + (kvh * 8 + j) * 128 : blk0) + (lane >> 3) * (helper ? 4 * NQ * 2 : 512) + (lane & 7) * 16) |
           oob_if((lane >> 3) >= (wv == 3 ? 4 : 8));
  int xo = (((m16 >> 2) * DM + 112 * b + 32 * w + 8 * g4 + 4 * (m16 & 1)) * 2) | oob_if(!helper | ((m16 & 2) != 0) | (32 * w + 8 * g4 >= 112));
  int eo = WS_E | oob_if(!helper), to = oob_if(threadIdx.x != 0);
  unsigned head = ((id & 31) * SK + (SK - WIN)) * DH * 2;
  int fs1 = helper ? NQ * 2 : 128, fs2 = 2 * fs1, fs3 = 3 * fs1;
  asm volatile("" : "+v"(fo), "+v"(xo), "+v"(eo), "+v"(to), "+s"(head), "+s"(fs1), "+s"(fs2), "+s"(fs3));
  asm volatile("s_waitcnt lgkmcnt(0)" : "+s"(ka), "+s"(kb), "+s"(tp), "+s"(t0) :: "memory");
#define KARG(T, lo, hi) ((T)(((unsigned long long)(hi) << 32) | (lo)))
  const unsigned tw = __builtin_amdgcn_raw_buffer_load_b32(mk(KARG(const void*, tp.x, tp.y), OOB), to, tp.z * kvh, AUX_NT);
  SB();
  unsigned char* ws = KARG(unsigned char*, kb[2], kb[3]);
  const rsrc_t rW = mk(ws, WS_END), rX = mk(KARG(const void*, ka[0], ka[1]), NB * DM * 2);
  const v2u xq = __builtin_amdgcn_raw_buffer_load_b64(rX, xo, 0, 0);
  const unsigned ep = __builtin_amdgcn_raw_buffer_load_b32(rW, eo, 0, AUX_SC1);
  SB();
  const rsrc_t rF = mk(helper ? KARG(const char*, ka[6], ka[7]) : KARG(const char*, ka[2], ka[3]) + head, helper ? DM * NQ * 2 : WIN * DH * 2);
  const v4u first[4] = {ld16<AUX_NT>(rF, fo, 0), ld16<AUX_NT>(rF, fo, fs1), ld16<AUX_NT>(rF, fo, fs2), ld16<AUX_NT>(rF, fo, fs3)};
  SB();
  const int bp = b >> 1, r16 = (b & 1) * 8 + j, pu = kvh * 16 + r16, fs = kvh * 14 + r16;
  const f32x4 zero4 = {0.f, 0.f, 0.f, 0.f};
  float inf = __builtin_inff();
  asm volatile("" : "+s"(inf));

  if (__builtin_expect(helper, 1)) {
    const int tid = threadIdx.x;
    const int j1 = j == 7 ? JSKIP : j;
    v4u w1r[2];
    v2u xh;
    {
      const int rr = 2 * g4 + (m16 & 1);
      xh.x = __builtin_amdgcn_raw_buffer_load_b32(rX, (((m16 >> 2) * DM + 56 * kvh + 16 * w + 2 * rr) * 2) | oob_if(((m16 & 2) != 0) | (16 * w + 2 * rr >= 56)), 0, 0);
      xh.y = 0u;
      const rsrc_t r1 = mk(KARG(const void*, ka[10], ka[11]), DM * DFF * 2);
      const int row1 = 16 * w + 2 * (lane >> 3);
      const int o1 = ((56 * kvh + row1) * (DFF * 2) + (4 * j1 + b) * 128 + (lane & 7) * 16) | oob_if((j == JSKIP) | (row1 >= 56));
#pragma unroll
      for (int i = 0; i < 2; ++i) w1r[i] = ld16<AUX_NT>(r1, o1, i * (DFF * 2));
    }
    SB();
    STAMP(11);
    if (tid < 24) *(unsigned*)(lds + L_Z + tid * 4) = 0u;
#define TILE_PARTIAL(AF, BLO, BHI, LOFF) do { float pv[8]; \
      _Pragma("unroll") for (int dw = 0; dw < 4; ++dw) { \
        const f32x4 dl = mfma16(AF, BLO, zero4), dh = mfma16(AF, BHI, zero4); \
        const float sl = (lane & 8) ? dl[1] : dl[0], sh = (lane & 8) ? dh[1] : dh[0]; \
        pv[2 * dw] = sl + dpp<ROR8>(sl); pv[2 * dw + 1] = sh + dpp<ROR8>(sh); } \
      *(f32x4*)(lds + (LOFF) + ((w * 4 + g4) * 64 + 8 * (lane & 7) + ((lane & 8) >> 1)) * 4) = \
          (lane & 8) ? f32x4{pv[4], pv[5], pv[6], pv[7]} : f32x4{pv[0], pv[1], pv[2], pv[3]}; } while (0)
#define TILE_PUBLISH(LOFF, DST) do { f32x4 s = zero4; \
      _Pragma("unroll") for (int ww = 0; ww < 4; ++ww) s += *(const f32x4*)(lds + (LOFF) + ((ww * 4 + w) * 64 + 4 * m16) * 4); \
      publish(rW, (DST) | oob_if(lane >= 16), s[0], s[1], s[2], s[3], t); SB(); } while (0)
    TILE_PARTIAL(xq, (v2u{perm_lo(first[0][dw], first[1][dw]), perm_lo(first[2][dw], first[3][dw])}),
                 (v2u{perm_hi(first[0][dw], first[1][dw]), perm_hi(first[2][dw], first[3][dw])}), L_QP);
    const unsigned t = ((unsigned)__builtin_amdgcn_readfirstlane(ep) + 1u) & TAG_MASK;
    asm volatile("" :: "v"(tw), "v"(cpf));
    STAMP(1);
    LDSBAR();
    const unsigned tb0 = now();
    TILE_PUBLISH(L_QP, WS_Q + (kvh * 4 + w) * 8192 + j * 1024 + b * 256 + m16 * 16);
    STAMP(2);
    {
      const unsigned tv0 = (tb0 << 10) | t;
      __builtin_amdgcn_raw_buffer_store_b128(v4u{tv0, tv0, tv0, tv0}, rW,
                                             (WS_T + (tid >> 3) * 1024 + kvh * 128 + (tid & 7) * 16) | oob_if((id >= 8) | (tid >= 64)), 0, AUX_SC1);
      SB();
    }
    TILE_PARTIAL(xh, (v2u{perm_lo(w1r[0][dw], w1r[1][dw]), 0u}), (v2u{perm_hi(w1r[0][dw], w1r[1][dw]), 0u}), L_QP + 4096);
    lds_arrive(lds, L_CNT, lane);
    lds_wait(lds, L_CNT, 4);
    TILE_PUBLISH(L_QP + 4096, (WS_H + ((4 * j1 + b) * 4 + w) * 2048 + kvh * 256 + m16 * 16) | oob_if(j == JSKIP));
    STAMP(3);
    lds_wait(lds, L_CNT + 8, 1);
    STAMP(5);
    {
      int qgo = WS_Q + (kvh * 4 + b) * 8192 + (2 * w + (lane >> 5)) * 1024 + (g4 & 1) * 256 + m16 * 16;
      v4u gt = ld16<AUX_SC1>(rW, (WS_T + kvh * 1024 + lane * 128) | oob_if((w != 2) | (lane >= 8)), 0);
      v4u g0, g1;
      _Pragma("nounroll") for (unsigned spin = 0; spin < SPIN_CAP; ++spin) {
        REREAD(qgo);
        g0 = ld16<AUX_SC1>(rW, qgo, 0);
        g1 = ld16<AUX_SC1>(rW, qgo, 512);
        if (wave_none((untag(g0, t) | untag(g1, t)) != 0u)) break;
        __builtin_amdgcn_s_sleep(8);
      }
      float q[4];
#pragma unroll
      for (int k = 0; k < 4; ++k) q[k] = x16_sum(u2f(g0[k]) + u2f(g1[k]));
      if ((g4 & 1) == 0) *(v2u*)(lds + L_Q + ((2 * w + (lane >> 5)) * 64 + 4 * m16) * 2) = v2u{pkbf(q[0], q[1]), pkbf(q[2], q[3])};
      const bool seen = (untag(gt, t) == 0u) & (lane < 8);
      const float sum = row_sum(seen ? (float)((int)(gt.x - (tb0 << 10)) >> 10) : 0.f);
      if (tid == 128) *(unsigned*)(lds + L_TAG + 12) = tb0 + (int)(sum * 0.125f);
    }
    STAMP(4);
    LDSBAR();
    {
      const unsigned pw = __builtin_amdgcn_raw_buffer_load_b32(rW, (WS_O + ((bp * 8 + kvh) * 16 + r16) * 4096 + tid * 128) | oob_if(tid >= 32), 0, AUX_SC1);
      asm volatile("" :: "v"(pw));
    }
    if (w == 3) {
      const int go_ = (lane * 16) | oob_if(lane >= 56);
      const v4u xg0 = ld16<0>(rX, go_, 2 * bp * DM * 2), xg1 = ld16<0>(rX, go_, (2 * bp + 1) * DM * 2);
      const v4u gg = ld16<0>(mk(KARG(const void*, ka[14], ka[15]), DM * 2), go_, 0);
      const bool hon = r16 < 14;
      int hgo = (WS_H + ((fs >> 2) * 4 + 2 * bp + (lane >> 5)) * 2048 + ((lane >> 2) & 7) * 256 + (fs & 3) * 64 + (lane & 3) * 16) | oob_if(!hon);
      v4u gh;
      _Pragma("nounroll") for (unsigned spin = 0; spin < SPIN_CAP; ++spin) {
        REREAD(hgo);
        gh = ld16<AUX_SC1>(rW, hgo, 0);
        if (wave_none(hon & (untag(gh, t) != 0u))) break;
        __builtin_amdgcn_s_sleep(8);
      }
      float d0 = 0.f, d1 = 0.f;
#pragma unroll
      for (int k = 0; k < 4; ++k) { d0 = dot2(xg0[k], gg[k], d0); d1 = dot2(xg1[k], gg[k], d1); }
      const float gate = 1.f / (1.f + __expf(-x16_sum(row_sum(fold32(d0, d1)))));
      if ((lane & 31) == 0) *(float*)(lds + L_TAG + 4 + 4 * (lane >> 5)) = gate;
      if (lane == 1) *(unsigned*)(lds + L_TAG) = t;
      float h[4];
#pragma unroll
      for (int k = 0; k < 4; ++k) {
        float a = hon ? u2f(gh[k]) : 0.f;
        a += dpp<ROR4>(a);
        a += dpp<ROR8>(a);
        h[k] = fmaxf(x16_sum(a), 0.f) * (1.f - gate);
      }
      if ((lane & 28) == 0) *(v2u*)(lds + L_HP + (lane >> 5) * 32 + (lane & 3) * 8) = v2u{pkbf(h[0], h[1]), pkbf(h[2], h[3])};
    }
    lds_wait(lds, L_CNT + 4, 4);
    const f32x4 e0 = *(const f32x4*)(lds + L_ST + (tid >> 7) * 32), e1 = *(const f32x4*)(lds + L_ST + (tid >> 7) * 32 + 16);
    const float M = fmx(fmx(e0[0], e0[2], inf), fmx(e1[0], e1[2], inf), inf);
    f32x4 fc = {__builtin_amdgcn_exp2f(e0[0] - M), __builtin_amdgcn_exp2f(e0[2] - M), __builtin_amdgcn_exp2f(e1[0] - M), __builtin_amdgcn_exp2f(e1[2] - M)};
    const float L = (fc[0] * e0[1] + fc[1] * e0[3]) + (fc[2] * e1[1] + fc[3] * e1[3]);
    fc *= 1.f / L;
    const float lse = M + __builtin_amdgcn_logf(L);
    float lse_hi = u2f(f2u(lse) & ~TAG_MASK), lse_lo = lse - lse_hi;
    int oa = L_OM + 2 * (tid & 3) * OM_ROW + (tid >> 2) * 4, fa = L_FM + 2 * (tid & 3) * FM_ROW + (tid >> 2) * 8;
    int odst = WS_O + ((bp * 8 + kvh) * 32 + (tid >> 3)) * 2048 + (r16 ^ ((tid >> 3) & 7)) * 128 + (tid & 7) * 16;
    int fdst = (WS_F + (bp * 28 + (tid >> 3)) * 16384 + (pu ^ ((tid >> 3) & 7)) * 128 + (tid & 7) * 16) | oob_if(tid >= 224);
    unsigned tv = t, keep = ~TAG_MASK;
    asm volatile("" : "+v"(lse_hi), "+v"(lse_lo), "+v"(fc), "+v"(oa), "+v"(fa), "+v"(odst), "+v"(fdst), "+v"(tv), "+s"(keep));
    STAMP(6);
    LDSBAR();
    {
      float a = 0.f, c = 0.f;
#pragma unroll
      for (int ww = 0; ww < 4; ++ww) {
        a = fmaf(fc[ww], *(const float*)(lds + oa + ww * 8 * OM_ROW), a);
        c = fmaf(fc[ww], *(const float*)(lds + oa + ww * 8 * OM_ROW + OM_ROW), c);
      }
      publish(rW, odst, a, c, lse_hi, lse_lo, tv, keep);
    }
    STAMP(7);
    LDSBAR();
    {
      f32x2 p0[4], p1[4];
#pragma unroll
      for (int ww = 0; ww < 4; ++ww) {
        p0[ww] = *(const f32x2*)(lds + fa + ww * 8 * FM_ROW);
        p1[ww] = *(const f32x2*)(lds + fa + ww * 8 * FM_ROW + FM_ROW);
      }
      const f32x2 s0 = (p0[0] + p0[1]) + (p0[2] + p0[3]), s1 = (p1[0] + p1[1]) + (p1[2] + p1[3]);
      publish(rW, fdst, s0.x, s0.y, s1.x, s1.y, tv, keep);
    }
    STAMP(9);
  } else {
    const rsrc_t rV = mk(KARG(const char*, ka[4], ka[5]) + head, WIN * DH * 2);
    v4u kr[32], vr[32];
    int lag = 0;
    unsigned tref = t0.x + PACE_A;
#pragma unroll
    for (int i = 0; i < 4; ++i) kr[i] = first[i];
    STAMP(1);
#pragma unroll
    for (int i = 4; i < 32; ++i) {
      if (i >= 12) THROTTLE(); else if (i >= 8) VMWAIT(WIN_BASE - 1);
      kr[i] = ld16<AUX_NT>(rF, fo, (i >> 2) * BSTR + (i & 3) * 128);
      if (i >= 11 && (i & 3) == 3) PACE(i);
      if (i == KB0) { LDSBAR(); STAMP(2); }
      if (i == QG_AT && w == 0) lds_arrive(lds, L_CNT + 8, lane);
    }
    const int vo = blk0 + lane * 16;
#define VSTEP(i) do { THROTTLE(); vr[i] = ld16<AUX_NT>(rV, vo, ((i) >> 2) * BSTR + ((i) & 3) * 1024); SB(); \
                      if (((i) & 3) == 3) PACE(32 + (i)); } while (0)
    asm volatile("" :: "v"(cpf), "v"(tw), "v"(xq), "v"(ep));
    const int lagk = lag;
    STAMP(3);
#pragma unroll
    for (int i = 0; i < VB2; ++i) VSTEP(i);
    LDSBAR();
    STAMP(4);
    tref = __builtin_amdgcn_readfirstlane(*(const unsigned*)(lds + L_TAG + 12)) + (PACE_B - (PACE_T * 31) / 10);
    PACE(31 + VB2);
    v4u pa[4];
    {
      v4u qf[2][4];
#pragma unroll
      for (int h = 0; h < 2; ++h)
#pragma unroll
        for (int t = 0; t < 4; ++t) qf[h][t] = *(const v4u*)(lds + L_Q + (h * 256 + 64 * t + 8 * (lane & 7)) * 2);
      VSTEP(VB2);
      float s[8][2];
#pragma unroll
      for (int i = 0; i < 8; i += 2) {
        float a0 = 0.f, a1 = 0.f, c0 = 0.f, c1 = 0.f;
#pragma unroll
        for (int t = 0; t < 4; ++t) {
#pragma unroll
          for (int dw = 0; dw < 4; ++dw) {
            a0 = dot2(kr[4 * i + t][dw], qf[0][t][dw], a0); c0 = dot2(kr[4 * i + 4 + t][dw], qf[0][t][dw], c0);
            a1 = dot2(kr[4 * i + t][dw], qf[1][t][dw], a1); c1 = dot2(kr[4 * i + 4 + t][dw], qf[1][t][dw], c1);
          }
          if (t == 1) VSTEP(VB2 + 1 + i);
        }
        s[i][0] = a0; s[i][1] = a1; s[i + 1][0] = c0; s[i + 1][1] = c1;
        VSTEP(VB2 + 2 + i);
      }
#pragma unroll
      for (int i = 0; i < 4; ++i)
#pragma unroll
        for (int h = 0; h < 2; ++h) {
          const float lo = s[i][h] + dpp<XOR7>(s[i][h]), hi = s[i + 4][h] + dpp<XOR7>(s[i + 4][h]);
          s[i][h] = (lane & 4) ? hi : lo;
        }
#pragma unroll
      for (int i = 0; i < 2; ++i)
#pragma unroll
        for (int h = 0; h < 2; ++h) {
          const float lo = s[i][h] + dpp<XOR2>(s[i][h]), hi = s[i + 2][h] + dpp<XOR2>(s[i + 2][h]);
          s[i][h] = (lane & 2) ? hi : lo;
        }
#pragma unroll
      for (int h = 0; h < 2; ++h) {
        const float lo = s[0][h] + dpp<XOR1>(s[0][h]), hi = s[1][h] + dpp<XOR1>(s[1][h]);
        s[0][h] = (lane & 1) ? hi : lo;
      }
      VSTEP(VB2 + 9);
      constexpr float C2 = 0.0625f * 1.4426950408889634f;
      const float z0 = s[0][0] * C2, z1 = s[0][1] * C2;
      const float m0 = wave_max(z0, inf), m1 = wave_max(z1, inf);
      const float p0 = __builtin_amdgcn_exp2f(z0 - m0), p1 = __builtin_amdgcn_exp2f(z1 - m1);
      const unsigned pp = pkbf(p0, p1);
      unsigned short* pl = (unsigned short*)(lds + L_P + w * 256 + ((lane >> 3) & 1) * 64 + (lane & 7) * 8 + (lane >> 4) * 2);
      pl[0] = (unsigned short)pp;
      pl[64] = (unsigned short)(pp >> 16);
      VSTEP(VB2 + 10);
      const float l01 = x16_sum(row_sum(fold32(p0, p1)));
      if ((lane & 31) == 0) *(f32x2*)(lds + L_ST + ((lane >> 5) * 4 + w) * 8) = f32x2{lane ? m1 : m0, l01};
      lds_arrive(lds, L_CNT + 4, lane);
      const bool valid = (m16 & 3) == 0 && ((m16 >> 2) & 1) == (g4 & 1);
      const int a = valid ? L_P + w * 256 + (m16 >> 3) * 128 + (g4 >> 1) * 64 : L_Z;
#pragma unroll
      for (int G = 0; G < 4; ++G) pa[G] = *(const v4u*)(lds + a + G * 16);
    }
    STAMP(5);
    f32x4 acc[8];
#pragma unroll
    for (int e = 0; e < 8; ++e) acc[e] = zero4;
    v4u tr[12], bf[8];
    const rsrc_t rO = mk(KARG(const void*, ka[8], ka[9]), NQ * DM * 2), r2 = mk(KARG(const void*, ka[12], ka[13]), DFF * DM * 2);
    const int o2 = ((16 * fs + 4 * w) * (DM * 2) + lane * 16) | oob_if((r16 >= 14) | (lane >= 56));
    const int oo = ((kvh * 512 + 32 * r16 + 8 * w) * (DM * 2) + lane * 16) | oob_if(lane >= 56);
#define TLOAD(i) tr[i] = (i) < 4 ? ld16<AUX_NT>(r2, o2, (i) * (DM * 2)) : ld16<AUX_NT>(rO, oo, ((i) - 4) * (DM * 2))
#define PAIRUP(src, pr) do { _Pragma("unroll") for (int dw = 0; dw < 4; ++dw) { \
      unsigned lo_ = perm_lo((src)[2 * (pr)][dw], (src)[2 * (pr) + 1][dw]), hi_ = perm_hi((src)[2 * (pr)][dw], (src)[2 * (pr) + 1][dw]); \
      asm volatile("" : "+v"(lo_), "+v"(hi_)); bf[2 * dw][pr] = lo_; bf[2 * dw + 1][pr] = hi_; } SB(); } while (0)
#define PVMMA(G) do { _Pragma("unroll") for (int e = 0; e < 8; ++e) acc[e] = mfma32(pa[G], bf[e], acc[e]); } while (0)
#define PVGROUP(G) do { PAIRUP(vr + 8 * (G), 0); PAIRUP(vr + 8 * (G), 1); PAIRUP(vr + 8 * (G), 2); PAIRUP(vr + 8 * (G), 3); PVMMA(G); SB(); } while (0)
#pragma unroll
    for (int i = VB2 + 11; i < 32; ++i) {
      VSTEP(i);
      if (i == VB2 + 11) PVGROUP(0);
      if (i == 24) PVGROUP(1);
      if (i == 31) PVGROUP(2);
    }
#pragma unroll
    for (int k = 0; k < 8; ++k) {
      VMWAIT(7);
      TLOAD(k);
      SB();
      if (k == 1) PAIRUP(vr + 24, 0);
      if (k == 3) PAIRUP(vr + 24, 1);
      if (k == 5) PAIRUP(vr + 24, 2);
    }
    int oma = L_OM + w * 8 * OM_ROW + lane * 4;
    asm volatile("" : "+v"(oma));
    SB();
#pragma unroll
    for (int dw = 0; dw < 4; ++dw) { bf[2 * dw][3] = perm_lo(vr[30][dw], vr[31][dw]); bf[2 * dw + 1][3] = perm_hi(vr[30][dw], vr[31][dw]); }
    PVMMA(3);
    STAMP(6);
#pragma unroll
    for (int e = 0; e < 8; ++e) *(float*)(lds + oma + e * OM_ROW) = acc[e][0];
    LDSBAR();
    const unsigned tb3 = now();
    STAMP(7);
#pragma unroll
    for (int i = 8; i < 12; ++i) TLOAD(i);
    SB();
    const bool diag = (m16 >> 2) == g4;
    const v4u tg = *(const v4u*)(lds + L_TAG);
    const v2u ah = *(const v2u*)(lds + ((diag && (m16 & 3) < 2) ? L_HP + (m16 & 3) * 32 + w * 8 : L_Z));
    const unsigned t = tg.x;
#pragma unroll
    for (int dw = 0; dw < 4; ++dw) {
      acc[2 * dw] = mfma16(ah, v2u{perm_lo(tr[0][dw], tr[1][dw]), perm_lo(tr[2][dw], tr[3][dw])}, zero4);
      acc[2 * dw + 1] = mfma16(ah, v2u{perm_hi(tr[0][dw], tr[1][dw]), perm_hi(tr[2][dw], tr[3][dw])}, zero4);
    }
    SB();
    int ogo = WS_O + (((bp * 8 + kvh) * 16 + r16) * 2 + (w >> 1)) * 2048 + (lane >> 2) * 128 + (w & 1) * 64 + (lane & 3) * 16;
    float gate = u2f(lane < 32 ? tg.y : tg.z);
    unsigned m0 = (diag && (lane & 1) == 0) ? 0xffffffffu : 0u, m1 = (diag && (lane & 1) != 0) ? 0xffffffffu : 0u;
    int fma_ = L_FM + w * 8 * FM_ROW + lane * 8;
    asm volatile("" : "+v"(gate), "+v"(m0), "+v"(m1), "+v"(fma_), "+v"(ogo));
    SB();
    PAIRUP(tr + 4, 0);
    PAIRUP(tr + 4, 1);
    while ((int)(now() - tb3) < O_DELAY) {}
    SB();
    v4u go = ld16<AUX_SC1>(rW, ogo, 0);
    SB();
    PAIRUP(tr + 4, 2);
    PAIRUP(tr + 4, 3);
    STAMP(11);
    if (!wave_none(untag(go, t) != 0u)) {
      _Pragma("nounroll") for (unsigned spin = 0; spin < SPIN_CAP; ++spin) {
        REREAD(ogo);
        go = ld16<AUX_SC1>(rW, ogo, 0);
        if (wave_none(untag(go, t) != 0u)) break;
      }
    }
    STAMP(8);
    {
      const float lm = u2f(go.z) + u2f(go.w);
      float M = fmx(lm, dpp<ROR4>(lm), inf);
      M = fmx(M, dpp<ROR8>(M), inf);
      M = x16_max(M, inf);
      float d = __builtin_amdgcn_exp2f(lm - M);
      float a = d * u2f(go.x), c = d * u2f(go.y);
      a += dpp<ROR4>(a); c += dpp<ROR4>(c); d += dpp<ROR4>(d);
      a += dpp<ROR8>(a); c += dpp<ROR8>(c); d += dpp<ROR8>(d);
      a = x16_sum(a); c = x16_sum(c);
      d = gate * __builtin_amdgcn_rcpf(x16_sum(d));
      const unsigned op = pkbf(a * d, c * d);
      const auto sw = __builtin_amdgcn_permlane32_swap(op, op, false, false);
      const v4u b0 = {dppu<0x00>(sw[0]), dppu<0x55>(sw[0]), dppu<0xAA>(sw[0]), dppu<0xFF>(sw[0])};
      const v4u b1 = {dppu<0x00>(sw[1]), dppu<0x55>(sw[1]), dppu<0xAA>(sw[1]), dppu<0xFF>(sw[1])};
      const v4u ao = (b0 & m0) | (b1 & m1);
#pragma unroll
      for (int e = 0; e < 8; ++e) acc[e] = mfma32(ao, bf[e], acc[e]);
#pragma unroll
      for (int e = 0; e < 8; ++e) *(f32x2*)(lds + fma_ + e * FM_ROW) = f32x2{acc[e][0], acc[e][1]};
    }
    if (w == 1) __builtin_amdgcn_raw_buffer_store_b64(v2u{tb3 - tref, (unsigned)lagk}, mk(ws + WS_DIAG, 65536), (id * 128) | oob_if(lane != 0), 0, AUX_SC1);
    LDSBAR();
    if (w == 0 && pu < 112) {
      const unsigned tb4 = now();
      int fgo = WS_F + (bp * 28 + (pu >> 2)) * 16384 + (lane >> 1) * 128 + ((pu & 3) * 2 + (lane & 1)) * 16;
      int oo2 = (((2 * bp + (lane >> 5)) * DM + 4 * pu + 2 * (lane & 1) + (g4 & 1)) * 2) | oob_if((lane & 14) != 0);
      asm volatile("" : "+v"(oo2));
      v4u gf[4];
      while ((int)(now() - tb4) < F_DELAY) {}
      SB();
      _Pragma("nounroll") for (unsigned spin = 0; spin < SPIN_CAP; ++spin) {
        REREAD(fgo);
#pragma unroll
        for (int k = 0; k < 4; ++k) gf[k] = ld16<AUX_SC1>(rW, fgo, k * 4096);
        const unsigned miss = (untag(gf[0], t) | untag(gf[1], t)) | (untag(gf[2], t) | untag(gf[3], t));
        if (wave_none(miss != 0u)) break;
      }
      float v[4];
#pragma unroll
      for (int k = 0; k < 4; ++k) v[k] = (u2f(gf[0][k]) + u2f(gf[1][k])) + (u2f(gf[2][k]) + u2f(gf[3][k]));
      float r = fold16(fold32(v[0], v[1]), fold32(v[2], v[3]));
      r += dpp<ROR8>(r); r += dpp<ROR4>(r); r += dpp<XOR2>(r);
      __builtin_amdgcn_raw_buffer_store_b16((unsigned short)pkbf(r, r), mk(KARG(const void*, kb[0], kb[1]), NB * DM * 2), oo2, 0, 0);
      if (id == 0 && lane == 0) __builtin_amdgcn_raw_buffer_store_b32(t, rW, WS_E, 0, AUX_SC1);
      if (id == 0) {
        const unsigned tend = now();
        while ((int)(now() - tend) < 300) {}
        float sa = 0.f, mxa = -1e9f, sb = 0.f;
        for (int k = 0; k < 4; ++k) {
          const v2u z = __builtin_amdgcn_raw_buffer_load_b64(mk(ws + WS_DIAG, 65536), (k * 64 + lane) * 128, 0, AUX_SC1);
          const float a = (float)(int)z.x, c = (float)(int)z.y;
          sa += a; sb += c; mxa = fmaxf(mxa, a);
        }
        sa = x16_sum(row_sum(fold32(sa, sa))) * (1.f / 256.f); sb = x16_sum(row_sum(fold32(sb, sb))) * (1.f / 256.f); mxa = wave_max(mxa, inf);
        float x = PROBE == 5 ? sa - 950.f : PROBE == 6 ? mxa - 950.f : PROBE == 7 ? sb + 100.f : 0.f;
        x = fminf(fmaxf(x, 0.f), 800.f);
        const int xi = __builtin_amdgcn_readfirstlane((int)x) * 10;
        const unsigned tb = now();
        while ((int)(now() - tb) < xi) {}
      }
    }
  }
}

}

extern "C" void kernel_launch(void* const* d_in, const int* in_sizes, int n_in, void* d_out, int out_size, void* d_ws, size_t ws_size,
                              hipStream_t stream) {
  (void)in_sizes; (void)n_in; (void)out_size; (void)ws_size;
  const char* const kc = (const char*)d_in[1]; const char* const vc = (const char*)d_in[2];
  const char* const wq = (const char*)d_in[3]; const char* const wo = (const char*)d_in[4];
  const char* const w1 = (const char*)d_in[6]; const char* const w2 = (const char*)d_in[7];
  const char* const ws = (const char*)d_ws;
  constexpr unsigned HEAD = SK * DH * 2, W0 = (SK - WIN) * DH * 2, WB = WIN * DH * 2, WQB = DM * NQ * 2, W1B = DM * DFF * 2;
  Touch tt;
  for (int n = 0; n < 16; ++n)
    tt.e[n] = TouchEntry{((n >= 4 && n < 12) ? vc : kc) + (size_t)(n & 3) * HKV * HEAD + W0 + (((n >> 2) & 1) ? 0 : WB - 128), HEAD, 0u};
  const char* const rest[16] = {wq, wq + WQB / 2, wq + WQB - 1024, w1, w1 + W1B - 1024, w2, w2 + W1B - 1024, wo, wo + WQB / 8 - 128,
                                (const char*)d_in[0], (const char*)d_in[8], (const char*)d_out, ws + 1024, ws + WS_O - 1024, ws + WS_END,
                                vc + W0 + WB / 2};
  const unsigned step[16] = {128, 128, 128, 128, 128, 128, 128, WQB / 8, WQB / 8, 128, 112, 128, 128, 128, 128, HEAD};
  for (int n = 0; n < 16; ++n) tt.e[16 + n] = TouchEntry{rest[n], step[n], 0u};
  gated_mix_decode_kernel<<<256, 512, 0, stream>>>(
      (const unsigned short*)d_in[0], (const unsigned short*)kc, (const unsigned short*)vc, (const unsigned short*)wq, (const unsigned short*)wo,
      (const unsigned short*)w1, (const unsigned short*)w2, (const unsigned short*)d_in[8], (unsigned short*)d_out, (unsigned char*)d_ws, tt);
}
```

```cpp
#define PROBE 4
#include <hip/hip_runtime.h>
#include <hip/hip_bf16.h>
#include <cstdint>

namespace {

typedef unsigned v4u __attribute__((ext_vector_type(4)));
typedef unsigned v2u __attribute__((ext_vector_type(2)));
typedef unsigned v16u __attribute__((ext_vector_type(16)));
typedef float f32x4 __attribute__((ext_vector_type(4)));
typedef float f32x2 __attribute__((ext_vector_type(2)));
typedef short s16x4 __attribute__((ext_vector_type(4)));
typedef __bf16 bf16x8 __attribute__((ext_vector_type(8)));
typedef __bf16 bf16x2 __attribute__((ext_vector_type(2)));
typedef __amdgpu_buffer_rsrc_t rsrc_t;
typedef __attribute__((address_space(3))) unsigned lds_u32;

constexpr int NB = 4, DM = 448, HKV = 8, SK = 8192, DH = 256, WIN = 2048, DFF = 1792, NQ = 4096;
constexpr int AUX_NT = 2, AUX_SC1 = 16;
constexpr int OOB = 0x40000000;
constexpr unsigned SPIN_CAP = 200000, TAG_MASK = 1023u;

#define WIN_BASE 5
#define WIN_LAG 10
#define PACE_A 80
#define PACE_B 315
#define PACE_T 160
#define KB0 8
#define QG_AT 22
#define VB2 4
#define T_LAG 30
#define T_HOLD 3
#define O_AT 1145
#define O_MIN 40
#define O_MAX 85
#define F_DELAY 66
#define CODE_LINES 102
constexpr int BSTR = 131072;
constexpr int JSKIP = 3;

constexpr int WS_E = 0;
constexpr int WS_T = 4096;
constexpr int WS_Q = WS_T + 8192;
constexpr int WS_H = WS_Q + 8 * 4 * 8192;
constexpr int WS_O = WS_H + 28 * 4 * 2048 + 4096;
constexpr int WS_F = WS_O + 16 * 16 * 4096;
constexpr int WS_END = WS_F + 2 * 28 * 16384;
constexpr int WS_DIAG = 8 << 20;

constexpr int OM_ROW = 68 * 4, FM_ROW = 544;
constexpr int L_QP = 0;
constexpr int L_Q = 8192;
constexpr int L_P = 9216;
constexpr int L_Z = 10240;
constexpr int L_CNT = 10304;
constexpr int L_TAG = 10320;
constexpr int L_HP = 10368;
constexpr int L_ST = 10432;
constexpr int L_OM = 10496;
constexpr int L_FM = L_OM + 32 * OM_ROW;
constexpr int L_END = L_FM + 32 * FM_ROW;

__device__ __forceinline__ rsrc_t mk(const void* p, int bytes) { return __builtin_amdgcn_make_buffer_rsrc((void*)p, 0, bytes, 0x00020000); }
template <int AUX> __device__ __forceinline__ v4u ld16(rsrc_t r, int vo, int so) { return __builtin_amdgcn_raw_buffer_load_b128(r, vo, so, AUX); }
__device__ __forceinline__ int oob_if(bool c) { return c ? OOB : 0; }
__device__ __forceinline__ float u2f(unsigned u) { return __builtin_bit_cast(float, u); }
__device__ __forceinline__ unsigned f2u(float f) { return __builtin_bit_cast(unsigned, f); }
__device__ __forceinline__ unsigned now() { return (unsigned)__builtin_amdgcn_s_memrealtime(); }
__device__ __forceinline__ void publish(rsrc_t r, int vo, float a, float b, float c, float d, unsigned t, unsigned keep = ~TAG_MASK) {
  __builtin_amdgcn_raw_buffer_store_b128(v4u{(f2u(a) & keep) | t, (f2u(b) & keep) | t, (f2u(c) & keep) | t, (f2u(d) & keep) | t}, r, vo, 0, AUX_SC1);
}
__device__ __forceinline__ unsigned untag(v4u& g, unsigned t) { g ^= t; return (g.x | g.y | g.z | g.w) & TAG_MASK; }
__device__ __forceinline__ bool wave_none(bool p) { return __builtin_amdgcn_ballot_w64(p) == 0; }
__device__ __forceinline__ float dot2(unsigned a, unsigned b, float c) {
  return __builtin_amdgcn_fdot2_f32_bf16(__builtin_bit_cast(bf16x2, a), __builtin_bit_cast(bf16x2, b), c, false);
}
__device__ __forceinline__ unsigned pkbf(float lo, float hi) {
  const bf16x2 v = {(__bf16)lo, (__bf16)hi};
  return __builtin_bit_cast(unsigned, v);
}
__device__ __forceinline__ unsigned perm_lo(unsigned a, unsigned b) { return __builtin_amdgcn_perm(b, a, 0x05040100u); }
__device__ __forceinline__ unsigned perm_hi(unsigned a, unsigned b) { return __builtin_amdgcn_perm(b, a, 0x07060302u); }
template <int CTRL> __device__ __forceinline__ unsigned dppu(unsigned x) { return (unsigned)__builtin_amdgcn_mov_dpp((int)x, CTRL, 0xf, 0xf, true); }
template <int CTRL> __device__ __forceinline__ float dpp(float x) { return u2f(dppu<CTRL>(f2u(x))); }
constexpr int XOR1 = 0xB1, XOR2 = 0x4E, XOR7 = 0x141, ROR8 = 0x128, ROR4 = 0x124;
__device__ __forceinline__ float fmx(float a, float c, float inf) { return __builtin_amdgcn_fmed3f(a, c, inf); }
__device__ __forceinline__ float fold16(float a, float b) {
  auto s = __builtin_amdgcn_permlane16_swap(f2u(a), f2u(b), false, false);
  return u2f(s[0]) + u2f(s[1]);
}
__device__ __forceinline__ float fold32(float a, float b) {
  auto s = __builtin_amdgcn_permlane32_swap(f2u(a), f2u(b), false, false);
  return u2f(s[0]) + u2f(s[1]);
}
__device__ __forceinline__ float x16_sum(float x) { return fold16(x, x); }
__device__ __forceinline__ float x16_max(float x, float inf) {
  auto s = __builtin_amdgcn_permlane16_swap(f2u(x), f2u(x), false, false);
  return fmx(u2f(s[0]), u2f(s[1]), inf);
}
__device__ __forceinline__ float row_sum(float x) { x += dpp<XOR1>(x); x += dpp<XOR2>(x); x += dpp<ROR4>(x); x += dpp<ROR8>(x); return x; }
__device__ __forceinline__ float wave_max(float x, float inf) {
  x = fmx(x, dpp<XOR1>(x), inf); x = fmx(x, dpp<XOR2>(x), inf); x = fmx(x, dpp<ROR4>(x), inf); x = fmx(x, dpp<ROR8>(x), inf);
  x = x16_max(x, inf);
  auto s = __builtin_amdgcn_permlane32_swap(f2u(x), f2u(x), false, false);
  return fmx(u2f(s[0]), u2f(s[1]), inf);
}
__device__ __forceinline__ f32x4 mfma32(v4u a, v4u b, f32x4 c) {
  return __builtin_amdgcn_mfma_f32_16x16x32_bf16(__builtin_bit_cast(bf16x8, a), __builtin_bit_cast(bf16x8, b), c, 0, 0, 0);
}
__device__ __forceinline__ f32x4 mfma16(v2u a, v2u b, f32x4 c) {
  return __builtin_amdgcn_mfma_f32_16x16x16bf16_1k(__builtin_bit_cast(s16x4, a), __builtin_bit_cast(s16x4, b), c, 0, 0, 0);
}

#define SB() __builtin_amdgcn_sched_barrier(0)
#define REREAD(off) asm volatile("" : "+v"(off) :: "memory")
#define VMWAIT(n) do { SB(); __builtin_amdgcn_s_waitcnt(((n) & 15) | (7 << 4) | (15 << 8) | ((((n) >> 4) & 3) << 14)); SB(); } while (0)
#define THROTTLE() do { VMWAIT(WIN_LAG - 1); \
    asm volatile("s_cmp_gt_i32 %0, 0\n\ts_cbranch_scc1 1f\n\ts_waitcnt vmcnt(%1)\n1:" :: "s"(lag), "i"(WIN_BASE - 1) : "scc"); SB(); } while (0)
#define PACE(k) do { lag = (int)(now() - tref) - (PACE_T * (k)) / 10; SB(); } while (0)
#define LDSBAR() do { SB(); __builtin_amdgcn_fence(__ATOMIC_RELEASE, "workgroup", "local"); __builtin_amdgcn_s_barrier(); \
                      __builtin_amdgcn_fence(__ATOMIC_ACQUIRE, "workgroup", "local"); SB(); } while (0)

__device__ __forceinline__ void lds_arrive(unsigned char* lds, int off, int lane) {
  __builtin_amdgcn_fence(__ATOMIC_RELEASE, "workgroup", "local");
  if (lane == 0) __hip_atomic_fetch_add((lds_u32*)(lds + off), 1u, __ATOMIC_RELAXED, __HIP_MEMORY_SCOPE_WORKGROUP);
}
__device__ __forceinline__ void lds_wait(unsigned char* lds, int off, unsigned want) {
  _Pragma("nounroll") for (unsigned spin = 0; spin < 16u * SPIN_CAP; ++spin) {
    if ((unsigned)__builtin_amdgcn_readfirstlane(__hip_atomic_load((lds_u32*)(lds + off), __ATOMIC_RELAXED, __HIP_MEMORY_SCOPE_WORKGROUP)) >= want) break;
    __builtin_amdgcn_s_sleep(1);
  }
  __builtin_amdgcn_fence(__ATOMIC_ACQUIRE, "workgroup", "local");
}

#define STAMP(k) do { } while (0)

struct TouchEntry { const void* p; unsigned stride, zero; };
struct Touch { TouchEntry e[32]; };

__global__ void __launch_bounds__(512, 2)
gated_mix_decode_kernel(const unsigned short*, const unsigned short*, const unsigned short*, const unsigned short*, const unsigned short*,
                        const unsigned short*, const unsigned short*, const unsigned short*, unsigned short*, unsigned char*, Touch) {
  __shared__ __attribute__((aligned(128))) unsigned char lds[L_END];
  STAMP(0);
  v16u ka; v4u kb, tp; v2u t0;
  asm volatile("s_load_dwordx4 %2, %4, %5 offset:0x50\n\ts_load_dwordx16 %0, %4, 0x0\n\ts_load_dwordx4 %1, %4, 0x40\n\ts_memrealtime %3"
               : "=&s"(ka), "=&s"(kb), "=&s"(tp), "=&s"(t0) : "s"(__builtin_amdgcn_kernarg_segment_ptr()), "s"((blockIdx.x >> 3) * 16u) : "memory");
  SB();
  unsigned cpf;
  {
    unsigned long long pc;
    asm volatile("s_getpc_b64 %0" : "=s"(pc));
    constexpr int PER = (CODE_LINES + 3) / 4;
    const int line = ((blockIdx.x >> 3) & 3) * PER + threadIdx.x;
    cpf = __builtin_amdgcn_raw_buffer_load_b32(mk((const void*)(pc & ~127ull), CODE_LINES * 128), (line * 128) | oob_if(threadIdx.x >= PER), 0, 0);
    SB();
  }
  const int id = blockIdx.x, kvh = id & 7, b = (id >> 3) & 3, j = id >> 5;
  const int lane = threadIdx.x & 63, wv = __builtin_amdgcn_readfirstlane(threadIdx.x >> 6), w = wv & 3;
  const int m16 = lane & 15, g4 = lane >> 4;
  const bool helper = wv < 4;
  const int blk0 = j * 16384 + w * 4096;
  int fo = ((helper ? (112 * b + 32 * w) * (NQ * 2) + (kvh * 8 + j) * 128 : blk0) + (lane >> 3) * (helper ? 4 * NQ * 2 : 512) + (lane & 7) * 16) |
           oob_if((lane >> 3) >= (wv == 3 ? 4 : 8));
  int xo = (((m16 >> 2) * DM + 112 * b + 32 * w + 8 * g4 + 4 * (m16 & 1)) * 2) | oob_if(!helper | ((m16 & 2) != 0) | (32 * w + 8 * g4 >= 112));
  int eo = WS_E | oob_if(!helper), to = oob_if(threadIdx.x != 0);
  unsigned head = ((id & 31) * SK + (SK - WIN)) * DH * 2;
  int fs1 = helper ? NQ * 2 : 128, fs2 = 2 * fs1, fs3 = 3 * fs1;
  asm volatile("" : "+v"(fo), "+v"(xo), "+v"(eo), "+v"(to));
  asm volatile("" : "+s"(head), "+s"(fs1), "+s"(fs2), "+s"(fs3));
  asm volatile("s_waitcnt lgkmcnt(0)" : "+s"(ka), "+s"(kb), "+s"(tp), "+s"(t0) :: "memory");
#define KARG(T, lo, hi) ((T)(((unsigned long long)(hi) << 32) | (lo)))
  const unsigned tw = __builtin_amdgcn_raw_buffer_load_b32(mk(KARG(const void*, tp.x, tp.y), OOB), to, tp.z * kvh, AUX_NT);
  SB();
  unsigned char* ws = KARG(unsigned char*, kb[2], kb[3]);
  const rsrc_t rW = mk(ws, WS_END), rX = mk(KARG(const void*, ka[0], ka[1]), NB * DM * 2);
  const v2u xq = __builtin_amdgcn_raw_buffer_load_b64(rX, xo, 0, 0);
  const unsigned ep = __builtin_amdgcn_raw_buffer_load_b32(rW, eo, 0, AUX_SC1);
  SB();
  const rsrc_t rF = mk(helper ? KARG(const char*, ka[6], ka[7]) : KARG(const char*, ka[2], ka[3]) + head, helper ? DM * NQ * 2 : WIN * DH * 2);
  const v4u first[4] = {ld16<AUX_NT>(rF, fo, 0), ld16<AUX_NT>(rF, fo, fs1), ld16<AUX_NT>(rF, fo, fs2), ld16<AUX_NT>(rF, fo, fs3)};
  SB();
  const int bp = b >> 1, r16 = (b & 1) * 8 + j, pu = kvh * 16 + r16, fs = kvh * 14 + r16;
  const f32x4 zero4 = {0.f, 0.f, 0.f, 0.f};
  float inf = __builtin_inff();
  asm volatile("" : "+s"(inf));

  if (__builtin_expect(helper, 1)) {
    const int tid = threadIdx.x;
    const int j1 = j == 7 ? JSKIP : j;
    v4u w1r[2];
    v2u xh;
    {
      const int rr = 2 * g4 + (m16 & 1);
      xh.x = __builtin_amdgcn_raw_buffer_load_b32(rX, (((m16 >> 2) * DM + 56 * kvh + 16 * w + 2 * rr) * 2) | oob_if(((m16 & 2) != 0) | (16 * w + 2 * rr >= 56)), 0, 0);
      xh.y = 0u;
      const rsrc_t r1 = mk(KARG(const void*, ka[10], ka[11]), DM * DFF * 2);
      const int row1 = 16 * w + 2 * (lane >> 3);
      const int o1 = ((56 * kvh + row1) * (DFF * 2) + (4 * j1 + b) * 128 + (lane & 7) * 16) | oob_if((j == JSKIP) | (row1 >= 56));
#pragma unroll
      for (int i = 0; i < 2; ++i) w1r[i] = ld16<AUX_NT>(r1, o1, i * (DFF * 2));
    }
    SB();
    STAMP(11);
    if (tid < 24) *(unsigned*)(lds + L_Z + tid * 4) = 0u;
#define TILE_PARTIAL(AF, BLO, BHI, LOFF) do { float pv[8]; \
      _Pragma("unroll") for (int dw = 0; dw < 4; ++dw) { \
        const f32x4 dl = mfma16(AF, BLO, zero4), dh = mfma16(AF, BHI, zero4); \
        const float sl = (lane & 8) ? dl[1] : dl[0], sh = (lane & 8) ? dh[1] : dh[0]; \
        pv[2 * dw] = sl + dpp<ROR8>(sl); pv[2 * dw + 1] = sh + dpp<ROR8>(sh); } \
      *(f32x4*)(lds + (LOFF) + ((w * 4 + g4) * 64 + 8 * (lane & 7) + ((lane & 8) >> 1)) * 4) = \
          (lane & 8) ? f32x4{pv[4], pv[5], pv[6], pv[7]} : f32x4{pv[0], pv[1], pv[2], pv[3]}; } while (0)
#define TILE_PUBLISH(LOFF, DST) do { f32x4 s = zero4; \
      _Pragma("unroll") for (int ww = 0; ww < 4; ++ww) s += *(const f32x4*)(lds + (LOFF) + ((ww * 4 + w) * 64 + 4 * m16) * 4); \
      publish(rW, (DST) | oob_if(lane >= 16), s[0], s[1], s[2], s[3], t); SB(); } while (0)
    TILE_PARTIAL(xq, (v2u{perm_lo(first[0][dw], first[1][dw]), perm_lo(first[2][dw], first[3][dw])}),
                 (v2u{perm_hi(first[0][dw], first[1][dw]), perm_hi(first[2][dw], first[3][dw])}), L_QP);
    const unsigned t = ((unsigned)__builtin_amdgcn_readfirstlane(ep) + 1u) & TAG_MASK;
    asm volatile("" :: "v"(tw), "v"(cpf));
    STAMP(1);
    LDSBAR();
    const unsigned tb0 = now();
    TILE_PUBLISH(L_QP, WS_Q + (kvh * 4 + w) * 8192 + j * 1024 + b * 256 + m16 * 16);
    STAMP(2);
    {
      const unsigned tv0 = (tb0 << 10) | t;
      __builtin_amdgcn_raw_buffer_store_b128(v4u{tv0, tv0, tv0, tv0}, rW,
                                             (WS_T + (tid >> 3) * 1024 + kvh * 128 + (tid & 7) * 16) | oob_if((id >= 8) | (tid >= 64)), 0, AUX_SC1);
      SB();
    }
    TILE_PARTIAL(xh, (v2u{perm_lo(w1r[0][dw], w1r[1][dw]), 0u}), (v2u{perm_hi(w1r[0][dw], w1r[1][dw]), 0u}), L_QP + 4096);
    lds_arrive(lds, L_CNT, lane);
    lds_wait(lds, L_CNT, 4);
    TILE_PUBLISH(L_QP + 4096, (WS_H + ((4 * j1 + b) * 4 + w) * 2048 + kvh * 256 + m16 * 16) | oob_if(j == JSKIP));
    STAMP(3);
    lds_wait(lds, L_CNT + 8, 1);
    STAMP(5);
    {
      int qgo = WS_Q + (kvh * 4 + b) * 8192 + (2 * w + (lane >> 5)) * 1024 + (g4 & 1) * 256 + m16 * 16;
      v4u gt = ld16<AUX_SC1>(rW, (WS_T + kvh * 1024 + lane * 128) | oob_if((w != 2) | (lane >= 8)), 0);
      v4u g0, g1;
      _Pragma("nounroll") for (unsigned spin = 0; spin < SPIN_CAP; ++spin) {
        REREAD(qgo);
        g0 = ld16<AUX_SC1>(rW, qgo, 0);
        g1 = ld16<AUX_SC1>(rW, qgo, 512);
        if (wave_none((untag(g0, t) | untag(g1, t)) != 0u)) break;
        __builtin_amdgcn_s_sleep(8);
      }
      float q[4];
#pragma unroll
      for (int k = 0; k < 4; ++k) q[k] = x16_sum(u2f(g0[k]) + u2f(g1[k]));
      if ((g4 & 1) == 0) *(v2u*)(lds + L_Q + ((2 * w + (lane >> 5)) * 64 + 4 * m16) * 2) = v2u{pkbf(q[0], q[1]), pkbf(q[2], q[3])};
      const bool seen = (untag(gt, t) == 0u) & (lane < 8);
      const float sum = row_sum(seen ? (float)((int)(gt.x - (tb0 << 10)) >> 10) : 0.f);
      if (tid == 128) *(unsigned*)(lds + L_TAG + 12) = tb0 + (int)(sum * 0.125f);
    }
    STAMP(4);
    LDSBAR();
    {
      const unsigned pw = __builtin_amdgcn_raw_buffer_load_b32(rW, (WS_O + ((bp * 8 + kvh) * 16 + r16) * 4096 + tid * 128) | oob_if(tid >= 32), 0, AUX_SC1);
      asm volatile("" :: "v"(pw));
    }
    if (w == 3) {
      const int go_ = (lane * 16) | oob_if(lane >= 56);
      const v4u xg0 = ld16<0>(rX, go_, 2 * bp * DM * 2), xg1 = ld16<0>(rX, go_, (2 * bp + 1) * DM * 2);
      const v4u gg = ld16<0>(mk(KARG(const void*, ka[14], ka[15]), DM * 2), go_, 0);
      const bool hon = r16 < 14;
      int hgo = (WS_H + ((fs >> 2) * 4 + 2 * bp + (lane >> 5)) * 2048 + ((lane >> 2) & 7) * 256 + (fs & 3) * 64 + (lane & 3) * 16) | oob_if(!hon);
      v4u gh;
      _Pragma("nounroll") for (unsigned spin = 0; spin < SPIN_CAP; ++spin) {
        REREAD(hgo);
        gh = ld16<AUX_SC1>(rW, hgo, 0);
        if (wave_none(hon & (untag(gh, t) != 0u))) break;
        __builtin_amdgcn_s_sleep(8);
      }
      float d0 = 0.f, d1 = 0.f;
#pragma unroll
      for (int k = 0; k < 4; ++k) { d0 = dot2(xg0[k], gg[k], d0); d1 = dot2(xg1[k], gg[k], d1); }
      const float gate = 1.f / (1.f + __expf(-x16_sum(row_sum(fold32(d0, d1)))));
      if ((lane & 31) == 0) *(float*)(lds + L_TAG + 4 + 4 * (lane >> 5)) = gate;
      if (lane == 1) *(unsigned*)(lds + L_TAG) = t;
      float h[4];
#pragma unroll
      for (int k = 0; k < 4; ++k) {
        float a = hon ? u2f(gh[k]) : 0.f;
        a += dpp<ROR4>(a);
        a += dpp<ROR8>(a);
        h[k] = fmaxf(x16_sum(a), 0.f) * (1.f - gate);
      }
      if ((lane & 28) == 0) *(v2u*)(lds + L_HP + (lane >> 5) * 32 + (lane & 3) * 8) = v2u{pkbf(h[0], h[1]), pkbf(h[2], h[3])};
    }
    lds_wait(lds, L_CNT + 4, 4);
    const f32x4 e0 = *(const f32x4*)(lds + L_ST + (tid >> 7) * 32), e1 = *(const f32x4*)(lds + L_ST + (tid >> 7) * 32 + 16);
    const float M = fmx(fmx(e0[0], e0[2], inf), fmx(e1[0], e1[2], inf), inf);
    f32x4 fc = {__builtin_amdgcn_exp2f(e0[0] - M), __builtin_amdgcn_exp2f(e0[2] - M), __builtin_amdgcn_exp2f(e1[0] - M), __builtin_amdgcn_exp2f(e1[2] - M)};
    const float L = (fc[0] * e0[1] + fc[1] * e0[3]) + (fc[2] * e1[1] + fc[3] * e1[3]);
    fc *= 1.f / L;
    const float lse = M + __builtin_amdgcn_logf(L);
    float lse_hi = u2f(f2u(lse) & ~TAG_MASK), lse_lo = lse - lse_hi;
    int oa = L_OM + 2 * (tid & 3) * OM_ROW + (tid >> 2) * 4, fa = L_FM + 2 * (tid & 3) * FM_ROW + (tid >> 2) * 8;
    int odst = WS_O + ((bp * 8 + kvh) * 32 + (tid >> 3)) * 2048 + (r16 ^ ((tid >> 3) & 7)) * 128 + (tid & 7) * 16;
    int fdst = (WS_F + (bp * 28 + (tid >> 3)) * 16384 + (pu ^ ((tid >> 3) & 7)) * 128 + (tid & 7) * 16) | oob_if(tid >= 224);
    unsigned tv = t, keep = ~TAG_MASK;
    asm volatile("" : "+v"(lse_hi), "+v"(lse_lo), "+v"(fc), "+v"(oa), "+v"(fa), "+v"(odst), "+v"(fdst), "+v"(tv));
    asm volatile("" : "+s"(keep));
    STAMP(6);
    LDSBAR();
    {
      float a = 0.f, c = 0.f;
#pragma unroll
      for (int ww = 0; ww < 4; ++ww) {
        a = fmaf(fc[ww], *(const float*)(lds + oa + ww * 8 * OM_ROW), a);
        c = fmaf(fc[ww], *(const float*)(lds + oa + ww * 8 * OM_ROW + OM_ROW), c);
      }
      publish(rW, odst, a, c, lse_hi, lse_lo, tv, keep);
    }
    STAMP(7);
    LDSBAR();
    {
      f32x2 p0[4], p1[4];
#pragma unroll
      for (int ww = 0; ww < 4; ++ww) {
        p0[ww] = *(const f32x2*)(lds + fa + ww * 8 * FM_ROW);
        p1[ww] = *(const f32x2*)(lds + fa + ww * 8 * FM_ROW + FM_ROW);
      }
      const f32x2 s0 = (p0[0] + p0[1]) + (p0[2] + p0[3]), s1 = (p1[0] + p1[1]) + (p1[2] + p1[3]);
      publish(rW, fdst, s0.x, s0.y, s1.x, s1.y, tv, keep);
    }
    STAMP(9);
  } else {
    const rsrc_t rV = mk(KARG(const char*, ka[4], ka[5]) + head, WIN * DH * 2);
    v4u kr[32], vr[32];
    int lag = 0;
    unsigned tb0s = 0;
    unsigned tref = t0.x + PACE_A;
#pragma unroll
    for (int i = 0; i < 4; ++i) kr[i] = first[i];
    STAMP(1);
#pragma unroll
    for (int i = 4; i < 32; ++i) {
      if (i >= 12) THROTTLE(); else if (i >= 8) VMWAIT(WIN_BASE - 1);
      kr[i] = ld16<AUX_NT>(rF, fo, (i >> 2) * BSTR + (i & 3) * 128);
      if (i >= 11 && (i & 3) == 3) PACE(i);
      if (i == KB0) { LDSBAR(); tb0s = now(); }
      if (i == QG_AT && w == 0) lds_arrive(lds, L_CNT + 8, lane);
    }
    const int vo = blk0 + lane * 16;
#define VSTEP(i) do { THROTTLE(); vr[i] = ld16<AUX_NT>(rV, vo, ((i) >> 2) * BSTR + ((i) & 3) * 1024); SB(); \
                      if (((i) & 3) == 3) PACE(32 + (i)); } while (0)
    asm volatile("" :: "v"(cpf), "v"(tw), "v"(xq), "v"(ep));
    STAMP(3);
#pragma unroll
    for (int i = 0; i < VB2; ++i) VSTEP(i);
    LDSBAR();
    STAMP(4);
    tref = __builtin_amdgcn_readfirstlane(*(const unsigned*)(lds + L_TAG + 12)) + (PACE_B - (PACE_T * 31) / 10);
    PACE(31 + VB2);
    v4u pa[4];
    {
      v4u qf[2][4];
#pragma unroll
      for (int h = 0; h < 2; ++h)
#pragma unroll
        for (int t = 0; t < 4; ++t) qf[h][t] = *(const v4u*)(lds + L_Q + (h * 256 + 64 * t + 8 * (lane & 7)) * 2);
      VSTEP(VB2);
      float s[8][2];
#pragma unroll
      for (int i = 0; i < 8; i += 2) {
        float a0 = 0.f, a1 = 0.f, c0 = 0.f, c1 = 0.f;
#pragma unroll
        for (int t = 0; t < 4; ++t) {
#pragma unroll
          for (int dw = 0; dw < 4; ++dw) {
            a0 = dot2(kr[4 * i + t][dw], qf[0][t][dw], a0); c0 = dot2(kr[4 * i + 4 + t][dw], qf[0][t][dw], c0);
            a1 = dot2(kr[4 * i + t][dw], qf[1][t][dw], a1); c1 = dot2(kr[4 * i + 4 + t][dw], qf[1][t][dw], c1);
          }
          if (t == 1) VSTEP(VB2 + 1 + i);
        }
        s[i][0] = a0; s[i][1] = a1; s[i + 1][0] = c0; s[i + 1][1] = c1;
        VSTEP(VB2 + 2 + i);
      }
#pragma unroll
      for (int i = 0; i < 4; ++i)
#pragma unroll
        for (int h = 0; h < 2; ++h) {
          const float lo = s[i][h] + dpp<XOR7>(s[i][h]), hi = s[i + 4][h] + dpp<XOR7>(s[i + 4][h]);
          s[i][h] = (lane & 4) ? hi : lo;
        }
#pragma unroll
      for (int i = 0; i < 2; ++i)
#pragma unroll
        for (int h = 0; h < 2; ++h) {
          const float lo = s[i][h] + dpp<XOR2>(s[i][h]), hi = s[i + 2][h] + dpp<XOR2>(s[i + 2][h]);
          s[i][h] = (lane & 2) ? hi : lo;
        }
#pragma unroll
      for (int h = 0; h < 2; ++h) {
        const float lo = s[0][h] + dpp<XOR1>(s[0][h]), hi = s[1][h] + dpp<XOR1>(s[1][h]);
        s[0][h] = (lane & 1) ? hi : lo;
      }
      VSTEP(VB2 + 9);
      constexpr float C2 = 0.0625f * 1.4426950408889634f;
      const float z0 = s[0][0] * C2, z1 = s[0][1] * C2;
      const float m0 = wave_max(z0, inf), m1 = wave_max(z1, inf);
      const float p0 = __builtin_amdgcn_exp2f(z0 - m0), p1 = __builtin_amdgcn_exp2f(z1 - m1);
      const unsigned pp = pkbf(p0, p1);
      unsigned short* pl = (unsigned short*)(lds + L_P + w * 256 + ((lane >> 3) & 1) * 64 + (lane & 7) * 8 + (lane >> 4) * 2);
      pl[0] = (unsigned short)pp;
      pl[64] = (unsigned short)(pp >> 16);
      VSTEP(VB2 + 10);
      const float l01 = x16_sum(row_sum(fold32(p0, p1)));
      if ((lane & 31) == 0) *(f32x2*)(lds + L_ST + ((lane >> 5) * 4 + w) * 8) = f32x2{lane ? m1 : m0, l01};
      lds_arrive(lds, L_CNT + 4, lane);
      const bool valid = (m16 & 3) == 0 && ((m16 >> 2) & 1) == (g4 & 1);
      const int a = valid ? L_P + w * 256 + (m16 >> 3) * 128 + (g4 >> 1) * 64 : L_Z;
#pragma unroll
      for (int G = 0; G < 4; ++G) pa[G] = *(const v4u*)(lds + a + G * 16);
    }
    STAMP(5);
    f32x4 acc[8];
#pragma unroll
    for (int e = 0; e < 8; ++e) acc[e] = zero4;
    v4u tr[12], bf[8];
    const rsrc_t rO = mk(KARG(const void*, ka[8], ka[9]), NQ * DM * 2), r2 = mk(KARG(const void*, ka[12], ka[13]), DFF * DM * 2);
    const int o2 = ((16 * fs + 4 * w) * (DM * 2) + lane * 16) | oob_if((r16 >= 14) | (lane >= 56));
    const int oo = ((kvh * 512 + 32 * r16 + 8 * w) * (DM * 2) + lane * 16) | oob_if(lane >= 56);
#define TLOAD(i) tr[i] = (i) < 4 ? ld16<AUX_NT>(r2, o2, (i) * (DM * 2)) : ld16<AUX_NT>(rO, oo, ((i) - 4) * (DM * 2))
#define PAIRUP(src, pr) do { _Pragma("unroll") for (int dw = 0; dw < 4; ++dw) { \
      unsigned lo_ = perm_lo((src)[2 * (pr)][dw], (src)[2 * (pr) + 1][dw]), hi_ = perm_hi((src)[2 * (pr)][dw], (src)[2 * (pr) + 1][dw]); \
      asm volatile("" : "+v"(lo_), "+v"(hi_)); bf[2 * dw][pr] = lo_; bf[2 * dw + 1][pr] = hi_; } SB(); } while (0)
#define PVMMA(G) do { _Pragma("unroll") for (int e = 0; e < 8; ++e) acc[e] = mfma32(pa[G], bf[e], acc[e]); } while (0)
#define PVGROUP(G) do { PAIRUP(vr + 8 * (G), 0); PAIRUP(vr + 8 * (G), 1); PAIRUP(vr + 8 * (G), 2); PAIRUP(vr + 8 * (G), 3); PVMMA(G); SB(); } while (0)
#pragma unroll
    for (int i = VB2 + 11; i < 32; ++i) {
      VSTEP(i);
      if (i == VB2 + 11) PVGROUP(0);
      if (i == 24) PVGROUP(1);
      if (i == 31) PVGROUP(2);
    }
    lag += T_LAG;
#pragma unroll
    for (int k = 0; k < 8; ++k) {
      VMWAIT(7);
      if (k < 4) asm volatile("s_cmp_gt_i32 %0, 0\n\ts_cbranch_scc1 1f\n\ts_waitcnt vmcnt(%1)\n1:" :: "s"(lag), "i"(T_HOLD) : "scc");
      SB();
      TLOAD(k);
      SB();
      if (k == 1) PAIRUP(vr + 24, 0);
      if (k == 3) PAIRUP(vr + 24, 1);
      if (k == 5) PAIRUP(vr + 24, 2);
    }
    int oma = L_OM + w * 8 * OM_ROW + lane * 4;
    asm volatile("" : "+v"(oma));
    SB();
#pragma unroll
    for (int dw = 0; dw < 4; ++dw) { bf[2 * dw][3] = perm_lo(vr[30][dw], vr[31][dw]); bf[2 * dw + 1][3] = perm_hi(vr[30][dw], vr[31][dw]); }
    PVMMA(3);
    STAMP(6);
#pragma unroll
    for (int e = 0; e < 8; ++e) *(float*)(lds + oma + e * OM_ROW) = acc[e][0];
    LDSBAR();
    const unsigned tb3 = now();
    STAMP(7);
#pragma unroll
    for (int i = 8; i < 12; ++i) TLOAD(i);
    SB();
    const bool diag = (m16 >> 2) == g4;
    const v4u tg = *(const v4u*)(lds + L_TAG);
    const v2u ah = *(const v2u*)(lds + ((diag && (m16 & 3) < 2) ? L_HP + (m16 & 3) * 32 + w * 8 : L_Z));
    const unsigned t = tg.x;
#pragma unroll
    for (int dw = 0; dw < 4; ++dw) {
      acc[2 * dw] = mfma16(ah, v2u{perm_lo(tr[0][dw], tr[1][dw]), perm_lo(tr[2][dw], tr[3][dw])}, zero4);
      acc[2 * dw + 1] = mfma16(ah, v2u{perm_hi(tr[0][dw], tr[1][dw]), perm_hi(tr[2][dw], tr[3][dw])}, zero4);
    }
    SB();
    int ogo = WS_O + (((bp * 8 + kvh) * 16 + r16) * 2 + (w >> 1)) * 2048 + (lane >> 2) * 128 + (w & 1) * 64 + (lane & 3) * 16;
    float gate = u2f(lane < 32 ? tg.y : tg.z);
    unsigned m0 = (diag && (lane & 1) == 0) ? 0xffffffffu : 0u, m1 = (diag && (lane & 1) != 0) ? 0xffffffffu : 0u;
    int fma_ = L_FM + w * 8 * FM_ROW + lane * 8;
    asm volatile("" : "+v"(gate), "+v"(m0), "+v"(m1), "+v"(fma_), "+v"(ogo));
    SB();
    PAIRUP(tr + 4, 0);
    PAIRUP(tr + 4, 1);
    {
      int od = (int)(tref - tb3) + O_AT;
      od = od < O_MIN ? O_MIN : od > O_MAX ? O_MAX : od;
      while ((int)(now() - tb3) < od) {}
    }
    SB();
    v4u go = ld16<AUX_SC1>(rW, ogo, 0);
    SB();
    PAIRUP(tr + 4, 2);
    PAIRUP(tr + 4, 3);
    STAMP(11);
    if (!wave_none(untag(go, t) != 0u)) {
      _Pragma("nounroll") for (unsigned spin = 0; spin < SPIN_CAP; ++spin) {
        REREAD(ogo);
        go = ld16<AUX_SC1>(rW, ogo, 0);
        if (wave_none(untag(go, t) != 0u)) break;
      }
    }
    STAMP(8);
    {
      const float lm = u2f(go.z) + u2f(go.w);
      float M = fmx(lm, dpp<ROR4>(lm), inf);
      M = fmx(M, dpp<ROR8>(M), inf);
      M = x16_max(M, inf);
      float d = __builtin_amdgcn_exp2f(lm - M);
      float a = d * u2f(go.x), c = d * u2f(go.y);
      a += dpp<ROR4>(a); c += dpp<ROR4>(c); d += dpp<ROR4>(d);
      a += dpp<ROR8>(a); c += dpp<ROR8>(c); d += dpp<ROR8>(d);
      a = x16_sum(a); c = x16_sum(c);
      d = gate * __builtin_amdgcn_rcpf(x16_sum(d));
      const unsigned op = pkbf(a * d, c * d);
      const auto sw = __builtin_amdgcn_permlane32_swap(op, op, false, false);
      const v4u b0 = {dppu<0x00>(sw[0]), dppu<0x55>(sw[0]), dppu<0xAA>(sw[0]), dppu<0xFF>(sw[0])};
      const v4u b1 = {dppu<0x00>(sw[1]), dppu<0x55>(sw[1]), dppu<0xAA>(sw[1]), dppu<0xFF>(sw[1])};
      const v4u ao = (b0 & m0) | (b1 & m1);
#pragma unroll
      for (int e = 0; e < 8; ++e) acc[e] = mfma32(ao, bf[e], acc[e]);
#pragma unroll
      for (int e = 0; e < 8; ++e) *(f32x2*)(lds + fma_ + e * FM_ROW) = f32x2{acc[e][0], acc[e][1]};
    }
    if (w == 1) __builtin_amdgcn_raw_buffer_store_b128(v4u{t0.x, tb0s, tb3, 0u}, mk(ws + WS_DIAG, 65536), (id * 128) | oob_if(lane != 0), 0, AUX_SC1);
    LDSBAR();
    if (w == 0 && pu < 112) {
      const unsigned tb4 = now();
      int fgo = WS_F + (bp * 28 + (pu >> 2)) * 16384 + (lane >> 1) * 128 + ((pu & 3) * 2 + (lane & 1)) * 16;
      int oo2 = (((2 * bp + (lane >> 5)) * DM + 4 * pu + 2 * (lane & 1) + (g4 & 1)) * 2) | oob_if((lane & 14) != 0);
      asm volatile("" : "+v"(oo2));
      v4u gf[4];
      while ((int)(now() - tb4) < F_DELAY) {}
      SB();
      _Pragma("nounroll") for (unsigned spin = 0; spin < SPIN_CAP; ++spin) {
        REREAD(fgo);
#pragma unroll
        for (int k = 0; k < 4; ++k) gf[k] = ld16<AUX_SC1>(rW, fgo, k * 4096);
        const unsigned miss = (untag(gf[0], t) | untag(gf[1], t)) | (untag(gf[2], t) | untag(gf[3], t));
        if (wave_none(miss != 0u)) break;
      }
      float v[4];
#pragma unroll
      for (int k = 0; k < 4; ++k) v[k] = (u2f(gf[0][k]) + u2f(gf[1][k])) + (u2f(gf[2][k]) + u2f(gf[3][k]));
      float r = fold16(fold32(v[0], v[1]), fold32(v[2], v[3]));
      r += dpp<ROR8>(r); r += dpp<ROR4>(r); r += dpp<XOR2>(r);
      __builtin_amdgcn_raw_buffer_store_b16((unsigned short)pkbf(r, r), mk(KARG(const void*, kb[0], kb[1]), NB * DM * 2), oo2, 0, 0);
      if (id == 0 && lane == 0) __builtin_amdgcn_raw_buffer_store_b32(t, rW, WS_E, 0, AUX_SC1);
      if (id == 0) {
        const unsigned tend = now();
        while ((int)(now() - tend) < 300) {}
        float e0 = 1e9f, e1 = -1e9f, v1 = -1e9f, b1 = -1e9f, sb = 0.f;
        for (int k = 0; k < 4; ++k) {
          const v4u z = __builtin_amdgcn_raw_buffer_load_b128(mk(ws + WS_DIAG, 65536), (k * 64 + lane) * 128, 0, AUX_SC1);
          const float a = (float)(int)(z.x - t0.x), bb = (float)(int)(z.y - t0.x), c = (float)(int)(z.z - t0.x);
          e0 = fminf(e0, a); e1 = fmaxf(e1, a); v1 = fmaxf(v1, c); b1 = fmaxf(b1, bb); sb += bb - a;
        }
        e1 = wave_max(e1, inf); v1 = wave_max(v1, inf); e0 = -wave_max(-e0, inf); b1 = wave_max(b1, inf);
        sb = x16_sum(row_sum(fold32(sb, sb))) * (1.f / 256.f);
        const float te = (float)(int)(tend - t0.x);
        float x = PROBE == 1 ? e1 - e0 : PROBE == 2 ? sb - 150.f : PROBE == 3 ? v1 - e1 - 900.f : PROBE == 4 ? te - v1 - 150.f : PROBE == 5 ? te - e0 - 1200.f : b1 - e0 - 200.f;
        x = fminf(fmaxf(x, 0.f), 900.f);
        const int xi = __builtin_amdgcn_readfirstlane((int)x) * 10;
        const unsigned tb = now();
        while ((int)(now() - tb) < xi) {}
      }
    }
  }
}

}

extern "C" void kernel_launch(void* const* d_in, const int* in_sizes, int n_in, void* d_out, int out_size, void* d_ws, size_t ws_size,
                              hipStream_t stream) {
  (void)in_sizes; (void)n_in; (void)out_size; (void)ws_size;
  const char* const kc = (const char*)d_in[1]; const char* const vc = (const char*)d_in[2];
  const char* const wq = (const char*)d_in[3]; const char* const wo = (const char*)d_in[4];
  const char* const w1 = (const char*)d_in[6]; const char* const w2 = (const char*)d_in[7];
  const char* const ws = (const char*)d_ws;
  constexpr unsigned HEAD = SK * DH * 2, W0 = (SK - WIN) * DH * 2, WB = WIN * DH * 2, WQB = DM * NQ * 2, W1B = DM * DFF * 2;
  Touch tt;
  for (int n = 0; n < 16; ++n)
    tt.e[n] = TouchEntry{((n >= 4 && n < 12) ? vc : kc) + (size_t)(n & 3) * HKV * HEAD + W0 + (((n >> 2) & 1) ? 0 : WB - 128), HEAD, 0u};
  const char* const rest[16] = {wq, wq + WQB / 2, wq + WQB - 1024, w1, w1 + W1B - 1024, w2, w2 + W1B - 1024, wo, wo + WQB / 8 - 128,
                                (const char*)d_in[0], (const char*)d_in[8], (const char*)d_out, ws + 1024, ws + WS_O - 1024, ws + WS_END,
                                vc + W0 + WB / 2};
  const unsigned step[16] = {128, 128, 128, 128, 128, 128, 128, WQB / 8, WQB / 8, 128, 112, 128, 128, 128, 128, HEAD};
  for (int n = 0; n < 16; ++n) tt.e[16 + n] = TouchEntry{rest[n], step[n], 0u};
  gated_mix_decode_kernel<<<256, 512, 0, stream>>>(
      (const unsigned short*)d_in[0], (const unsigned short*)kc, (const unsigned short*)vc, (const unsigned short*)wq, (const unsigned short*)wo,
      (const unsigned short*)w1, (const unsigned short*)w2, (const unsigned short*)d_in[8], (unsigned short*)d_out, (unsigned char*)d_ws, tt);
}
```

```cpp
#include <hip/hip_runtime.h>
#include <hip/hip_bf16.h>
#include <cstdint>

namespace {

typedef unsigned v4u __attribute__((ext_vector_type(4)));
typedef unsigned v2u __attribute__((ext_vector_type(2)));
typedef unsigned v16u __attribute__((ext_vector_type(16)));
typedef float f32x4 __attribute__((ext_vector_type(4)));
typedef float f32x2 __attribute__((ext_vector_type(2)));
typedef short s16x4 __attribute__((ext_vector_type(4)));
typedef __bf16 bf16x8 __attribute__((ext_vector_type(8)));
typedef __bf16 bf16x2 __attribute__((ext_vector_type(2)));
typedef __amdgpu_buffer_rsrc_t rsrc_t;
typedef __attribute__((address_space(3))) unsigned lds_u32;

constexpr int NB = 4, DM = 448, HKV = 8, SK = 8192, DH = 256, WIN = 2048, DFF = 1792, NQ = 4096;
constexpr int AUX_NT = 2, AUX_SC1 = 16;
constexpr int OOB = 0x40000000;
constexpr unsigned SPIN_CAP = 200000, TAG_MASK = 1023u;

constexpr int WIN_BASE = 5;
constexpr int WIN_LAG = 10;
constexpr int PACE_A = 80;
constexpr int PACE_B = 315;
constexpr int PACE_T = 160;
constexpr int KB0 = 8;
constexpr int QG_AT = 22;
constexpr int VB2 = 4;
constexpr int T_LAG = 30;
constexpr int T_HOLD = 3;
constexpr int O_AT = 1145;
constexpr int O_MIN = 40;
constexpr int O_MAX = 85;
constexpr int F_DELAY = 66;
constexpr int CODE_LINES = 102;
constexpr int BSTR = 131072;
constexpr int JSKIP = 3;

constexpr unsigned WS_ALIGN = 4u << 20;
constexpr int WS_E = 0;
constexpr int WS_T = 4096;
constexpr int WS_Q = WS_T + 8192;
constexpr int WS_H = WS_Q + 8 * 4 * 8192;
constexpr int WS_O = WS_H + 28 * 4 * 2048;
constexpr int WS_F = WS_O + 16 * 16 * 4096;
constexpr int WS_END = WS_F + 2 * 28 * 16384;

constexpr int OM_ROW = 68 * 4, FM_ROW = 544;
constexpr int L_QP = 0;
constexpr int L_Q = 8192;
constexpr int L_P = 9216;
constexpr int L_Z = 10240;
constexpr int L_CNT = 10304;
constexpr int L_TAG = 10320;
constexpr int L_HP = 10368;
constexpr int L_ST = 10432;
constexpr int L_OM = 10496;
constexpr int L_FM = L_OM + 32 * OM_ROW;
constexpr int L_END = L_FM + 32 * FM_ROW;

__device__ __forceinline__ rsrc_t mk(const void* p, int bytes) { return __builtin_amdgcn_make_buffer_rsrc((void*)p, 0, bytes, 0x00020000); }
template <int AUX> __device__ __forceinline__ v4u ld16(rsrc_t r, int vo, int so) { return __builtin_amdgcn_raw_buffer_load_b128(r, vo, so, AUX); }
__device__ __forceinline__ unsigned long long align_up(unsigned long long p, unsigned mask) { return (p + mask) & ~(unsigned long long)mask; }
__device__ __forceinline__ int oob_if(bool c) { return c ? OOB : 0; }
__device__ __forceinline__ float u2f(unsigned u) { return __builtin_bit_cast(float, u); }
__device__ __forceinline__ unsigned f2u(float f) { return __builtin_bit_cast(unsigned, f); }
__device__ __forceinline__ unsigned now() { return (unsigned)__builtin_amdgcn_s_memrealtime(); }
__device__ __forceinline__ void publish(rsrc_t r, int vo, float a, float b, float c, float d, unsigned t, unsigned keep = ~TAG_MASK) {
  __builtin_amdgcn_raw_buffer_store_b128(v4u{(f2u(a) & keep) | t, (f2u(b) & keep) | t, (f2u(c) & keep) | t, (f2u(d) & keep) | t}, r, vo, 0, AUX_SC1);
}
__device__ __forceinline__ float half_up(float v) { return u2f(f2u(v) + (TAG_MASK + 1) / 2); }
__device__ __forceinline__ unsigned untag(v4u& g, unsigned t) { g ^= t; return (g.x | g.y | g.z | g.w) & TAG_MASK; }
__device__ __forceinline__ bool wave_none(bool p) { return __builtin_amdgcn_ballot_w64(p) == 0; }
__device__ __forceinline__ float dot2(unsigned a, unsigned b, float c) {
  return __builtin_amdgcn_fdot2_f32_bf16(__builtin_bit_cast(bf16x2, a), __builtin_bit_cast(bf16x2, b), c, false);
}
__device__ __forceinline__ unsigned pkbf(float lo, float hi) {
  const bf16x2 v = {(__bf16)lo, (__bf16)hi};
  return __builtin_bit_cast(unsigned, v);
}
__device__ __forceinline__ unsigned perm_lo(unsigned a, unsigned b) { return __builtin_amdgcn_perm(b, a, 0x05040100u); }
__device__ __forceinline__ unsigned perm_hi(unsigned a, unsigned b) { return __builtin_amdgcn_perm(b, a, 0x07060302u); }
template <int CTRL> __device__ __forceinline__ unsigned dppu(unsigned x) { return (unsigned)__builtin_amdgcn_mov_dpp((int)x, CTRL, 0xf, 0xf, true); }
template <int CTRL> __device__ __forceinline__ float dpp(float x) { return u2f(dppu<CTRL>(f2u(x))); }
constexpr int XOR1 = 0xB1, XOR2 = 0x4E, XOR7 = 0x141, ROR8 = 0x128, ROR4 = 0x124;
__device__ __forceinline__ float fmx(float a, float c, float inf) { return __builtin_amdgcn_fmed3f(a, c, inf); }
__device__ __forceinline__ float fold16(float a, float b) {
  auto s = __builtin_amdgcn_permlane16_swap(f2u(a), f2u(b), false, false);
  return u2f(s[0]) + u2f(s[1]);
}
__device__ __forceinline__ float fold32(float a, float b) {
  auto s = __builtin_amdgcn_permlane32_swap(f2u(a), f2u(b), false, false);
  return u2f(s[0]) + u2f(s[1]);
}
__device__ __forceinline__ float x16_sum(float x) { return fold16(x, x); }
__device__ __forceinline__ float x16_max(float x, float inf) {
  auto s = __builtin_amdgcn_permlane16_swap(f2u(x), f2u(x), false, false);
  return fmx(u2f(s[0]), u2f(s[1]), inf);
}
__device__ __forceinline__ float row_sum(float x) { x += dpp<XOR1>(x); x += dpp<XOR2>(x); x += dpp<ROR4>(x); x += dpp<ROR8>(x); return x; }
__device__ __forceinline__ float wave_max(float x, float inf) {
  x = fmx(x, dpp<XOR1>(x), inf); x = fmx(x, dpp<XOR2>(x), inf); x = fmx(x, dpp<ROR4>(x), inf); x = fmx(x, dpp<ROR8>(x), inf);
  x = x16_max(x, inf);
  auto s = __builtin_amdgcn_permlane32_swap(f2u(x), f2u(x), false, false);
  return fmx(u2f(s[0]), u2f(s[1]), inf);
}
__device__ __forceinline__ f32x4 mfma32(v4u a, v4u b, f32x4 c) {
  return __builtin_amdgcn_mfma_f32_16x16x32_bf16(__builtin_bit_cast(bf16x8, a), __builtin_bit_cast(bf16x8, b), c, 0, 0, 0);
}
__device__ __forceinline__ f32x4 mfma16(v2u a, v2u b, f32x4 c) {
  return __builtin_amdgcn_mfma_f32_16x16x16bf16_1k(__builtin_bit_cast(s16x4, a), __builtin_bit_cast(s16x4, b), c, 0, 0, 0);
}

#define SB() __builtin_amdgcn_sched_barrier(0)
#define REREAD(off) asm volatile("" : "+v"(off) :: "memory")
#define VMWAIT(n) do { SB(); __builtin_amdgcn_s_waitcnt(((n) & 15) | (7 << 4) | (15 << 8) | ((((n) >> 4) & 3) << 14)); SB(); } while (0)
#define THROTTLE() do { VMWAIT(WIN_LAG - 1); \
    asm volatile("s_cmp_gt_i32 %0, 0\n\ts_cbranch_scc1 1f\n\ts_waitcnt vmcnt(%1)\n1:" :: "s"(lag), "i"(WIN_BASE - 1) : "scc"); SB(); } while (0)
#define PACE(k) do { lag = (int)(now() - tref) - (PACE_T * (k)) / 10; SB(); } while (0)
#define LDSBAR() do { SB(); __builtin_amdgcn_fence(__ATOMIC_RELEASE, "workgroup", "local"); __builtin_amdgcn_s_barrier(); \
                      __builtin_amdgcn_fence(__ATOMIC_ACQUIRE, "workgroup", "local"); SB(); } while (0)

__device__ __forceinline__ void lds_arrive(unsigned char* lds, int off, int lane) {
  __builtin_amdgcn_fence(__ATOMIC_RELEASE, "workgroup", "local");
  if (lane == 0) __hip_atomic_fetch_add((lds_u32*)(lds + off), 1u, __ATOMIC_RELAXED, __HIP_MEMORY_SCOPE_WORKGROUP);
}
__device__ __forceinline__ void lds_wait(unsigned char* lds, int off, unsigned want) {
  _Pragma("nounroll") for (unsigned spin = 0; spin < 16u * SPIN_CAP; ++spin) {
    if ((unsigned)__builtin_amdgcn_readfirstlane(__hip_atomic_load((lds_u32*)(lds + off), __ATOMIC_RELAXED, __HIP_MEMORY_SCOPE_WORKGROUP)) >= want) break;
    __builtin_amdgcn_s_sleep(1);
  }
  __builtin_amdgcn_fence(__ATOMIC_ACQUIRE, "workgroup", "local");
}

struct TouchEntry { const void* p; unsigned stride, align_mask; };
struct Touch { TouchEntry e[32]; };

__global__ void __launch_bounds__(512, 2)
gated_mix_decode_kernel(const unsigned short*, const unsigned short*, const unsigned short*, const unsigned short*, const unsigned short*,
                        const unsigned short*, const unsigned short*, const unsigned short*, unsigned short*, unsigned char*, Touch) {
  __shared__ __attribute__((aligned(128))) unsigned char lds[L_END];
  v16u ka; v4u kb, tp; v2u t0;
  asm volatile("s_load_dwordx4 %2, %4, %5 offset:0x50\n\ts_load_dwordx16 %0, %4, 0x0\n\ts_load_dwordx4 %1, %4, 0x40\n\ts_memrealtime %3"
               : "=&s"(ka), "=&s"(kb), "=&s"(tp), "=&s"(t0) : "s"(__builtin_amdgcn_kernarg_segment_ptr()), "s"((blockIdx.x >> 3) * 16u) : "memory");
  SB();
  unsigned cpf;
  {
    unsigned long long pc;
    asm volatile("s_getpc_b64 %0" : "=s"(pc));
    constexpr int PER = (CODE_LINES + 3) / 4;
    const int line = ((blockIdx.x >> 3) & 3) * PER + threadIdx.x;
    cpf = __builtin_amdgcn_raw_buffer_load_b32(mk((const void*)(pc & ~127ull), CODE_LINES * 128), (line * 128) | oob_if(threadIdx.x >= PER), 0, 0);
    SB();
  }
  const int id = blockIdx.x, kvh = id & 7, b = (id >> 3) & 3, j = id >> 5;
  const int lane = threadIdx.x & 63, wv = __builtin_amdgcn_readfirstlane(threadIdx.x >> 6), w = wv & 3;
  const int m16 = lane & 15, g4 = lane >> 4;
  const bool helper = wv < 4;
  const int blk0 = j * 16384 + w * 4096;
  int fo = ((helper ? (112 * b + 32 * w) * (NQ * 2) + (kvh * 8 + j) * 128 : blk0) + (lane >> 3) * (helper ? 4 * NQ * 2 : 512) + (lane & 7) * 16) |
           oob_if((lane >> 3) >= (wv == 3 ? 4 : 8));
  int xo = (((m16 >> 2) * DM + 112 * b + 32 * w + 8 * g4 + 4 * (m16 & 1)) * 2) | oob_if(((m16 & 2) != 0) | (32 * w + 8 * g4 >= 112));
  int to = oob_if(threadIdx.x != 0);
  unsigned head = ((id & 31) * SK + (SK - WIN)) * DH * 2;
  int fs1 = helper ? NQ * 2 : 128, fs2 = 2 * fs1, fs3 = 3 * fs1;
  asm volatile("" : "+v"(fo), "+v"(xo), "+v"(to));
  asm volatile("" : "+s"(head), "+s"(fs1), "+s"(fs2), "+s"(fs3));
  asm volatile("s_waitcnt lgkmcnt(0)" : "+s"(ka), "+s"(kb), "+s"(tp), "+s"(t0) :: "memory");
#define KARG(T, lo, hi) ((T)(((unsigned long long)(hi) << 32) | (lo)))
  const unsigned tw = __builtin_amdgcn_raw_buffer_load_b32(mk((const void*)align_up(KARG(unsigned long long, tp.x, tp.y), tp.w), OOB), to, tp.z * kvh, AUX_NT);
  SB();
  unsigned char* ws = KARG(unsigned char*, kb[2], kb[3]);
  const rsrc_t rW = mk((const void*)align_up((unsigned long long)ws, WS_ALIGN - 1), WS_END), rX = mk(KARG(const void*, ka[0], ka[1]), NB * DM * 2);
  const rsrc_t rF = mk(helper ? KARG(const char*, ka[6], ka[7]) : KARG(const char*, ka[2], ka[3]) + head, helper ? DM * NQ * 2 : WIN * DH * 2);
  const v4u first[4] = {ld16<AUX_NT>(rF, fo, 0), ld16<AUX_NT>(rF, fo, fs1), ld16<AUX_NT>(rF, fo, fs2), ld16<AUX_NT>(rF, fo, fs3)};
  SB();
  const int bp = b >> 1, r16 = (b & 1) * 8 + j, pu = kvh * 16 + r16, fs = kvh * 14 + r16;
  const f32x4 zero4 = {0.f, 0.f, 0.f, 0.f};
  float inf = __builtin_inff();
  asm volatile("" : "+s"(inf));

  if (__builtin_expect(helper, 1)) {
    const int tid = threadIdx.x;
    const v2u xq = __builtin_amdgcn_raw_buffer_load_b64(rX, xo, 0, 0);
    const unsigned ep = __builtin_amdgcn_raw_buffer_load_b32(rW, WS_E, 0, AUX_SC1);
    SB();
    const int j1 = j == 7 ? JSKIP : j;
    v4u w1r[2];
    v2u xh;
    {
      const int rr = 2 * g4 + (m16 & 1);
      xh.x = __builtin_amdgcn_raw_buffer_load_b32(rX, (((m16 >> 2) * DM + 56 * kvh + 16 * w + 2 * rr) * 2) | oob_if(((m16 & 2) != 0) | (16 * w + 2 * rr >= 56)), 0, 0);
      xh.y = 0u;
      const rsrc_t r1 = mk(KARG(const void*, ka[10], ka[11]), DM * DFF * 2);
      const int row1 = 16 * w + 2 * (lane >> 3);
      const int o1 = ((56 * kvh + row1) * (DFF * 2) + (4 * j1 + b) * 128 + (lane & 7) * 16) | oob_if((j == JSKIP) | (row1 >= 56));
#pragma unroll
      for (int i = 0; i < 2; ++i) w1r[i] = ld16<AUX_NT>(r1, o1, i * (DFF * 2));
    }
    SB();
    if (tid < 24) *(unsigned*)(lds + L_Z + tid * 4) = 0u;
#define TILE_PARTIAL(AF, BLO, BHI, LOFF) do { float pv[8]; \
      _Pragma("unroll") for (int dw = 0; dw < 4; ++dw) { \
        const f32x4 dl = mfma16(AF, BLO, zero4), dh = mfma16(AF, BHI, zero4); \
        const float sl = (lane & 8) ? dl[1] : dl[0], sh = (lane & 8) ? dh[1] : dh[0]; \
        pv[2 * dw] = sl + dpp<ROR8>(sl); pv[2 * dw + 1] = sh + dpp<ROR8>(sh); } \
      *(f32x4*)(lds + (LOFF) + ((w * 4 + g4) * 64 + 8 * (lane & 7) + ((lane & 8) >> 1)) * 4) = \
          (lane & 8) ? f32x4{pv[4], pv[5], pv[6], pv[7]} : f32x4{pv[0], pv[1], pv[2], pv[3]}; } while (0)
#define TILE_PUBLISH(LOFF, DST) do { f32x4 s = zero4; \
      _Pragma("unroll") for (int ww = 0; ww < 4; ++ww) s += *(const f32x4*)(lds + (LOFF) + ((ww * 4 + w) * 64 + 4 * m16) * 4); \
      publish(rW, (DST) | oob_if(lane >= 16), half_up(s[0]), half_up(s[1]), half_up(s[2]), half_up(s[3]), t); SB(); } while (0)
    TILE_PARTIAL(xq, (v2u{perm_lo(first[0][dw], first[1][dw]), perm_lo(first[2][dw], first[3][dw])}),
                 (v2u{perm_hi(first[0][dw], first[1][dw]), perm_hi(first[2][dw], first[3][dw])}), L_QP);
    const unsigned t = ((unsigned)__builtin_amdgcn_readfirstlane(ep) + 1u) & TAG_MASK;
    asm volatile("" :: "v"(tw), "v"(cpf));
    LDSBAR();
    const unsigned tb0 = now();
    TILE_PUBLISH(L_QP, WS_Q + (kvh * 4 + w) * 8192 + j * 1024 + b * 256 + m16 * 16);
    {
      const unsigned tv0 = (tb0 << 10) | t;
      __builtin_amdgcn_raw_buffer_store_b128(v4u{tv0, tv0, tv0, tv0}, rW,
                                             (WS_T + (tid >> 3) * 1024 + kvh * 128 + (tid & 7) * 16) | oob_if((id >= 8) | (tid >= 64)), 0, AUX_SC1);
      SB();
    }
    TILE_PARTIAL(xh, (v2u{perm_lo(w1r[0][dw], w1r[1][dw]), 0u}), (v2u{perm_hi(w1r[0][dw], w1r[1][dw]), 0u}), L_QP + 4096);
    lds_arrive(lds, L_CNT, lane);
    lds_wait(lds, L_CNT, 4);
    TILE_PUBLISH(L_QP + 4096, (WS_H + ((4 * j1 + b) * 4 + w) * 2048 + kvh * 256 + m16 * 16) | oob_if(j == JSKIP));
    lds_wait(lds, L_CNT + 8, 1);
    {
      int qgo = WS_Q + (kvh * 4 + b) * 8192 + (2 * w + (lane >> 5)) * 1024 + (g4 & 1) * 256 + m16 * 16;
      v4u gt = ld16<AUX_SC1>(rW, (WS_T + kvh * 1024 + lane * 128) | oob_if((w != 2) | (lane >= 8)), 0);
      v4u g0, g1;
      _Pragma("nounroll") for (unsigned spin = 0; spin < SPIN_CAP; ++spin) {
        REREAD(qgo);
        g0 = ld16<AUX_SC1>(rW, qgo, 0);
        g1 = ld16<AUX_SC1>(rW, qgo, 512);
        if (wave_none((untag(g0, t) | untag(g1, t)) != 0u)) break;
        __builtin_amdgcn_s_sleep(8);
      }
      float q[4];
#pragma unroll
      for (int k = 0; k < 4; ++k) q[k] = x16_sum(u2f(g0[k]) + u2f(g1[k]));
      if ((g4 & 1) == 0) *(v2u*)(lds + L_Q + ((2 * w + (lane >> 5)) * 64 + 4 * m16) * 2) = v2u{pkbf(q[0], q[1]), pkbf(q[2], q[3])};
      const bool seen = (untag(gt, t) == 0u) & (lane < 8);
      const float sum = row_sum(seen ? (float)((int)(gt.x - (tb0 << 10)) >> 10) : 0.f);
      if (tid == 128) *(unsigned*)(lds + L_TAG + 12) = tb0 + (int)(sum * 0.125f);
    }
    LDSBAR();
    {
      const unsigned pw = __builtin_amdgcn_raw_buffer_load_b32(rW, (WS_O + ((bp * 8 + kvh) * 16 + r16) * 4096 + tid * 128) | oob_if(tid >= 32), 0, AUX_SC1);
      asm volatile("" :: "v"(pw));
    }
    if (w == 3) {
      const int go_ = (lane * 16) | oob_if(lane >= 56);
      const v4u xg0 = ld16<0>(rX, go_, 2 * bp * DM * 2), xg1 = ld16<0>(rX, go_, (2 * bp + 1) * DM * 2);
      const v4u gg = ld16<0>(mk(KARG(const void*, ka[14], ka[15]), DM * 2), go_, 0);
      const bool hon = r16 < 14;
      int hgo = (WS_H + ((fs >> 2) * 4 + 2 * bp + (lane >> 5)) * 2048 + ((lane >> 2) & 7) * 256 + (fs & 3) * 64 + (lane & 3) * 16) | oob_if(!hon);
      v4u gh;
      _Pragma("nounroll") for (unsigned spin = 0; spin < SPIN_CAP; ++spin) {
        REREAD(hgo);
        gh = ld16<AUX_SC1>(rW, hgo, 0);
        if (wave_none(hon & (untag(gh, t) != 0u))) break;
        __builtin_amdgcn_s_sleep(8);
      }
      float d0 = 0.f, d1 = 0.f;
#pragma unroll
      for (int k = 0; k < 4; ++k) { d0 = dot2(xg0[k], gg[k], d0); d1 = dot2(xg1[k], gg[k], d1); }
      const float gate = 1.f / (1.f + __expf(-x16_sum(row_sum(fold32(d0, d1)))));
      if ((lane & 31) == 0) *(float*)(lds + L_TAG + 4 + 4 * (lane >> 5)) = gate;
      if (lane == 1) *(unsigned*)(lds + L_TAG) = t;
      float h[4];
#pragma unroll
      for (int k = 0; k < 4; ++k) {
        float a = hon ? u2f(gh[k]) : 0.f;
        a += dpp<ROR4>(a);
        a += dpp<ROR8>(a);
        h[k] = fmaxf(x16_sum(a), 0.f) * (1.f - gate);
      }
      if ((lane & 28) == 0) *(v2u*)(lds + L_HP + (lane >> 5) * 32 + (lane & 3) * 8) = v2u{pkbf(h[0], h[1]), pkbf(h[2], h[3])};
    }
    lds_wait(lds, L_CNT + 4, 4);
    const f32x4 e0 = *(const f32x4*)(lds + L_ST + (tid >> 7) * 32), e1 = *(const f32x4*)(lds + L_ST + (tid >> 7) * 32 + 16);
    const float M = fmx(fmx(e0[0], e0[2], inf), fmx(e1[0], e1[2], inf), inf);
    f32x4 fc = {__builtin_amdgcn_exp2f(e0[0] - M), __builtin_amdgcn_exp2f(e0[2] - M), __builtin_amdgcn_exp2f(e1[0] - M), __builtin_amdgcn_exp2f(e1[2] - M)};
    const float L = (fc[0] * e0[1] + fc[1] * e0[3]) + (fc[2] * e1[1] + fc[3] * e1[3]);
    fc *= 1.f / L;
    const float lse = M + __builtin_amdgcn_logf(L);
    float lse_hi = u2f(f2u(lse) & ~TAG_MASK), lse_lo = lse - lse_hi;
    int oa[4], fa[4];
#pragma unroll
    for (int ww = 0; ww < 4; ++ww) {
      oa[ww] = L_OM + (ww * 8 + 2 * (tid & 3)) * OM_ROW + (tid >> 2) * 4;
      fa[ww] = L_FM + (ww * 8 + 2 * (tid & 3)) * FM_ROW + (tid >> 2) * 8;
      asm volatile("" : "+v"(oa[ww]), "+v"(fa[ww]));
    }
    int odst = WS_O + ((bp * 8 + kvh) * 32 + (tid >> 3)) * 2048 + (r16 ^ ((tid >> 3) & 7)) * 128 + (tid & 7) * 16;
    int fdst = (WS_F + (bp * 28 + (tid >> 3)) * 16384 + (pu ^ ((tid >> 3) & 7)) * 128 + (tid & 7) * 16) | oob_if(tid >= 224);
    unsigned tv = t, keep = ~TAG_MASK;
    asm volatile("" : "+v"(lse_hi), "+v"(lse_lo), "+v"(fc), "+v"(odst), "+v"(fdst), "+v"(tv));
    asm volatile("" : "+s"(keep));
    LDSBAR();
    {
      float a = 0.f, c = 0.f;
#pragma unroll
      for (int ww = 0; ww < 4; ++ww) {
        a = fmaf(fc[ww], *(const float*)(lds + oa[ww]), a);
        c = fmaf(fc[ww], *(const float*)(lds + oa[ww] + OM_ROW), c);
      }
      publish(rW, odst, a, c, lse_hi, lse_lo, tv, keep);
    }
    LDSBAR();
    {
      f32x2 p0[4], p1[4];
#pragma unroll
      for (int ww = 0; ww < 4; ++ww) {
        p0[ww] = *(const f32x2*)(lds + fa[ww]);
        p1[ww] = *(const f32x2*)(lds + fa[ww] + FM_ROW);
      }
      const f32x2 s0 = (p0[0] + p0[1]) + (p0[2] + p0[3]), s1 = (p1[0] + p1[1]) + (p1[2] + p1[3]);
      publish(rW, fdst, s0.x, s0.y, s1.x, s1.y, tv, keep);
    }
  } else {
    const rsrc_t rV = mk(KARG(const char*, ka[4], ka[5]) + head, WIN * DH * 2);
    v4u kr[32], vr[32];
    int lag = 0;
    unsigned tref = t0.x + PACE_A;
#pragma unroll
    for (int i = 0; i < 4; ++i) kr[i] = first[i];
#pragma unroll
    for (int i = 4; i < 32; ++i) {
      if (i >= 12) THROTTLE(); else if (i >= 8) VMWAIT(WIN_BASE - 1);
      kr[i] = ld16<AUX_NT>(rF, fo, (i >> 2) * BSTR + (i & 3) * 128);
      if (i >= 11 && (i & 3) == 3) PACE(i);
      if (i == KB0) LDSBAR();
      if (i == QG_AT && w == 0) lds_arrive(lds, L_CNT + 8, lane);
    }
    const int vo = blk0 + lane * 16;
#define VSTEP(i) do { THROTTLE(); vr[i] = ld16<AUX_NT>(rV, vo, ((i) >> 2) * BSTR + ((i) & 3) * 1024); SB(); \
                      if (((i) & 3) == 3) PACE(32 + (i)); } while (0)
    asm volatile("" :: "v"(cpf), "v"(tw));
#pragma unroll
    for (int i = 0; i < VB2; ++i) VSTEP(i);
    LDSBAR();
    tref = __builtin_amdgcn_readfirstlane(*(const unsigned*)(lds + L_TAG + 12)) + (PACE_B - (PACE_T * 31) / 10);
    PACE(31 + VB2);
    v4u pa[4];
    {
      v4u qf[2][4];
#pragma unroll
      for (int h = 0; h < 2; ++h)
#pragma unroll
        for (int t = 0; t < 4; ++t) qf[h][t] = *(const v4u*)(lds + L_Q + (h * 256 + 64 * t + 8 * (lane & 7)) * 2);
      VSTEP(VB2);
      float s[8][2];
#pragma unroll
      for (int i = 0; i < 8; i += 2) {
        float a0 = 0.f, a1 = 0.f, c0 = 0.f, c1 = 0.f;
#pragma unroll
        for (int t = 0; t < 4; ++t) {
#pragma unroll
          for (int dw = 0; dw < 4; ++dw) {
            a0 = dot2(kr[4 * i + t][dw], qf[0][t][dw], a0); c0 = dot2(kr[4 * i + 4 + t][dw], qf[0][t][dw], c0);
            a1 = dot2(kr[4 * i + t][dw], qf[1][t][dw], a1); c1 = dot2(kr[4 * i + 4 + t][dw], qf[1][t][dw], c1);
          }
          if (t == 1) VSTEP(VB2 + 1 + i);
        }
        s[i][0] = a0; s[i][1] = a1; s[i + 1][0] = c0; s[i + 1][1] = c1;
        VSTEP(VB2 + 2 + i);
      }
#pragma unroll
      for (int i = 0; i < 4; ++i)
#pragma unroll
        for (int h = 0; h < 2; ++h) {
          const float lo = s[i][h] + dpp<XOR7>(s[i][h]), hi = s[i + 4][h] + dpp<XOR7>(s[i + 4][h]);
          s[i][h] = (lane & 4) ? hi : lo;
        }
#pragma unroll
      for (int i = 0; i < 2; ++i)
#pragma unroll
        for (int h = 0; h < 2; ++h) {
          const float lo = s[i][h] + dpp<XOR2>(s[i][h]), hi = s[i + 2][h] + dpp<XOR2>(s[i + 2][h]);
          s[i][h] = (lane & 2) ? hi : lo;
        }
#pragma unroll
      for (int h = 0; h < 2; ++h) {
        const float lo = s[0][h] + dpp<XOR1>(s[0][h]), hi = s[1][h] + dpp<XOR1>(s[1][h]);
        s[0][h] = (lane & 1) ? hi : lo;
      }
      VSTEP(VB2 + 9);
      constexpr float C2 = 0.0625f * 1.4426950408889634f;
      const float z0 = s[0][0] * C2, z1 = s[0][1] * C2;
      const float m0 = wave_max(z0, inf), m1 = wave_max(z1, inf);
      const float p0 = __builtin_amdgcn_exp2f(z0 - m0), p1 = __builtin_amdgcn_exp2f(z1 - m1);
      const unsigned pp = pkbf(p0, p1);
      unsigned short* pl = (unsigned short*)(lds + L_P + w * 256 + ((lane >> 3) & 1) * 64 + (lane & 7) * 8 + (lane >> 4) * 2);
      pl[0] = (unsigned short)pp;
      pl[64] = (unsigned short)(pp >> 16);
      VSTEP(VB2 + 10);
      const float l01 = x16_sum(row_sum(fold32(p0, p1)));
      if ((lane & 31) == 0) *(f32x2*)(lds + L_ST + ((lane >> 5) * 4 + w) * 8) = f32x2{lane ? m1 : m0, l01};
      lds_arrive(lds, L_CNT + 4, lane);
      const bool valid = (m16 & 3) == 0 && ((m16 >> 2) & 1) == (g4 & 1);
      const int a = valid ? L_P + w * 256 + (m16 >> 3) * 128 + (g4 >> 1) * 64 : L_Z;
#pragma unroll
      for (int G = 0; G < 4; ++G) pa[G] = *(const v4u*)(lds + a + G * 16);
    }
    f32x4 acc[8];
#pragma unroll
    for (int e = 0; e < 8; ++e) acc[e] = zero4;
    v4u tr[12], bf[8];
    const rsrc_t rO = mk(KARG(const void*, ka[8], ka[9]), NQ * DM * 2), r2 = mk(KARG(const void*, ka[12], ka[13]), DFF * DM * 2);
    const int o2 = ((16 * fs + 4 * w) * (DM * 2) + lane * 16) | oob_if((r16 >= 14) | (lane >= 56));
    const int oo = ((kvh * 512 + 32 * r16 + 8 * w) * (DM * 2) + lane * 16) | oob_if(lane >= 56);
#define TLOAD(i) tr[i] = (i) < 4 ? ld16<AUX_NT>(r2, o2, (i) * (DM * 2)) : ld16<AUX_NT>(rO, oo, ((i) - 4) * (DM * 2))
#define PAIRUP(src, pr) do { _Pragma("unroll") for (int dw = 0; dw < 4; ++dw) { \
      unsigned lo_ = perm_lo((src)[2 * (pr)][dw], (src)[2 * (pr) + 1][dw]), hi_ = perm_hi((src)[2 * (pr)][dw], (src)[2 * (pr) + 1][dw]); \
      asm volatile("" : "+v"(lo_), "+v"(hi_)); bf[2 * dw][pr] = lo_; bf[2 * dw + 1][pr] = hi_; } SB(); } while (0)
#define PVMMA(G) do { _Pragma("unroll") for (int e = 0; e < 8; ++e) acc[e] = mfma32(pa[G], bf[e], acc[e]); } while (0)
#define PVGROUP(G) do { PAIRUP(vr + 8 * (G), 0); PAIRUP(vr + 8 * (G), 1); PAIRUP(vr + 8 * (G), 2); PAIRUP(vr + 8 * (G), 3); PVMMA(G); SB(); } while (0)
#pragma unroll
    for (int i = VB2 + 11; i < 32; ++i) {
      VSTEP(i);
      if (i == VB2 + 11) PVGROUP(0);
      if (i == 24) PVGROUP(1);
      if (i == 31) PVGROUP(2);
    }
    lag += T_LAG;
#pragma unroll
    for (int k = 0; k < 8; ++k) {
      VMWAIT(7);
      if (k < 4) asm volatile("s_cmp_gt_i32 %0, 0\n\ts_cbranch_scc1 1f\n\ts_waitcnt vmcnt(%1)\n1:" :: "s"(lag), "i"(T_HOLD) : "scc");
      SB();
      TLOAD(k);
      SB();
      if (k == 1) PAIRUP(vr + 24, 0);
      if (k == 3) PAIRUP(vr + 24, 1);
      if (k == 5) PAIRUP(vr + 24, 2);
    }
    int oma = L_OM + w * 8 * OM_ROW + lane * 4, omb = oma + 4 * OM_ROW;
    asm volatile("" : "+v"(oma), "+v"(omb));
    SB();
#pragma unroll
    for (int dw = 0; dw < 4; ++dw) { bf[2 * dw][3] = perm_lo(vr[30][dw], vr[31][dw]); bf[2 * dw + 1][3] = perm_hi(vr[30][dw], vr[31][dw]); }
    PVMMA(3);
    SB();
#pragma unroll
    for (int e = 0; e < 8; ++e) *(float*)(lds + (e < 4 ? oma : omb) + (e & 3) * OM_ROW) = acc[e][0];
    LDSBAR();
    const unsigned tb3 = now();
#pragma unroll
    for (int i = 8; i < 12; ++i) TLOAD(i);
    SB();
    const bool diag = (m16 >> 2) == g4;
    const v4u tg = *(const v4u*)(lds + L_TAG);
    const v2u ah = *(const v2u*)(lds + ((diag && (m16 & 3) < 2) ? L_HP + (m16 & 3) * 32 + w * 8 : L_Z));
    const unsigned t = tg.x;
#pragma unroll
    for (int dw = 0; dw < 4; ++dw) {
      acc[2 * dw] = mfma16(ah, v2u{perm_lo(tr[0][dw], tr[1][dw]), perm_lo(tr[2][dw], tr[3][dw])}, zero4);
      acc[2 * dw + 1] = mfma16(ah, v2u{perm_hi(tr[0][dw], tr[1][dw]), perm_hi(tr[2][dw], tr[3][dw])}, zero4);
    }
    SB();
    int ogo = WS_O + (((bp * 8 + kvh) * 16 + r16) * 2 + (w >> 1)) * 2048 + (lane >> 2) * 128 + (w & 1) * 64 + (lane & 3) * 16;
    float gate = u2f(lane < 32 ? tg.y : tg.z);
    unsigned m0 = (diag && (lane & 1) == 0) ? 0xffffffffu : 0u, m1 = (diag && (lane & 1) != 0) ? 0xffffffffu : 0u;
    int fma_ = L_FM + w * 8 * FM_ROW + lane * 8, fmb_ = fma_ + 4 * FM_ROW;
    asm volatile("" : "+v"(gate), "+v"(m0), "+v"(m1), "+v"(fma_), "+v"(fmb_), "+v"(ogo));
    SB();
    PAIRUP(tr + 4, 0);
    PAIRUP(tr + 4, 1);
    {
      int od = (int)(tref - tb3) + O_AT;
      od = od < O_MIN ? O_MIN : od > O_MAX ? O_MAX : od;
      while ((int)(now() - tb3) < od) {}
    }
    SB();
    v4u go = ld16<AUX_SC1>(rW, ogo, 0);
    SB();
    PAIRUP(tr + 4, 2);
    PAIRUP(tr + 4, 3);
    if (!wave_none(untag(go, t) != 0u)) {
      _Pragma("nounroll") for (unsigned spin = 0; spin < SPIN_CAP; ++spin) {
        REREAD(ogo);
        go = ld16<AUX_SC1>(rW, ogo, 0);
        if (wave_none(untag(go, t) != 0u)) break;
      }
    }
    {
      const float lm = u2f(go.z) + u2f(go.w);
      float M = fmx(lm, dpp<ROR4>(lm), inf);
      M = fmx(M, dpp<ROR8>(M), inf);
      M = x16_max(M, inf);
      float d = __builtin_amdgcn_exp2f(lm - M);
      float a = d * u2f(go.x), c = d * u2f(go.y);
      a += dpp<ROR4>(a); c += dpp<ROR4>(c); d += dpp<ROR4>(d);
      a += dpp<ROR8>(a); c += dpp<ROR8>(c); d += dpp<ROR8>(d);
      a = x16_sum(a); c = x16_sum(c);
      d = gate * __builtin_amdgcn_rcpf(x16_sum(d));
      const unsigned op = pkbf(a * d, c * d);
      const auto sw = __builtin_amdgcn_permlane32_swap(op, op, false, false);
      const v4u b0 = {dppu<0x00>(sw[0]), dppu<0x55>(sw[0]), dppu<0xAA>(sw[0]), dppu<0xFF>(sw[0])};
      const v4u b1 = {dppu<0x00>(sw[1]), dppu<0x55>(sw[1]), dppu<0xAA>(sw[1]), dppu<0xFF>(sw[1])};
      const v4u ao = (b0 & m0) | (b1 & m1);
#pragma unroll
      for (int e = 0; e < 8; ++e) acc[e] = mfma32(ao, bf[e], acc[e]);
      SB();
#pragma unroll
      for (int e = 0; e < 8; ++e) *(f32x2*)(lds + (e < 4 ? fma_ : fmb_) + (e & 3) * FM_ROW) = f32x2{acc[e][0], acc[e][1]};
    }
    LDSBAR();
    if (w == 0 && pu < 112) {
      const unsigned tb4 = now();
      int fgo = WS_F + (bp * 28 + (pu >> 2)) * 16384 + (lane >> 1) * 128 + ((pu & 3) * 2 + (lane & 1)) * 16;
      int oo2 = (((2 * bp + (lane >> 5)) * DM + 4 * pu + 2 * (lane & 1) + (g4 & 1)) * 2) | oob_if((lane & 14) != 0);
      asm volatile("" : "+v"(oo2));
      v4u gf[4];
      while ((int)(now() - tb4) < F_DELAY) {}
      SB();
      _Pragma("nounroll") for (unsigned spin = 0; spin < SPIN_CAP; ++spin) {
        REREAD(fgo);
#pragma unroll
        for (int k = 0; k < 4; ++k) gf[k] = ld16<AUX_SC1>(rW, fgo, k * 4096);
        const unsigned miss = (untag(gf[0], t) | untag(gf[1], t)) | (untag(gf[2], t) | untag(gf[3], t));
        if (wave_none(miss != 0u)) break;
      }
      float v[4];
#pragma unroll
      for (int k = 0; k < 4; ++k) v[k] = (u2f(gf[0][k]) + u2f(gf[1][k])) + (u2f(gf[2][k]) + u2f(gf[3][k]));
      float r = fold16(fold32(v[0], v[1]), fold32(v[2], v[3]));
      r += dpp<ROR8>(r); r += dpp<ROR4>(r); r += dpp<XOR2>(r);
      __builtin_amdgcn_raw_buffer_store_b16((unsigned short)pkbf(r, r), mk(KARG(const void*, kb[0], kb[1]), NB * DM * 2), oo2, 0, 0);
      if (id == 0 && lane == 0) __builtin_amdgcn_raw_buffer_store_b32(t, rW, WS_E, 0, AUX_SC1);
    }
  }
}

}

extern "C" void kernel_launch(void* const* d_in, const int* in_sizes, int n_in, void* d_out, int out_size, void* d_ws, size_t ws_size,
                              hipStream_t stream) {
  (void)in_sizes; (void)n_in; (void)out_size; (void)ws_size;
  const char* const kc = (const char*)d_in[1]; const char* const vc = (const char*)d_in[2];
  const char* const wq = (const char*)d_in[3]; const char* const wo = (const char*)d_in[4];
  const char* const w1 = (const char*)d_in[6]; const char* const w2 = (const char*)d_in[7];
  const char* const ws = (const char*)d_ws;
  constexpr unsigned HEAD = SK * DH * 2, W0 = (SK - WIN) * DH * 2, WB = WIN * DH * 2, WQB = DM * NQ * 2, W1B = DM * DFF * 2;
  Touch tt;
  for (int n = 0; n < 16; ++n)
    tt.e[n] = TouchEntry{((n >= 4 && n < 12) ? vc : kc) + (size_t)(n & 3) * HKV * HEAD + W0 + (((n >> 2) & 1) ? 0 : WB - 128), HEAD, 0u};
  const char* const rest[16] = {wq, wq + WQB / 2, wq + WQB - 1024, w1, w1 + W1B - 1024, w2, w2 + W1B - 1024, wo, wo + WQB / 8 - 128,
                                (const char*)d_in[0], (const char*)d_in[8], (const char*)d_out, ws, wq + WQB / 4, wq + WQB / 4 * 3,
                                vc + W0 + WB / 2};
  const unsigned step[16] = {128, 128, 128, 128, 128, 128, 128, WQB / 8, WQB / 8, 128, 112, 128, 256, 128, 128, HEAD};
  for (int n = 0; n < 16; ++n) tt.e[16 + n] = TouchEntry{rest[n], step[n], rest[n] == ws ? WS_ALIGN - 1 : 0u};
  gated_mix_decode_kernel<<<256, 512, 0, stream>>>(
      (const unsigned short*)d_in[0], (const unsigned short*)kc, (const unsigned short*)vc, (const unsigned short*)wq, (const unsigned short*)wo,
      (const unsigned short*)w1, (const unsigned short*)w2, (const unsigned short*)d_in[8], (unsigned short*)d_out, (unsigned char*)d_ws, tt);
}
```
